```python
import torch

def _softmax(x, axis=-1):
    return torch.softmax(x, dim=axis)
def _gelu(x):
    return torch.nn.functional.gelu(x, approximate="tanh")
def _silu(x):
    return torch.nn.functional.silu(x)
def _sigmoid(x):
    return torch.sigmoid(x)
def _layernorm(x, axis=-1, eps=1e-5):
    m = x.mean(dim=axis, keepdim=True)
    v = x.var(dim=axis, keepdim=True, unbiased=False)
    return (x - m) / torch.sqrt(v + eps)
def _scatter_add(tgt, idx, src, axis=0):
    return tgt.clone().index_add_(axis, idx, src)
def _segment_sum(src, seg, n):
    out = torch.zeros((n,) + src.shape[1:], dtype=src.dtype)
    return out.index_add_(0, seg, src)
def _pad(x, pw):
    flat = tuple(v for pair in reversed(pw) for v in pair)
    return torch.nn.functional.pad(x, flat)

kernel_name = 'sequential_preLN__gqa_block_cached__geglu_ffn__decode'

def setup_inputs():

    _w = (lambda a: (lambda t: t.to(torch.bfloat16) if t.is_floating_point() else t)(torch.as_tensor(a)))
    def _A_setup():
        B, Sq, D = 1, 1, 448
        Hq, Hkv, Sk, Dh = 16, 8, 8192, 256
        assert Hq % Hkv == 0, (Hq, Hkv)
        return {
            "x": _w((lambda *s: torch.randn(*s, dtype=torch.bfloat16) * 0.05)(B, Sq, D)),
            "k_cache": _w((lambda *s: torch.randn(*s, dtype=torch.bfloat16) * 3.2)(B, Hkv, Sk, Dh)),
            "v_cache": _w((lambda *s: torch.randn(*s, dtype=torch.bfloat16) * 0.4)(B, Hkv, Sk, Dh)),
            "wq": _w((lambda *s: torch.randn(*s, dtype=torch.bfloat16) * 3.2)(D, Hq * Dh) * (D ** -0.5)),
            "wo": _w((lambda *s: torch.randn(*s, dtype=torch.bfloat16) * 0.4)(Hq * Dh, D) * ((Hq * Dh) ** -0.5)),
        }
    _A_in = _A_setup()

    def _B_setup():
        B, S, D, F = 1, 1, 448, 1792
        return {
            "x": _w((lambda *s: torch.randn(*s, dtype=torch.bfloat16) * 0.05)(B, S, D)),
            "w_gate": _w((lambda *s: torch.randn(*s, dtype=torch.bfloat16) * 0.05)(D, F)),
            "w_up": _w((lambda *s: torch.randn(*s, dtype=torch.bfloat16) * 0.05)(D, F)),
            "w_down": _w((lambda *s: torch.randn(*s, dtype=torch.bfloat16) * 0.05)(F, D)),
        }
    _B_in = _B_setup()

    return {
        "A_x": _A_in["x"],
        "A_k_cache": _A_in["k_cache"],
        "A_v_cache": _A_in["v_cache"],
        "A_wq": _A_in["wq"],
        "A_wo": _A_in["wo"],
        "B_x": _B_in["x"],
        "B_w_gate": _B_in["w_gate"],
        "B_w_up": _B_in["w_up"],
        "B_w_down": _B_in["w_down"],
    }

def reference(A_x, A_k_cache, A_v_cache, A_wq, A_wo, B_x, B_w_gate, B_w_up, B_w_down):
    def _A_ref(x, k_cache, v_cache, wq, wo):
        B, Sq, D = x.shape
        Hq, Dh = 16, 256
        Hkv = k_cache.shape[1]
        rep = Hq // Hkv
        q = (lambda x, *d: x.permute(*d))((x @ wq).reshape(B, Sq, Hq, Dh), 0, 2, 1, 3)
        k = (lambda x, n, axis: torch.repeat_interleave(x, n, dim=axis))(k_cache, rep, axis=1)
        v = (lambda x, n, axis: torch.repeat_interleave(x, n, dim=axis))(v_cache, rep, axis=1)
        scores = torch.einsum("bhqd,bhkd->bhqk", q, k) * (Dh ** -0.5)
        p = _softmax(scores, axis=-1)
        o = torch.einsum("bhqk,bhkd->bhqd", p, v)
        return (lambda x, *d: x.permute(*d))(o, 0, 2, 1, 3).reshape(B, Sq, Hq * Dh) @ wo

    def _B_ref(x, w_gate, w_up, w_down):
        return (_gelu(x @ w_gate) * (x @ w_up)) @ w_down

    x = A_x
    h = x + _A_ref(_layernorm(x, axis=-1), A_k_cache, A_v_cache, A_wq, A_wo)
    h2 = h + _B_ref(_layernorm(h, axis=-1), B_w_gate, B_w_up, B_w_down)
    return h2
```

```cpp
#include <hip/hip_runtime.h>
#include <hip/hip_bf16.h>
#include <stdint.h>
#define MIMIC 0
typedef unsigned short u16;
typedef unsigned v4u __attribute__((ext_vector_type(4)));
__device__ __forceinline__ float bf2f(u16 b){ return __uint_as_float(((unsigned)b)<<16); }
__device__ __forceinline__ float rbf(float f){
  return f;
}
__device__ __forceinline__ u16 f2bf(float f){ unsigned u=__float_as_uint(f); u += 0x7FFFu + ((u>>16)&1u); return (u16)(u>>16); }
constexpr int D=448, HQ=16, HKV=8, SK=8192, DH=256, F=1792, NS=32, KPS=SK/NS;

__device__ float block_sum(float v, float* red){
  for(int o=32;o>0;o>>=1) v += __shfl_xor(v,o);
  int w=threadIdx.x>>6; __syncthreads();
  if((threadIdx.x&63)==0) red[w]=v; __syncthreads();
  float s=0; for(int i=0;i<(int)(blockDim.x>>6);++i) s+=red[i]; return s;
}
__device__ void layern(const float xv, float* xn, float* red){
  int t=threadIdx.x;
  float s=block_sum(t<D?xv:0.f,red); float mean_f=s/D; float m=rbf(mean_f);
  float d=t<D?(xv-mean_f):0.f; float v=rbf(block_sum(d*d,red)/D);
  float den=rbf(sqrtf(rbf(v+1e-5f)));
  if(t<D) xn[t]=rbf(rbf(xv-m)/den);
  __syncthreads();
}
__global__ void __launch_bounds__(512) k_q(const u16* x,const u16* wq,float* q){
  __shared__ float xn[D]; __shared__ float red[8]; __shared__ float part[8][64];
  int t=threadIdx.x; float xv=t<D?bf2f(x[t]):0.f; layern(xv,xn,red);
  int col=blockIdx.x*64+(t&63); int w=t>>6; float a=0;
  for(int k=w*56;k<w*56+56;++k) a+=xn[k]*bf2f(wq[(size_t)k*4096+col]);
  part[w][t&63]=a; __syncthreads();
  if(t<64){ float s=0; for(int i=0;i<8;++i) s+=part[i][t]; q[blockIdx.x*64+t]=rbf(s);}
}
__global__ void __launch_bounds__(256) k_attn(const u16* K,const u16* V,const float* q,float* pm,float* pl,float* po){
  __shared__ float qs[2][DH]; __shared__ float sc[2][KPS]; __shared__ float red[8]; __shared__ float oacc[8][2][DH];
  int kvh=blockIdx.x&7, s=blockIdx.x>>3; int t=threadIdx.x;
  for(int i=t;i<512;i+=256) qs[i>>8][i&255]=q[kvh*512+i];
  __syncthreads();
  int lane=t&63,w=t>>6; int half=lane>>5, c=lane&31;
  float q0[8],q1[8]; for(int j=0;j<8;++j){q0[j]=qs[0][c*8+j]; q1[j]=qs[1][c*8+j];}
  const u16* Kb=K+((size_t)kvh*SK+s*KPS)*DH; const u16* Vb=V+((size_t)kvh*SK+s*KPS)*DH;
  for(int k=w*2+half;k<KPS;k+=8){
    v4u kv=*(const v4u*)(Kb+(size_t)k*DH+c*8); float a0=0,a1=0;
    for(int j=0;j<4;++j){ float lo=__uint_as_float(kv[j]<<16), hi=__uint_as_float(kv[j]&0xffff0000u);
      a0+=lo*q0[2*j]+hi*q0[2*j+1]; a1+=lo*q1[2*j]+hi*q1[2*j+1]; }
    for(int o=16;o>0;o>>=1){ a0+=__shfl_xor(a0,o); a1+=__shfl_xor(a1,o);}
    if(c==0){ sc[0][k]=rbf(a0)*0.0625f; sc[1][k]=rbf(a1)*0.0625f; }
  }
  __syncthreads();
  float m[2],l[2];
  for(int h=0;h<2;++h){ float v=sc[h][t]; for(int o=32;o>0;o>>=1) v=fmaxf(v,__shfl_xor(v,o)); __syncthreads(); if(lane==0) red[w]=v; __syncthreads();
    m[h]=fmaxf(fmaxf(red[0],red[1]),fmaxf(red[2],red[3])); float p=__expf(sc[h][t]-m[h]); l[h]=block_sum(p,red); sc[h][t]=p; }
  __syncthreads();
  float o0[8]={0},o1[8]={0};
  for(int k=w*2+half;k<KPS;k+=8){
    v4u vv=*(const v4u*)(Vb+(size_t)k*DH+c*8); float p0=sc[0][k],p1=sc[1][k];
    for(int j=0;j<4;++j){ float lo=__uint_as_float(vv[j]<<16), hi=__uint_as_float(vv[j]&0xffff0000u);
      o0[2*j]+=p0*lo;o0[2*j+1]+=p0*hi;o1[2*j]+=p1*lo;o1[2*j+1]+=p1*hi; }
  }
  for(int j=0;j<8;++j){ oacc[w*2+half][0][c*8+j]=o0[j]; oacc[w*2+half][1][c*8+j]=o1[j]; }
  __syncthreads();
  for(int i=t;i<512;i+=256){ float a=0; for(int r=0;r<8;++r) a+=oacc[r][i>>8][i&255]; po[((size_t)(kvh*2+(i>>8))*NS+s)*DH+(i&255)]=a; }
  if(t<2){ pm[(kvh*2+t)*NS+s]=m[t]; pl[(kvh*2+t)*NS+s]=l[t]; }
}
__global__ void __launch_bounds__(256) k_comb(const float* pm,const float* pl,const float* po,float* o){
  int h=blockIdx.x,t=threadIdx.x; float M=-INFINITY; for(int s=0;s<NS;++s) M=fmaxf(M,pm[h*NS+s]);
  float L=0,a=0; for(int s=0;s<NS;++s){ float w=__expf(pm[h*NS+s]-M); L+=w*pl[h*NS+s]; a+=w*po[((size_t)h*NS+s)*DH+t]; }
  o[h*DH+t]=rbf(a/L);
}
__global__ void __launch_bounds__(512) k_wo(const float* o,const u16* wo,const u16* x,float* h){
  __shared__ float part[64][8];
  int t=threadIdx.x; int c=t&7, r=t>>3; int col=blockIdx.x*8+c; float a=0;
  for(int k=r;k<4096;k+=64) a+=o[k]*bf2f(wo[(size_t)k*D+col]);
  part[r][c]=a; __syncthreads();
  if(t<8){ float s=0; for(int i=0;i<64;++i) s+=part[i][t]; h[blockIdx.x*8+t]=rbf(bf2f(x[blockIdx.x*8+t])+rbf(s)); }
}
__global__ void __launch_bounds__(512) k_gu(const float* h,const u16* wg,const u16* wu,float* act){
  __shared__ float xn[D]; __shared__ float red[8]; __shared__ float pg[64][8],pu[64][8];
  int t=threadIdx.x; float xv=t<D?h[t]:0.f; layern(xv,xn,red);
  int c=t&7,r=t>>3; int col=blockIdx.x*8+c; float g=0,u=0;
  for(int k=r;k<D;k+=64){ g+=xn[k]*bf2f(wg[(size_t)k*F+col]); u+=xn[k]*bf2f(wu[(size_t)k*F+col]); }
  pg[r][c]=g; pu[r][c]=u; __syncthreads();
  if(t<8){ float G=0,U=0; for(int i=0;i<64;++i){G+=pg[i][t];U+=pu[i][t];} G=rbf(G);U=rbf(U);
    float ge=0.5f*G*(1.f+tanhf(0.7978845608028654f*(G+0.044715f*G*G*G))); act[blockIdx.x*8+t]=rbf(rbf(ge)*U); }
}
__global__ void __launch_bounds__(512) k_dn(const float* act,const u16* wd,const float* h,u16* out){
  __shared__ float part[64][8];
  int t=threadIdx.x; int c=t&7, r=t>>3; int col=blockIdx.x*8+c; float a=0;
  for(int k=r;k<F;k+=64) a+=act[k]*bf2f(wd[(size_t)k*D+col]);
  part[r][c]=a; __syncthreads();
  if(t<8){ float s=0; for(int i=0;i<64;++i) s+=part[i][t]; out[blockIdx.x*8+t]=f2bf(h[blockIdx.x*8+t]+rbf(s)); }
}
extern "C" void kernel_launch(void* const* d_in, const int* in_sizes, int n_in, void* d_out, int out_size, void* d_ws, size_t ws_size, hipStream_t stream){
  const u16* x=(const u16*)d_in[0]; const u16* K=(const u16*)d_in[1]; const u16* V=(const u16*)d_in[2];
  const u16* wq=(const u16*)d_in[3]; const u16* wo=(const u16*)d_in[4]; const u16* wg=(const u16*)d_in[6]; const u16* wu=(const u16*)d_in[7]; const u16* wd=(const u16*)d_in[8];
  float* ws=(float*)d_ws; float* q=ws; float* pm=ws+4096; float* pl=pm+512; float* po=pl+512; float* o=po+16*32*256; float* h=o+4096; float* act=h+512;
  k_q<<<64,512,0,stream>>>(x,wq,q);
  k_attn<<<256,256,0,stream>>>(K,V,q,pm,pl,po);
  k_comb<<<16,256,0,stream>>>(pm,pl,po,o);
  k_wo<<<56,512,0,stream>>>(o,wo,x,h);
  k_gu<<<224,512,0,stream>>>(h,wg,wu,act);
  k_dn<<<56,512,0,stream>>>(act,wd,h,(u16*)d_out);
}
```

```cpp
#include <hip/hip_runtime.h>
#include <hip/hip_bf16.h>
#include <stdint.h>

typedef unsigned short u16;
typedef unsigned int u32;
typedef unsigned long long u64;
typedef u32 v4u __attribute__((ext_vector_type(4)));
typedef u32 v2u __attribute__((ext_vector_type(2)));
typedef float v4f __attribute__((ext_vector_type(4)));
typedef float v2f __attribute__((ext_vector_type(2)));
typedef __bf16 bf16x2 __attribute__((ext_vector_type(2)));
typedef __bf16 bf16x8 __attribute__((ext_vector_type(8)));
typedef short s16x4 __attribute__((ext_vector_type(4)));

#define LAS __attribute__((address_space(3)))
#define DIAG 0

namespace {

constexpr int D = 448, SK = 8192, DH = 256, FF = 1792, NQ = 4096, NSP = 28;
constexpr int WS_EPOCH = 0;
constexpr int WS_Q = 4096;
constexpr int WS_ML = 32768;
constexpr int WS_PO = 131072;
constexpr int WS_PH = WS_PO + 524288;
constexpr int WS_PD = WS_PH + 65536;
constexpr int WS_END = WS_PD + 28 * 3584;
constexpr int WS_STAMP = 1 << 20;
constexpr int AUX_SC1 = 16, AUX_NT = 2;
constexpr int OOB = 0x7ffffff0;
constexpr u32 SPIN_CAP = 1u << 20;
constexpr float LOG2E = 1.4426950408889634f;

constexpr int L_QS = 0;
constexpr int L_WMAX = 256;
constexpr int L_WSUM = 272;
constexpr int L_S1 = 288;
constexpr int L_FLAG = 544;
constexpr int L_CML = 548;
constexpr int L_ORED = 576;
constexpr int L_CMB = L_ORED + 4096;
constexpr int L_END = L_CMB + 5 * 256;
constexpr int L_QR = 0;
constexpr int L_ST = 1024;
constexpr int L_XN = 1040;
constexpr int L_HS = 1296;
constexpr int L_GU = 1312;
constexpr int L_DN = 2336;

__device__ __forceinline__ float bflo(u32 u) { return __uint_as_float(u << 16); }
__device__ __forceinline__ float bfhi(u32 u) { return __uint_as_float(u & 0xffff0000u); }
__device__ __forceinline__ u32 cvtpk(float lo, float hi) {
  v2f v = {lo, hi};
  return __builtin_bit_cast(u32, __builtin_convertvector(v, bf16x2));
}
__device__ __forceinline__ float rbf(float f) { return bflo(cvtpk(f, 0.f)); }
__device__ __forceinline__ float dot2(u32 w, u32 a, float acc) {
  return __builtin_amdgcn_fdot2_f32_bf16(__builtin_bit_cast(bf16x2, w), __builtin_bit_cast(bf16x2, a), acc, false);
}
__device__ __forceinline__ float bsel(u32 m, float a, float b) {
  return __uint_as_float((__float_as_uint(a) & m) | (__float_as_uint(b) & ~m));
}
template <int CTRL> __device__ __forceinline__ float dpp(float x) {
  return __builtin_bit_cast(float, __builtin_amdgcn_mov_dpp(__builtin_bit_cast(int, x), CTRL, 0xf, 0xf, true));
}
constexpr int X1 = 0xB1, X2 = 0x4E, X7 = 0x141, X8 = 0x128;
__device__ __forceinline__ float sw32_sum(float a, float b) {
  auto r = __builtin_amdgcn_permlane32_swap(__float_as_uint(a), __float_as_uint(b), false, false);
  return __uint_as_float(r[0]) + __uint_as_float(r[1]);
}
__device__ __forceinline__ float sw16_sum(float a, float b) {
  auto r = __builtin_amdgcn_permlane16_swap(__float_as_uint(a), __float_as_uint(b), false, false);
  return __uint_as_float(r[0]) + __uint_as_float(r[1]);
}
__device__ __forceinline__ float sw32_max(float a) {
  auto r = __builtin_amdgcn_permlane32_swap(__float_as_uint(a), __float_as_uint(a), false, false);
  return fmaxf(__uint_as_float(r[0]), __uint_as_float(r[1]));
}
__device__ __forceinline__ float sw16_max(float a) {
  auto r = __builtin_amdgcn_permlane16_swap(__float_as_uint(a), __float_as_uint(a), false, false);
  return fmaxf(__uint_as_float(r[0]), __uint_as_float(r[1]));
}
template <int CTRL> __device__ __forceinline__ float tr_step(float lo, float hi, bool sel) {
  const float t1 = lo + dpp<CTRL>(lo), t2 = hi + dpp<CTRL>(hi);
  return sel ? t2 : t1;
}
__device__ __forceinline__ float wave_sum(float x) {
  x += dpp<X1>(x); x += dpp<X2>(x); x += dpp<X7>(x); x += dpp<X8>(x);
  x = sw16_sum(x, x); return sw32_sum(x, x);
}
__device__ __forceinline__ float half_sum(float x) {
  x += dpp<X1>(x); x += dpp<X2>(x); x += dpp<X7>(x); x += dpp<X8>(x);
  return sw16_sum(x, x);
}
__device__ __forceinline__ v4u ldnt(const u16* p) { return __builtin_nontemporal_load(reinterpret_cast<const v4u*>(p)); }
__device__ __forceinline__ v4u ldb(__amdgpu_buffer_rsrc_t rs, int off) { return __builtin_amdgcn_raw_buffer_load_b128(rs, off, 0, AUX_NT); }
__device__ __forceinline__ v4u ldg(__amdgpu_buffer_rsrc_t rs, int off) { return __builtin_amdgcn_raw_buffer_load_b128(rs, off, 0, AUX_SC1); }
__device__ __forceinline__ void stg(__amdgpu_buffer_rsrc_t rs, int off, u32 a, u32 b, u32 t0, u32 t1) {
  v4u v = {a, b, t0, t1};
  __builtin_amdgcn_raw_buffer_store_b128(v, rs, off, 0, AUX_SC1);
}
__device__ __forceinline__ void bar() {
  asm volatile("s_waitcnt lgkmcnt(0)" ::: "memory");
  __builtin_amdgcn_s_barrier();
  asm volatile("" ::: "memory");
}
__device__ __forceinline__ v4f mfma(v4u a, v4u b, v4f c) {
  return __builtin_amdgcn_mfma_f32_16x16x32_bf16(__builtin_bit_cast(bf16x8, a), __builtin_bit_cast(bf16x8, b), c, 0, 0, 0);
}
__device__ __forceinline__ u32 plo(u32 r0, u32 r1) { return __builtin_amdgcn_perm(r1, r0, 0x05040100u); }
__device__ __forceinline__ u32 phi(u32 r0, u32 r1) { return __builtin_amdgcn_perm(r1, r0, 0x07060302u); }
__device__ __forceinline__ void pairup(v4u& a, v4u& b) {
  v4u x, y;
  x[0] = plo(a[0], b[0]); x[1] = phi(a[0], b[0]); x[2] = plo(a[1], b[1]); x[3] = phi(a[1], b[1]);
  y[0] = plo(a[2], b[2]); y[1] = phi(a[2], b[2]); y[2] = plo(a[3], b[3]); y[3] = phi(a[3], b[3]);
  a = x; b = y;
}
__device__ __forceinline__ void fmap(float (&acc)[8], v4u a, v4u b, u32 n) {
#pragma unroll
  for (int j = 0; j < 4; ++j) { acc[j] = dot2(a[j], n, acc[j]); acc[4 + j] = dot2(b[j], n, acc[4 + j]); }
}
__device__ __forceinline__ void fma8(float (&acc)[8], v4u wv, u32 alo, u32 ahi) {
#pragma unroll
  for (int j = 0; j < 4; ++j) { acc[2 * j] = dot2(wv[j], alo, acc[2 * j]); acc[2 * j + 1] = dot2(wv[j], ahi, acc[2 * j + 1]); }
}
__device__ __forceinline__ u64 gtag(const v4u& g) { return ((u64)g[3] << 32) | (u64)g[2]; }
__device__ __forceinline__ void mark(v4u& g, bool unused, u64 tag) {
  g[2] = unused ? (u32)tag : g[2]; g[3] = unused ? (u32)(tag >> 32) : g[3];
}
template <int N, int MANY> __device__ __forceinline__ u32 poll(v4u srd, const int (&off)[N], v4u (&gr)[N], u64 tag) {
  u32 spins = 0;
  u64 m[N], any = 0;
#pragma unroll
  for (int i = 0; i < N; ++i) { m[i] = __ballot(gtag(gr[i]) != tag); any |= m[i]; }
  if (__builtin_expect(any != 0, 0)) {
    do {
      if (MANY < 64 && __builtin_popcountll(any) > MANY) __builtin_amdgcn_s_sleep(8);
#pragma unroll
      for (int i = 0; i < N; ++i)
        asm volatile("s_mov_b64 exec, %2\n\tbuffer_load_dwordx4 %0, %1, %3, 0 offen sc1\n\ts_mov_b64 exec, -1"
                     : "+v"(gr[i]) : "v"(off[i]), "s"(m[i]), "s"(srd) : "memory");
#pragma unroll
      for (int i = 0; i < N; ++i) asm volatile("s_waitcnt vmcnt(%1)" : "+v"(gr[i]) : "i"(N - 1 - i) : "memory");
      any = 0;
#pragma unroll
      for (int i = 0; i < N; ++i) { m[i] = __ballot(gtag(gr[i]) != tag); any |= m[i]; }
    } while (any != 0 && ++spins < SPIN_CAP);
    ++spins;
  }
  return spins;
}
__device__ __forceinline__ float rcp(float x) { return __builtin_amdgcn_rcpf(x); }
#define STAMP(i) do { } while (0)
#define STAMP_FLUSH() do { } while (0)
#define SB() __builtin_amdgcn_sched_barrier(0)
#define TAG() const u64 tag = (((u64)__builtin_amdgcn_readfirstlane(ep[1]) << 32) | (u64)__builtin_amdgcn_readfirstlane(ep[0])) + 1; \
  const u32 t0 = (u32)tag, t1 = (u32)(tag >> 32)

__global__ void __launch_bounds__(512, 2)
fused_decode_block(const u16* __restrict__ x, const u16* __restrict__ Kc, const u16* __restrict__ Vc, const u16* __restrict__ wq,
                   const u16* __restrict__ wo, const u16* __restrict__ wg, const u16* __restrict__ wu, const u16* __restrict__ wd,
                   u16* __restrict__ out, unsigned char* ws) {
  __shared__ __attribute__((aligned(16))) float lds[L_END];
  const int tid = threadIdx.x, lane = tid & 63;
  const int w = __builtin_amdgcn_readfirstlane(tid >> 6);
  const int b = blockIdx.x, g = b & 7, s = b >> 3;
  const __amdgpu_buffer_rsrc_t rs = __builtin_amdgcn_make_buffer_rsrc((void*)ws, 0, WS_END, 0x00020000);
  const v4u srd = {(u32)(uintptr_t)ws, (u32)((uintptr_t)ws >> 32) & 0xffffu, (u32)WS_END, 0x00020000u};
  STAMP(0);
  const v2u ep = __builtin_amdgcn_raw_buffer_load_b64(rs, WS_EPOCH, 0, AUX_SC1);

  if (s < NSP) {
    volatile LAS u32* lflag = (volatile LAS u32*)(&lds[L_FLAG]);
    if (tid == 0) { lflag[0] = 0u; lflag[1] = 0u; }
    __builtin_amdgcn_s_setprio(1);
    bar();
    const int g4 = lane >> 4, c = lane & 15;
    const int tile0 = (256 * s) / 7, nx = (256 * (s + 1)) / 7 - tile0 - 32;
    const bool five = w < nx;
    const int key0 = (tile0 + 4 * w + (five ? w : nx)) * 8;
    const __amdgpu_buffer_rsrc_t rk = __builtin_amdgcn_make_buffer_rsrc((void*)Kc, 0, 8 * SK * DH * 2, 0x00020000);
    const __amdgpu_buffer_rsrc_t rv = __builtin_amdgcn_make_buffer_rsrc((void*)Vc, 0, 8 * SK * DH * 2, 0x00020000);
    v4u kr[20], vr[20];
    {
      const int kb = ((g * SK + key0 + (c >> 1)) * DH + (c & 1) * 32 + g4 * 8) * 2;
      const int kb5 = five ? kb + 4 * 4096 : OOB;
#pragma unroll
      for (int t = 0; t < 4; ++t)
#pragma unroll
        for (int ks = 0; ks < 4; ++ks) kr[t * 4 + ks] = ldb(rk, kb + t * 4096 + ks * 128);
#pragma unroll
      for (int ks = 0; ks < 4; ++ks) kr[16 + ks] = ldb(rk, five ? kb5 + ks * 128 : OOB);
      SB();
      const int vb = ((g * SK + key0 + 2 * g4) * DH + 8 * c) * 2;
#pragma unroll
      for (int hf = 0; hf < 2; ++hf) {
#pragma unroll
        for (int j = 0; j < 8; ++j) vr[hf * 10 + j] = ldb(rv, vb + (8 * (j >> 1) + (j & 1)) * 512 + 256 * hf);
#pragma unroll
        for (int j = 0; j < 2; ++j) vr[hf * 10 + 8 + j] = ldb(rv, five ? vb + (32 + j) * 512 + 256 * hf : OOB);
        SB();
      }
    }
    const int oh = 2 * g + s / 14, cb = s % 14;
    const int sub = lane >> 3, gq = lane & 7;
    const __amdgpu_buffer_rsrc_t ro = __builtin_amdgcn_make_buffer_rsrc((void*)wo, 0, NQ * D * 2, 0x00020000);
    const int oo = ((oh * 256 + 32 * w + 4 * gq + 2 * (lane >> 5)) * D + 32 * cb + 8 * (sub & 3)) * 2;
    const v4u woa0 = ldb(ro, w >= 4 ? oo : OOB), woa1 = ldb(ro, w >= 4 ? oo + D * 2 : OOB);
    SB();
    STAMP(1);
    TAG();
    if (w == 0) {
      __builtin_amdgcn_s_setprio(3);
      v4u qg[2];
      int qoff[2];
#pragma unroll
      for (int i = 0; i < 2; ++i) { qoff[i] = WS_Q + (g * 128 + i * 64 + lane) * 16; qg[i] = ldg(rs, qoff[i]); }
      poll<2, 64>(srd, qoff, qg, tag);
#pragma unroll
      for (int i = 0; i < 2; ++i)
        *reinterpret_cast<v2u*>(&lds[L_QS + (i * 64 + lane) * 2]) = v2u{qg[i][0], qg[i][1]};
      asm volatile("s_waitcnt lgkmcnt(0)" ::: "memory");
      lflag[0] = 1u;
      __builtin_amdgcn_s_setprio(1);
    }
    for (u32 sp = 0; lflag[0] == 0u && sp < (SPIN_CAP << 4); ++sp) __builtin_amdgcn_s_sleep(1);
    asm volatile("" ::: "memory");
    STAMP(2);
    const int hd = (c >> 2) & 1;
    const u32 meta = ((c >> 1) & 1) ? 0xffffffffu : 0u, mvar = (c & 1) ? 0xffffffffu : 0u, mhi = (g4 >> 1) ? 0xffffffffu : 0u;
    float sc[10];
    {
      v4u qf[4];
#pragma unroll
      for (int ks = 0; ks < 4; ++ks) qf[ks] = *reinterpret_cast<const v4u*>(&lds[L_QS + hd * 128 + ks * 32 + ((c >> 1) & 1) * 16 + g4 * 4]);
#pragma unroll
      for (int t = 0; t < 5; ++t) {
        v4f d = {0.f, 0.f, 0.f, 0.f};
#pragma unroll
        for (int ks = 0; ks < 4; ++ks) d = mfma(kr[t * 4 + ks], qf[ks], d);
        const float sa = bsel(meta, d[1], d[0]), sb = bsel(meta, d[3], d[2]);
        sc[2 * t] = rbf(sa + dpp<X2>(sa)) * 0.0625f;
        sc[2 * t + 1] = rbf(sb + dpp<X2>(sb)) * 0.0625f;
      }
      if (!five) { sc[8] = -INFINITY; sc[9] = -INFINITY; }
    }
    float m = sc[0];
#pragma unroll
    for (int j = 1; j < 10; ++j) m = fmaxf(m, sc[j]);
    m = sw32_max(sw16_max(m));
    v4u pa;
    v4u pa5;
    {
      float p[10], l = 0.f;
#pragma unroll
      for (int j = 0; j < 10; ++j) { p[j] = __builtin_amdgcn_exp2f((sc[j] - m) * LOG2E); l += p[j]; }
      l = sw16_sum(l, l); l = sw32_sum(l, l);
      if ((lane & 0x3b) == 0) { lds[L_WMAX + w * 2 + hd] = m; lds[L_WSUM + w * 2 + hd] = l; }
#pragma unroll
      for (int j = 0; j < 10; ++j) { const float hi = rbf(p[j]); p[j] = bsel(mvar, p[j] - hi, hi); }
#pragma unroll
      for (int t = 0; t < 4; ++t) pa[t] = cvtpk(p[2 * t], p[2 * t + 1]);
      pa5[0] = cvtpk(p[8], p[9]); pa5[1] = 0u; pa5[2] = 0u; pa5[3] = 0u;
    }
    STAMP(3);
#pragma unroll
    for (int hf = 0; hf < 2; ++hf) {
      float xo[8];
#pragma unroll
      for (int d = 0; d < 8; ++d) {
        v4u bf;
        const u32 sel = (d & 1) ? 0x07060302u : 0x05040100u;
#pragma unroll
        for (int t = 0; t < 4; ++t) bf[t] = __builtin_amdgcn_perm(vr[hf * 10 + 2 * t + 1][d >> 1], vr[hf * 10 + 2 * t][d >> 1], sel);
        v4u b5;
        b5[0] = __builtin_amdgcn_perm(vr[hf * 10 + 9][d >> 1], vr[hf * 10 + 8][d >> 1], sel); b5[1] = 0u; b5[2] = 0u; b5[3] = 0u;
        v4f dd = mfma(pa, bf, v4f{0.f, 0.f, 0.f, 0.f});
        dd = mfma(pa5, b5, dd);
        xo[d] = dd[0] + dd[1];
      }
      v4f y;
#pragma unroll
      for (int e = 0; e < 4; ++e) y[e] = bsel(mhi, xo[4 + e], xo[e]);
      *reinterpret_cast<v4f*>(&lds[L_ORED + (w * 2 + (g4 & 1)) * 256 + 128 * hf + 8 * c + 4 * (g4 >> 1)]) = y;
    }
    STAMP(4);
    __builtin_amdgcn_s_setprio(0);
    asm volatile("s_waitcnt lgkmcnt(0)" ::: "memory");
    if (lane == 0) __hip_atomic_fetch_add((LAS u32*)(&lds[L_FLAG + 1]), 1u, __ATOMIC_RELAXED, __HIP_MEMORY_SCOPE_WORKGROUP);
    v4u wor[2];
    wor[0] = ldb(ro, w < 4 ? oo : OOB); wor[1] = ldb(ro, w < 4 ? oo + D * 2 : OOB);
    SB();
    if (w < 4) {
      v4u po[7], ml = {0u, 0u, 0u, 0u};
      int poff[7];
      const int mysp = w + 4 * (lane & 7);
      const bool mlwant = lane < 7 && mysp != s;
      const int mloff = WS_ML + (oh * 32 + mysp) * 128;
      mark(ml, !mlwant, tag);
      if (!mlwant) ml[0] = 0xff800000u;
#pragma unroll
      for (int i = 0; i < 7; ++i) {
        po[i] = v4u{0u, 0u, 0u, 0u};
        mark(po[i], w + 4 * i == s, tag);
        poff[i] = WS_PO + ((oh * 32 + w + 4 * i) * 64 + lane) * 16;
      }
      u64 mm = __ballot(gtag(ml) != tag);
      for (u32 sp = 0; sp < SPIN_CAP; ++sp) {
        const bool few = __builtin_popcountll(mm) <= 1;
        u64 pm[7], pany = 0;
#pragma unroll
        for (int i = 0; i < 7; ++i) { pm[i] = __ballot(gtag(po[i]) != tag); pany |= pm[i]; }
        if ((mm | pany) == 0) break;
        if (!few && sp) __builtin_amdgcn_s_sleep(8);
        asm volatile("s_mov_b64 exec, %2\n\tbuffer_load_dwordx4 %0, %1, %3, 0 offen sc1\n\ts_mov_b64 exec, -1"
                     : "+v"(ml) : "v"(mloff), "s"(mm), "s"(srd) : "memory");
#pragma unroll
        for (int i = 0; i < 7; ++i) {
          const u64 mk = (few || !((mm >> i) & 1)) ? pm[i] : 0ull;
          asm volatile("s_mov_b64 exec, %2\n\tbuffer_load_dwordx4 %0, %1, %3, 0 offen sc1\n\ts_mov_b64 exec, -1"
                       : "+v"(po[i]) : "v"(poff[i]), "s"(mk), "s"(srd) : "memory");
        }
        asm volatile("s_waitcnt vmcnt(0)" : "+v"(ml), "+v"(po[0]), "+v"(po[1]), "+v"(po[2]), "+v"(po[3]), "+v"(po[4]), "+v"(po[5]), "+v"(po[6]) :: "memory");
        mm = __ballot(gtag(ml) != tag);
      }
      STAMP(7);
      const float mi = __uint_as_float(ml[0]);
      float mx = fmaxf(mi, dpp<X1>(mi)); mx = fmaxf(mx, dpp<X2>(mx)); mx = fmaxf(mx, dpp<X7>(mx));
      const float wt = __builtin_amdgcn_exp2f((mi - mx) * LOG2E);
      float lw = wt * __uint_as_float(ml[1]);
      lw += dpp<X1>(lw); lw += dpp<X2>(lw); lw += dpp<X7>(lw);
      v4f o = {0.f, 0.f, 0.f, 0.f};
#pragma unroll
      for (int i = 0; i < 7; ++i) {
        const float f = __builtin_bit_cast(float, __builtin_amdgcn_readlane(__builtin_bit_cast(int, wt), i));
        o[0] = fmaf(f, bflo(po[i][0]), o[0]); o[1] = fmaf(f, bfhi(po[i][0]), o[1]);
        o[2] = fmaf(f, bflo(po[i][1]), o[2]); o[3] = fmaf(f, bfhi(po[i][1]), o[3]);
      }
      *reinterpret_cast<v4f*>(&lds[L_CMB + w * 256 + lane * 4]) = o;
      if (lane == 0) *reinterpret_cast<v2f*>(&lds[L_CML + w * 2]) = v2f{mx, lw};
    } else if (w < 6) {
      const int hh = w - 4;
      for (u32 sp = 0; lflag[1] < 8u && sp < (SPIN_CAP << 4); ++sp) { }
      asm volatile("" ::: "memory");
      STAMP(5);
      float mw[8], Mh;
#pragma unroll
      for (int ww = 0; ww < 8; ++ww) mw[ww] = lds[L_WMAX + ww * 2 + hh];
      Mh = fmaxf(fmaxf(fmaxf(mw[0], mw[1]), fmaxf(mw[2], mw[3])), fmaxf(fmaxf(mw[4], mw[5]), fmaxf(mw[6], mw[7])));
      v4f o = {0.f, 0.f, 0.f, 0.f}, o2 = {0.f, 0.f, 0.f, 0.f};
      float L = 0.f;
#pragma unroll
      for (int ww = 0; ww < 8; ++ww) {
        const float f = __builtin_amdgcn_exp2f((mw[ww] - Mh) * LOG2E);
        const v4f t = *reinterpret_cast<const v4f*>(&lds[L_ORED + (ww * 2 + hh) * 256 + lane * 4]);
        if (ww & 1) o2 += f * t; else o += f * t;
        L = fmaf(f, lds[L_WSUM + ww * 2 + hh], L);
      }
      o += o2;
      const u32 c0 = cvtpk(o[0], o[1]), c1 = cvtpk(o[2], o[3]);
      stg(rs, WS_PO + (((2 * g + hh) * 32 + s) * 64 + lane) * 16, c0, c1, t0, t1);
      if (lane == 0) stg(rs, WS_ML + ((2 * g + hh) * 32 + s) * 128, __float_as_uint(Mh), __float_as_uint(L), t0, t1);
      if (hh == (oh & 1)) {
        *reinterpret_cast<v4f*>(&lds[L_CMB + 4 * 256 + lane * 4]) = v4f{bflo(c0), bfhi(c0), bflo(c1), bfhi(c1)};
        if (lane == 0) *reinterpret_cast<v2f*>(&lds[L_CML + 8]) = v2f{Mh, L};
      }
      STAMP(6);
    }
    wor[0] |= woa0; wor[1] |= woa1;
    pairup(wor[0], wor[1]);
    asm volatile("" : "+v"(wor[0]), "+v"(wor[1]));
    bar();
    {
      v2f cm[5], co[5];
#pragma unroll
      for (int i = 0; i < 5; ++i) {
        cm[i] = *reinterpret_cast<const v2f*>(&lds[L_CML + i * 2]);
        co[i] = *reinterpret_cast<const v2f*>(&lds[L_CMB + i * 256 + 32 * w + 4 * gq + 2 * (lane >> 5)]);
      }
      const float Mx = fmaxf(fmaxf(fmaxf(cm[0][0], cm[1][0]), fmaxf(cm[2][0], cm[3][0])), cm[4][0]);
      float L = 0.f;
      v2f u = {0.f, 0.f};
#pragma unroll
      for (int i = 0; i < 5; ++i) {
        const float f = __builtin_amdgcn_exp2f((cm[i][0] - Mx) * LOG2E);
        L = fmaf(f, cm[i][1], L);
        u += f * co[i];
      }
      const float rl = rcp(L);
      const u32 n = cvtpk(u[0] * rl, u[1] * rl);
      float acc[8];
#pragma unroll
      for (int j = 0; j < 8; ++j) acc[j] = 0.f;
      fmap(acc, wor[0], wor[1], n);
      float r4[4], r2[2];
#pragma unroll
      for (int j = 0; j < 4; ++j) r4[j] = sw32_sum(acc[j], acc[j + 4]);
#pragma unroll
      for (int j = 0; j < 2; ++j) r2[j] = tr_step<X7>(r4[j], r4[j + 2], (lane & 4) != 0);
      float r1 = tr_step<X2>(r2[0], r2[1], (lane & 2) != 0);
      r1 += dpp<X1>(r1);
      if ((lane & 1) == 0) lds[L_S1 + w * 32 + (sub & 3) * 8 + (lane >> 5) * 4 + ((lane >> 2) & 1) * 2 + ((lane >> 1) & 1)] = r1;
    }
    bar();
    if (w == 0 && lane < 16) {
      v2f a[8];
#pragma unroll
      for (int ww = 0; ww < 8; ++ww) a[ww] = *reinterpret_cast<const v2f*>(&lds[L_S1 + ww * 32 + lane * 2]);
      const v2f t = ((a[0] + a[1]) + (a[2] + a[3])) + ((a[4] + a[5]) + (a[6] + a[7]));
      stg(rs, WS_PH + (oh * 224 + cb * 16 + lane) * 16, __float_as_uint(t[0]), __float_as_uint(t[1]), t0, t1);
    }
    STAMP(8);
    STAMP_FLUSH();
    return;
  }

  const int f = g * 4 + (s - NSP);
  const bool ffn = f < 28;
  const int r4 = lane >> 4;
  const v4u xv = *reinterpret_cast<const v4u*>(x + (lane < 56 ? lane : 0) * 8);
  u32 xq[7];
  v4u wa[14], wf[14], wdn[8];
  {
    const int row0 = 56 * w + 14 * r4;
#pragma unroll
    for (int i = 0; i < 7; ++i) xq[i] = *reinterpret_cast<const u32*>(x + row0 + 2 * i);
    const u16* p = wq + (size_t)row0 * NQ + 128 * f + (lane & 15) * 8;
#pragma unroll
    for (int i = 0; i < 14; ++i) wa[i] = ldnt(p + (size_t)i * NQ);
    SB();
    bar();
    SB();
    const int fc = ffn ? f : 0;
    const u16* pf = (((lane >> 3) & 1) ? wu : wg) + (size_t)row0 * FF + 64 * fc + (lane & 7) * 8;
    float xf[8];
#pragma unroll
    for (int j = 0; j < 4; ++j) { xf[2 * j] = bflo(xv[j]); xf[2 * j + 1] = bfhi(xv[j]); }
    float sm = 0.f;
#pragma unroll
    for (int j = 0; j < 8; ++j) sm += xf[j];
    sm = wave_sum(lane < 56 ? sm : 0.f);
    const float mean = sm * (1.0f / D);
    float sq = 0.f;
#pragma unroll
    for (int j = 0; j < 8; ++j) { const float d = xf[j] - mean; sq += d * d; }
    sq = wave_sum(lane < 56 ? sq : 0.f);
    const float mb = rbf(mean), den = rbf(sqrtf(rbf(rbf(sq * (1.0f / D)) + 1e-5f)));
    float acc[8];
#pragma unroll
    for (int j = 0; j < 8; ++j) acc[j] = 0.f;
#pragma unroll
    for (int i = 0; i < 7; ++i) {
      const u32 n = cvtpk(rbf(bflo(xq[i]) - mb) / den, rbf(bfhi(xq[i]) - mb) / den);
      pairup(wa[2 * i], wa[2 * i + 1]);
      fmap(acc, wa[2 * i], wa[2 * i + 1], n);
    }
    float a4[4];
#pragma unroll
    for (int j = 0; j < 4; ++j) a4[j] = sw32_sum(acc[j], acc[j + 4]);
    v2f a2;
#pragma unroll
    for (int j = 0; j < 2; ++j) a2[j] = sw16_sum(a4[j], a4[j + 2]);
    *reinterpret_cast<v2f*>(&lds[L_QR + w * 128 + (lane & 15) * 8 + (lane >> 5) * 4 + ((lane >> 4) & 1) * 2]) = a2;
    bar();
    TAG();
    if (w == 0 && lane < 32) {
      v4f qv = *reinterpret_cast<const v4f*>(&lds[L_QR + lane * 4]);
#pragma unroll
      for (int ww = 1; ww < 8; ++ww) qv += *reinterpret_cast<const v4f*>(&lds[L_QR + ww * 128 + lane * 4]);
      stg(rs, WS_Q + (f * 32 + lane) * 16, cvtpk(qv[0], qv[1]), cvtpk(qv[2], qv[3]), t0, t1);
    }
    STAMP(1);
    if (!ffn) { STAMP_FLUSH(); return; }
    SB();
#pragma unroll
    for (int i = 0; i < 14; ++i) wf[i] = ldnt(pf + (size_t)i * FF);
    SB();
  }
  const int l56 = lane < 56 ? lane : 0;
  {
    const u16* pd = wd + (size_t)(64 * f + 8 * w) * D + 8 * l56;
#pragma unroll
    for (int i = 0; i < 8; ++i) wdn[i] = ldnt(pd + i * D);
    SB();
  }
#pragma unroll
  for (int i = 0; i < 7; ++i) { pairup(wf[2 * i], wf[2 * i + 1]); asm volatile("" : "+v"(wf[2 * i]), "+v"(wf[2 * i + 1])); }
#pragma unroll
  for (int i = 0; i < 4; ++i) { pairup(wdn[2 * i], wdn[2 * i + 1]); asm volatile("" : "+v"(wdn[2 * i]), "+v"(wdn[2 * i + 1])); }
  {
    float acc[8];
#pragma unroll
    for (int j = 0; j < 8; ++j) acc[j] = 0.f;
#pragma unroll
    for (int i = 0; i < 7; ++i) fmap(acc, wf[2 * i], wf[2 * i + 1], 0x3f803f80u);
    float a4[4];
#pragma unroll
    for (int j = 0; j < 4; ++j) a4[j] = sw32_sum(acc[j], acc[j + 4]);
    v2f a2;
#pragma unroll
    for (int j = 0; j < 2; ++j) a2[j] = sw16_sum(a4[j], a4[j + 2]);
    *reinterpret_cast<v2f*>(&lds[L_QR + w * 128 + (lane & 15) * 8 + (lane >> 5) * 4 + ((lane >> 4) & 1) * 2]) = a2;
  }
  bar();
  const int u = 8 * w + (lane & 7);
  float SG = 0.f, SU = 0.f;
#pragma unroll
  for (int ww = 0; ww < 8; ++ww) { SG += lds[L_QR + ww * 128 + u]; SU += lds[L_QR + ww * 128 + 64 + u]; }
  asm volatile("" : "+v"(SG), "+v"(SU));
  TAG();
  STAMP(2);
  {
    const int hsub = lane >> 5;
    const bool act = (lane & 31) < 28;
    const int pp = act ? (lane & 31) : 0;
    int off[8];
    v4u gr[8];
    const u32 xp = *reinterpret_cast<const u32*>(x + 56 * w + 2 * pp);
#pragma unroll
    for (int i = 0; i < 8; ++i) { off[i] = act ? WS_PH + ((2 * i + hsub) * 224 + 28 * w + pp) * 16 : OOB; gr[i] = ldg(rs, off[i]); }
#pragma unroll
    for (int i = 0; i < 8; ++i) mark(gr[i], !act, tag);
    poll<8, 32>(srd, off, gr, tag);
    STAMP(9);
    float h0 = ((__uint_as_float(gr[0][0]) + __uint_as_float(gr[1][0])) + (__uint_as_float(gr[2][0]) + __uint_as_float(gr[3][0]))) +
               ((__uint_as_float(gr[4][0]) + __uint_as_float(gr[5][0])) + (__uint_as_float(gr[6][0]) + __uint_as_float(gr[7][0])));
    float h1 = ((__uint_as_float(gr[0][1]) + __uint_as_float(gr[1][1])) + (__uint_as_float(gr[2][1]) + __uint_as_float(gr[3][1]))) +
               ((__uint_as_float(gr[4][1]) + __uint_as_float(gr[5][1])) + (__uint_as_float(gr[6][1]) + __uint_as_float(gr[7][1])));
    h0 = sw32_sum(h0, h0); h1 = sw32_sum(h1, h1);
    const u32 a01 = cvtpk(h0, h1);
    const u32 h01 = cvtpk(bflo(xp) + bflo(a01), bfhi(xp) + bfhi(a01));
    h0 = bflo(h01); h1 = bfhi(h01);
    LAS u32* xn = (LAS u32*)(&lds[L_XN + w * 32]);
    if (lane < 28) xn[(lane / 7) * 8 + lane % 7] = h01;
    asm volatile("s_waitcnt lgkmcnt(0)" ::: "memory");
    const v4u na = *(const LAS v4u*)(xn + r4 * 8), nb = *(const LAS v4u*)(xn + r4 * 8 + 4);
    const float lm = half_sum(act ? h0 + h1 : 0.f) * (1.0f / 56.0f);
    const float d0 = h0 - lm, d1 = h1 - lm;
    const float m2 = half_sum(act ? d0 * d0 + d1 * d1 : 0.f);
    if (lane == 0) *reinterpret_cast<v2f*>(&lds[L_ST + w * 2]) = v2f{lm, m2};
    {
      const int cp = 28 * w + pp - 8 * f;
      if (act && hsub == 0 && cp >= 0 && cp < 8) *reinterpret_cast<v2f*>(&lds[L_HS + cp * 2]) = v2f{h0, h1};
    }
    float acc[8];
#pragma unroll
    for (int j = 0; j < 8; ++j) acc[j] = 0.f;
#pragma unroll
    for (int i = 0; i < 7; ++i) fmap(acc, wf[2 * i], wf[2 * i + 1], i < 4 ? na[i] : nb[i - 4]);
    float a4[4];
#pragma unroll
    for (int j = 0; j < 4; ++j) a4[j] = sw32_sum(acc[j], acc[j + 4]);
    v2f a2;
#pragma unroll
    for (int j = 0; j < 2; ++j) a2[j] = sw16_sum(a4[j], a4[j + 2]);
    *reinterpret_cast<v2f*>(&lds[L_GU + w * 128 + (lane & 15) * 8 + (lane >> 5) * 4 + ((lane >> 4) & 1) * 2]) = a2;
  }
  bar();
  {
    float gp[8], up[8], lms[8], m2s[8];
#pragma unroll
    for (int ww = 0; ww < 8; ++ww) {
      gp[ww] = lds[L_GU + ww * 128 + u]; up[ww] = lds[L_GU + ww * 128 + 64 + u];
      const v2f v = *reinterpret_cast<const v2f*>(&lds[L_ST + ww * 2]); lms[ww] = v[0]; m2s[ww] = v[1];
    }
    const float mean = (((lms[0] + lms[1]) + (lms[2] + lms[3])) + ((lms[4] + lms[5]) + (lms[6] + lms[7]))) * 0.125f;
#pragma unroll
    for (int ww = 0; ww < 8; ++ww) { const float d = lms[ww] - mean; m2s[ww] = fmaf(56.0f * d, d, m2s[ww]); }
    const float M2 = ((m2s[0] + m2s[1]) + (m2s[2] + m2s[3])) + ((m2s[4] + m2s[5]) + (m2s[6] + m2s[7]));
    const float mb = rbf(mean), rden = rcp(rbf(__builtin_amdgcn_sqrtf(rbf(rbf(M2 * (1.0f / D)) + 1e-5f))));
    float G = ((gp[0] + gp[1]) + (gp[2] + gp[3])) + ((gp[4] + gp[5]) + (gp[6] + gp[7]));
    float U = ((up[0] + up[1]) + (up[2] + up[3])) + ((up[4] + up[5]) + (up[6] + up[7]));
    G = rbf(fmaf(-mb, SG, G) * rden); U = rbf(fmaf(-mb, SU, U) * rden);
    const float z = 0.7978845608028654f * (G + 0.044715f * G * G * G);
    const float th = 1.0f - 2.0f * rcp(__builtin_amdgcn_exp2f(z * (2.0f * LOG2E)) + 1.0f);
    const float av = rbf(0.5f * G * (1.0f + th)) * U;
    const u32 pk = cvtpk(av, dpp<X1>(av));
    float acc[8];
#pragma unroll
    for (int j = 0; j < 8; ++j) acc[j] = 0.f;
#pragma unroll
    for (int i = 0; i < 4; ++i) fmap(acc, wdn[2 * i], wdn[2 * i + 1], __builtin_amdgcn_readlane(pk, 2 * i));
    if (lane < 56) {
      *reinterpret_cast<v4f*>(&lds[L_DN + w * 448 + lane * 8]) = v4f{acc[0], acc[1], acc[2], acc[3]};
      *reinterpret_cast<v4f*>(&lds[L_DN + w * 448 + lane * 8 + 4]) = v4f{acc[4], acc[5], acc[6], acc[7]};
    }
  }
  bar();
  if (tid < 224) {
    v2f a[8];
#pragma unroll
    for (int ww = 0; ww < 8; ++ww) a[ww] = *reinterpret_cast<const v2f*>(&lds[L_DN + ww * 448 + tid * 2]);
    const v2f t = ((a[0] + a[1]) + (a[2] + a[3])) + ((a[4] + a[5]) + (a[6] + a[7]));
    stg(rs, WS_PD + (f * 224 + tid) * 16, __float_as_uint(t[0]), __float_as_uint(t[1]), t0, t1);
  }
  STAMP(10);
  if (w == 7) {
    int off[4];
    v4u gr[4];
#pragma unroll
    for (int i = 0; i < 4; ++i) {
      off[i] = (i < 3 || lane < 32) ? WS_PD + (((lane >> 3) + 8 * i) * 224 + 8 * f + (lane & 7)) * 16 : OOB;
      gr[i] = ldg(rs, off[i]);
    }
    mark(gr[3], lane >= 32, tag);
    poll<4, 64>(srd, off, gr, tag);
    STAMP(11);
    float a0 = 0.f, a1 = 0.f;
#pragma unroll
    for (int i = 0; i < 4; ++i) { a0 += __uint_as_float(gr[i][0]); a1 += __uint_as_float(gr[i][1]); }
    float r = sw32_sum(a0, a1);
    r = sw16_sum(r, r); r += dpp<X8>(r);
    const float hv = lds[L_HS + (lane & 7) * 2 + (lane >> 5)];
    const u32 ob = cvtpk(hv + rbf(r), 0.f);
    if ((lane & 0x18) == 0) out[16 * f + (lane & 7) * 2 + (lane >> 5)] = (u16)ob;
    if (f == 0 && lane == 0) __builtin_amdgcn_raw_buffer_store_b64(v2u{t0, t1}, rs, WS_EPOCH, 0, AUX_SC1);
  }
  STAMP(12);
  STAMP_FLUSH();
}

}

extern "C" void kernel_launch(void* const* d_in, const int* in_sizes, int n_in, void* d_out, int out_size, void* d_ws,
                              size_t ws_size, hipStream_t stream) {
  fused_decode_block<<<256, 512, 0, stream>>>((const u16*)d_in[0], (const u16*)d_in[1], (const u16*)d_in[2], (const u16*)d_in[3],
                                              (const u16*)d_in[4], (const u16*)d_in[6], (const u16*)d_in[7], (const u16*)d_in[8],
                                              (u16*)d_out, (unsigned char*)d_ws);
}
```

```cpp
#include <hip/hip_runtime.h>
#include <hip/hip_bf16.h>
#include <stdint.h>

typedef unsigned short u16;
typedef unsigned int u32;
typedef unsigned long long u64;
typedef u32 v4u __attribute__((ext_vector_type(4)));
typedef u32 v2u __attribute__((ext_vector_type(2)));
typedef float v4f __attribute__((ext_vector_type(4)));
typedef float v2f __attribute__((ext_vector_type(2)));
typedef __bf16 bf16x2 __attribute__((ext_vector_type(2)));
typedef __bf16 bf16x8 __attribute__((ext_vector_type(8)));
typedef short s16x4 __attribute__((ext_vector_type(4)));

#define LAS __attribute__((address_space(3)))
#define CODE_PF 1
#define CODE_KB 18
#define DIAG 0

namespace {

constexpr int D = 448, SK = 8192, DH = 256, FF = 1792, NQ = 4096, NSP = 28;
constexpr int WS_EPOCH = 0;
constexpr int WS_Q = 4096;
constexpr int WS_ML = 32768;
constexpr int WS_PO = 131072;
constexpr int WS_PH = WS_PO + 524288;
constexpr int WS_PD = WS_PH + 65536;
constexpr int WS_END = WS_PD + 28 * 3584;
constexpr int WS_STAMP = 1 << 20;
constexpr int AUX_SC1 = 16, AUX_NT = 2;
constexpr int OOB = 0x7ffffff0;
constexpr u32 SPIN_CAP = 1u << 20;
constexpr float LOG2E = 1.4426950408889634f;

constexpr int L_QS = 0;
constexpr int L_WMAX = 256;
constexpr int L_WSUM = 272;
constexpr int L_S1 = 288;
constexpr int L_FLAG = 544;
constexpr int L_CML = 548;
constexpr int L_ORED = 576;
constexpr int L_CMB = L_ORED + 4096;
constexpr int L_END = L_CMB + 5 * 256;
constexpr int L_QR = 0;
constexpr int L_ST = 1024;
constexpr int L_XN = 1040;
constexpr int L_HS = 1296;
constexpr int L_GU = 1312;
constexpr int L_DN = 2336;

__device__ __forceinline__ float bflo(u32 u) { return __uint_as_float(u << 16); }
__device__ __forceinline__ float bfhi(u32 u) { return __uint_as_float(u & 0xffff0000u); }
__device__ __forceinline__ u32 cvtpk(float lo, float hi) {
  v2f v = {lo, hi};
  return __builtin_bit_cast(u32, __builtin_convertvector(v, bf16x2));
}
__device__ __forceinline__ float rbf(float f) { return bflo(cvtpk(f, 0.f)); }
__device__ __forceinline__ float dot2(u32 w, u32 a, float acc) {
  return __builtin_amdgcn_fdot2_f32_bf16(__builtin_bit_cast(bf16x2, w), __builtin_bit_cast(bf16x2, a), acc, false);
}
__device__ __forceinline__ float bsel(u32 m, float a, float b) {
  return __uint_as_float((__float_as_uint(a) & m) | (__float_as_uint(b) & ~m));
}
template <int CTRL> __device__ __forceinline__ float dpp(float x) {
  return __builtin_bit_cast(float, __builtin_amdgcn_mov_dpp(__builtin_bit_cast(int, x), CTRL, 0xf, 0xf, true));
}
constexpr int X1 = 0xB1, X2 = 0x4E, X7 = 0x141, X8 = 0x128;
__device__ __forceinline__ float sw32_sum(float a, float b) {
  auto r = __builtin_amdgcn_permlane32_swap(__float_as_uint(a), __float_as_uint(b), false, false);
  return __uint_as_float(r[0]) + __uint_as_float(r[1]);
}
__device__ __forceinline__ float sw16_sum(float a, float b) {
  auto r = __builtin_amdgcn_permlane16_swap(__float_as_uint(a), __float_as_uint(b), false, false);
  return __uint_as_float(r[0]) + __uint_as_float(r[1]);
}
__device__ __forceinline__ float sw32_max(float a) {
  auto r = __builtin_amdgcn_permlane32_swap(__float_as_uint(a), __float_as_uint(a), false, false);
  return fmaxf(__uint_as_float(r[0]), __uint_as_float(r[1]));
}
__device__ __forceinline__ float sw16_max(float a) {
  auto r = __builtin_amdgcn_permlane16_swap(__float_as_uint(a), __float_as_uint(a), false, false);
  return fmaxf(__uint_as_float(r[0]), __uint_as_float(r[1]));
}
template <int CTRL> __device__ __forceinline__ float tr_step(float lo, float hi, bool sel) {
  const float t1 = lo + dpp<CTRL>(lo), t2 = hi + dpp<CTRL>(hi);
  return sel ? t2 : t1;
}
__device__ __forceinline__ float wave_sum(float x) {
  x += dpp<X1>(x); x += dpp<X2>(x); x += dpp<X7>(x); x += dpp<X8>(x);
  x = sw16_sum(x, x); return sw32_sum(x, x);
}
__device__ __forceinline__ float half_sum(float x) {
  x += dpp<X1>(x); x += dpp<X2>(x); x += dpp<X7>(x); x += dpp<X8>(x);
  return sw16_sum(x, x);
}
__device__ __forceinline__ v4u ldnt(const u16* p) { return __builtin_nontemporal_load(reinterpret_cast<const v4u*>(p)); }
__device__ __forceinline__ v4u ldb(__amdgpu_buffer_rsrc_t rs, int off) { return __builtin_amdgcn_raw_buffer_load_b128(rs, off, 0, AUX_NT); }
__device__ __forceinline__ v4u ldg(__amdgpu_buffer_rsrc_t rs, int off) { return __builtin_amdgcn_raw_buffer_load_b128(rs, off, 0, AUX_SC1); }
__device__ __forceinline__ void stg(__amdgpu_buffer_rsrc_t rs, int off, u32 a, u32 b, u32 t0, u32 t1) {
  v4u v = {a, b, t0, t1};
  __builtin_amdgcn_raw_buffer_store_b128(v, rs, off, 0, AUX_SC1);
}
__device__ __forceinline__ void bar() {
  asm volatile("s_waitcnt lgkmcnt(0)" ::: "memory");
  __builtin_amdgcn_s_barrier();
  asm volatile("" ::: "memory");
}
__device__ __forceinline__ v4f mfma(v4u a, v4u b, v4f c) {
  return __builtin_amdgcn_mfma_f32_16x16x32_bf16(__builtin_bit_cast(bf16x8, a), __builtin_bit_cast(bf16x8, b), c, 0, 0, 0);
}
__device__ __forceinline__ u32 plo(u32 r0, u32 r1) { return __builtin_amdgcn_perm(r1, r0, 0x05040100u); }
__device__ __forceinline__ u32 phi(u32 r0, u32 r1) { return __builtin_amdgcn_perm(r1, r0, 0x07060302u); }
__device__ __forceinline__ void pairup(v4u& a, v4u& b) {
  v4u x, y;
  x[0] = plo(a[0], b[0]); x[1] = phi(a[0], b[0]); x[2] = plo(a[1], b[1]); x[3] = phi(a[1], b[1]);
  y[0] = plo(a[2], b[2]); y[1] = phi(a[2], b[2]); y[2] = plo(a[3], b[3]); y[3] = phi(a[3], b[3]);
  a = x; b = y;
}
__device__ __forceinline__ void fmap(float (&acc)[8], v4u a, v4u b, u32 n) {
#pragma unroll
  for (int j = 0; j < 4; ++j) { acc[j] = dot2(a[j], n, acc[j]); acc[4 + j] = dot2(b[j], n, acc[4 + j]); }
}
__device__ __forceinline__ void fma8(float (&acc)[8], v4u wv, u32 alo, u32 ahi) {
#pragma unroll
  for (int j = 0; j < 4; ++j) { acc[2 * j] = dot2(wv[j], alo, acc[2 * j]); acc[2 * j + 1] = dot2(wv[j], ahi, acc[2 * j + 1]); }
}
__device__ __forceinline__ u64 gtag(const v4u& g) { return ((u64)g[3] << 32) | (u64)g[2]; }
__device__ __forceinline__ void mark(v4u& g, bool unused, u64 tag) {
  g[2] = unused ? (u32)tag : g[2]; g[3] = unused ? (u32)(tag >> 32) : g[3];
}
template <int N, int MANY> __device__ __forceinline__ u32 poll(v4u srd, const int (&off)[N], v4u (&gr)[N], u64 tag) {
  u32 spins = 0;
  u64 m[N], any = 0;
#pragma unroll
  for (int i = 0; i < N; ++i) { m[i] = __ballot(gtag(gr[i]) != tag); any |= m[i]; }
  if (__builtin_expect(any != 0, 0)) {
    do {
      if (MANY < 64 && __builtin_popcountll(any) > MANY) __builtin_amdgcn_s_sleep(8);
#pragma unroll
      for (int i = 0; i < N; ++i)
        asm volatile("s_mov_b64 exec, %2\n\tbuffer_load_dwordx4 %0, %1, %3, 0 offen sc1\n\ts_mov_b64 exec, -1"
                     : "+v"(gr[i]) : "v"(off[i]), "s"(m[i]), "s"(srd) : "memory");
#pragma unroll
      for (int i = 0; i < N; ++i) asm volatile("s_waitcnt vmcnt(%1)" : "+v"(gr[i]) : "i"(N - 1 - i) : "memory");
      any = 0;
#pragma unroll
      for (int i = 0; i < N; ++i) { m[i] = __ballot(gtag(gr[i]) != tag); any |= m[i]; }
    } while (any != 0 && ++spins < SPIN_CAP);
    ++spins;
  }
  return spins;
}
__device__ __forceinline__ float rcp(float x) { return __builtin_amdgcn_rcpf(x); }
#define STAMP(i) do { } while (0)
#define STAMP_FLUSH() do { } while (0)
#define SB() __builtin_amdgcn_sched_barrier(0)
#define TAG() const u64 tag = (((u64)__builtin_amdgcn_readfirstlane(ep[1]) << 32) | (u64)__builtin_amdgcn_readfirstlane(ep[0])) + 1; \
  const u32 t0 = (u32)tag, t1 = (u32)(tag >> 32)

__global__ void __launch_bounds__(512, 2)
fused_decode_block(const u16* __restrict__ x, const u16* __restrict__ Kc, const u16* __restrict__ Vc, const u16* __restrict__ wq,
                   const u16* __restrict__ wo, const u16* __restrict__ wg, const u16* __restrict__ wu, const u16* __restrict__ wd,
                   u16* __restrict__ out, unsigned char* ws) {
  __shared__ __attribute__((aligned(16))) float lds[L_END];
  const int tid = threadIdx.x, lane = tid & 63;
  const int w = __builtin_amdgcn_readfirstlane(tid >> 6);
  const int b = blockIdx.x, g = b & 7, s = b >> 3;
  const __amdgpu_buffer_rsrc_t rs = __builtin_amdgcn_make_buffer_rsrc((void*)ws, 0, WS_END, 0x00020000);
  const v4u srd = {(u32)(uintptr_t)ws, (u32)((uintptr_t)ws >> 32) & 0xffffu, (u32)WS_END, 0x00020000u};
  STAMP(0);
  const v2u ep = __builtin_amdgcn_raw_buffer_load_b64(rs, WS_EPOCH, 0, AUX_SC1);
  u64 pc;
  asm volatile("s_getpc_b64 %0" : "=s"(pc));
  const v4u cpf = *reinterpret_cast<const __attribute__((address_space(1))) v4u*>((pc & ~(u64)255) + ((w == 7 && s < CODE_KB) ? s * 1024 + lane * 16 : 0));
#define CODE_PF_USE() asm volatile("" :: "v"(cpf))

  if (s < NSP) {
    volatile LAS u32* lflag = (volatile LAS u32*)(&lds[L_FLAG]);
    if (tid == 0) { lflag[0] = 0u; lflag[1] = 0u; }
    __builtin_amdgcn_s_setprio(1);
    bar();
    const int g4 = lane >> 4, c = lane & 15;
    const int tile0 = (256 * s) / 7, nx = (256 * (s + 1)) / 7 - tile0 - 32;
    const bool five = w < nx;
    const int key0 = (tile0 + 4 * w + (five ? w : nx)) * 8;
    const __amdgpu_buffer_rsrc_t rk = __builtin_amdgcn_make_buffer_rsrc((void*)Kc, 0, 8 * SK * DH * 2, 0x00020000);
    const __amdgpu_buffer_rsrc_t rv = __builtin_amdgcn_make_buffer_rsrc((void*)Vc, 0, 8 * SK * DH * 2, 0x00020000);
    v4u kr[20], vr[20];
    {
      const int kb = ((g * SK + key0 + (c >> 1)) * DH + (c & 1) * 32 + g4 * 8) * 2;
      const int kb5 = five ? kb + 4 * 4096 : OOB;
#pragma unroll
      for (int t = 0; t < 4; ++t)
#pragma unroll
        for (int ks = 0; ks < 4; ++ks) kr[t * 4 + ks] = ldb(rk, kb + t * 4096 + ks * 128);
#pragma unroll
      for (int ks = 0; ks < 4; ++ks) kr[16 + ks] = ldb(rk, five ? kb5 + ks * 128 : OOB);
      SB();
      const int vb = ((g * SK + key0 + 2 * g4) * DH + 8 * c) * 2;
#pragma unroll
      for (int hf = 0; hf < 2; ++hf) {
#pragma unroll
        for (int j = 0; j < 8; ++j) vr[hf * 10 + j] = ldb(rv, vb + (8 * (j >> 1) + (j & 1)) * 512 + 256 * hf);
#pragma unroll
        for (int j = 0; j < 2; ++j) vr[hf * 10 + 8 + j] = ldb(rv, five ? vb + (32 + j) * 512 + 256 * hf : OOB);
        SB();
      }
    }
    const int oh = 2 * g + s / 14, cb = s % 14;
    const int sub = lane >> 3, gq = lane & 7;
    const __amdgpu_buffer_rsrc_t ro = __builtin_amdgcn_make_buffer_rsrc((void*)wo, 0, NQ * D * 2, 0x00020000);
    const int oo = ((oh * 256 + 32 * w + 4 * gq + 2 * (lane >> 5)) * D + 32 * cb + 8 * (sub & 3)) * 2;
    const v4u woa0 = ldb(ro, w >= 4 ? oo : OOB), woa1 = ldb(ro, w >= 4 ? oo + D * 2 : OOB);
    SB();
    STAMP(1);
    TAG();
    CODE_PF_USE();
    if (w == 0) {
      __builtin_amdgcn_s_setprio(3);
      v4u qg[2];
      int qoff[2];
#pragma unroll
      for (int i = 0; i < 2; ++i) { qoff[i] = WS_Q + (g * 128 + i * 64 + lane) * 16; qg[i] = ldg(rs, qoff[i]); }
      poll<2, 64>(srd, qoff, qg, tag);
#pragma unroll
      for (int i = 0; i < 2; ++i)
        *reinterpret_cast<v2u*>(&lds[L_QS + (i * 64 + lane) * 2]) = v2u{qg[i][0], qg[i][1]};
      asm volatile("s_waitcnt lgkmcnt(0)" ::: "memory");
      lflag[0] = 1u;
      __builtin_amdgcn_s_setprio(1);
    }
    for (u32 sp = 0; lflag[0] == 0u && sp < (SPIN_CAP << 4); ++sp) __builtin_amdgcn_s_sleep(1);
    asm volatile("" ::: "memory");
    STAMP(2);
    const int hd = (c >> 2) & 1;
    const u32 meta = ((c >> 1) & 1) ? 0xffffffffu : 0u, mvar = (c & 1) ? 0xffffffffu : 0u, mhi = (g4 >> 1) ? 0xffffffffu : 0u;
    float sc[10];
    {
      v4u qf[4];
#pragma unroll
      for (int ks = 0; ks < 4; ++ks) qf[ks] = *reinterpret_cast<const v4u*>(&lds[L_QS + hd * 128 + ks * 32 + ((c >> 1) & 1) * 16 + g4 * 4]);
#pragma unroll
      for (int t = 0; t < 5; ++t) {
        v4f d = {0.f, 0.f, 0.f, 0.f};
#pragma unroll
        for (int ks = 0; ks < 4; ++ks) d = mfma(kr[t * 4 + ks], qf[ks], d);
        const float sa = bsel(meta, d[1], d[0]), sb = bsel(meta, d[3], d[2]);
        sc[2 * t] = rbf(sa + dpp<X2>(sa)) * 0.0625f;
        sc[2 * t + 1] = rbf(sb + dpp<X2>(sb)) * 0.0625f;
      }
      if (!five) { sc[8] = -INFINITY; sc[9] = -INFINITY; }
    }
    float m = sc[0];
#pragma unroll
    for (int j = 1; j < 10; ++j) m = fmaxf(m, sc[j]);
    m = sw32_max(sw16_max(m));
    v4u pa;
    v4u pa5;
    {
      float p[10], l = 0.f;
#pragma unroll
      for (int j = 0; j < 10; ++j) { p[j] = __builtin_amdgcn_exp2f((sc[j] - m) * LOG2E); l += p[j]; }
      l = sw16_sum(l, l); l = sw32_sum(l, l);
      if ((lane & 0x3b) == 0) { lds[L_WMAX + w * 2 + hd] = m; lds[L_WSUM + w * 2 + hd] = l; }
#pragma unroll
      for (int j = 0; j < 10; ++j) { const float hi = rbf(p[j]); p[j] = bsel(mvar, p[j] - hi, hi); }
#pragma unroll
      for (int t = 0; t < 4; ++t) pa[t] = cvtpk(p[2 * t], p[2 * t + 1]);
      pa5[0] = cvtpk(p[8], p[9]); pa5[1] = 0u; pa5[2] = 0u; pa5[3] = 0u;
    }
    STAMP(3);
#pragma unroll
    for (int hf = 0; hf < 2; ++hf) {
      float xo[8];
#pragma unroll
      for (int d = 0; d < 8; ++d) {
        v4u bf;
        const u32 sel = (d & 1) ? 0x07060302u : 0x05040100u;
#pragma unroll
        for (int t = 0; t < 4; ++t) bf[t] = __builtin_amdgcn_perm(vr[hf * 10 + 2 * t + 1][d >> 1], vr[hf * 10 + 2 * t][d >> 1], sel);
        v4u b5;
        b5[0] = __builtin_amdgcn_perm(vr[hf * 10 + 9][d >> 1], vr[hf * 10 + 8][d >> 1], sel); b5[1] = 0u; b5[2] = 0u; b5[3] = 0u;
        v4f dd = mfma(pa, bf, v4f{0.f, 0.f, 0.f, 0.f});
        dd = mfma(pa5, b5, dd);
        xo[d] = dd[0] + dd[1];
      }
      v4f y;
#pragma unroll
      for (int e = 0; e < 4; ++e) y[e] = bsel(mhi, xo[4 + e], xo[e]);
      *reinterpret_cast<v4f*>(&lds[L_ORED + (w * 2 + (g4 & 1)) * 256 + 128 * hf + 8 * c + 4 * (g4 >> 1)]) = y;
    }
    STAMP(4);
    __builtin_amdgcn_s_setprio(0);
    asm volatile("s_waitcnt lgkmcnt(0)" ::: "memory");
    if (lane == 0) __hip_atomic_fetch_add((LAS u32*)(&lds[L_FLAG + 1]), 1u, __ATOMIC_RELAXED, __HIP_MEMORY_SCOPE_WORKGROUP);
    v4u wor[2];
    wor[0] = ldb(ro, w < 4 ? oo : OOB); wor[1] = ldb(ro, w < 4 ? oo + D * 2 : OOB);
    SB();
    if (w < 4) {
      v4u po[7], ml = {0u, 0u, 0u, 0u};
      int poff[7];
      const int mysp = w + 4 * (lane & 7);
      const bool mlwant = lane < 7 && mysp != s;
      const int mloff = WS_ML + (oh * 32 + mysp) * 128;
      mark(ml, !mlwant, tag);
      if (!mlwant) ml[0] = 0xff800000u;
#pragma unroll
      for (int i = 0; i < 7; ++i) {
        po[i] = v4u{0u, 0u, 0u, 0u};
        mark(po[i], w + 4 * i == s, tag);
        poff[i] = WS_PO + ((oh * 32 + w + 4 * i) * 64 + lane) * 16;
      }
      u64 mm = __ballot(gtag(ml) != tag);
      for (u32 sp = 0; sp < SPIN_CAP; ++sp) {
        const bool few = __builtin_popcountll(mm) <= 1;
        u64 pm[7], pany = 0;
#pragma unroll
        for (int i = 0; i < 7; ++i) { pm[i] = __ballot(gtag(po[i]) != tag); pany |= pm[i]; }
        if ((mm | pany) == 0) break;
        if (!few && sp) __builtin_amdgcn_s_sleep(8);
        asm volatile("s_mov_b64 exec, %2\n\tbuffer_load_dwordx4 %0, %1, %3, 0 offen sc1\n\ts_mov_b64 exec, -1"
                     : "+v"(ml) : "v"(mloff), "s"(mm), "s"(srd) : "memory");
#pragma unroll
        for (int i = 0; i < 7; ++i) {
          const u64 mk = (few || !((mm >> i) & 1)) ? pm[i] : 0ull;
          asm volatile("s_mov_b64 exec, %2\n\tbuffer_load_dwordx4 %0, %1, %3, 0 offen sc1\n\ts_mov_b64 exec, -1"
                       : "+v"(po[i]) : "v"(poff[i]), "s"(mk), "s"(srd) : "memory");
        }
        asm volatile("s_waitcnt vmcnt(0)" : "+v"(ml), "+v"(po[0]), "+v"(po[1]), "+v"(po[2]), "+v"(po[3]), "+v"(po[4]), "+v"(po[5]), "+v"(po[6]) :: "memory");
        mm = __ballot(gtag(ml) != tag);
      }
      STAMP(7);
      const float mi = __uint_as_float(ml[0]);
      float mx = fmaxf(mi, dpp<X1>(mi)); mx = fmaxf(mx, dpp<X2>(mx)); mx = fmaxf(mx, dpp<X7>(mx));
      const float wt = __builtin_amdgcn_exp2f((mi - mx) * LOG2E);
      float lw = wt * __uint_as_float(ml[1]);
      lw += dpp<X1>(lw); lw += dpp<X2>(lw); lw += dpp<X7>(lw);
      v4f o = {0.f, 0.f, 0.f, 0.f};
#pragma unroll
      for (int i = 0; i < 7; ++i) {
        const float f = __builtin_bit_cast(float, __builtin_amdgcn_readlane(__builtin_bit_cast(int, wt), i));
        o[0] = fmaf(f, bflo(po[i][0]), o[0]); o[1] = fmaf(f, bfhi(po[i][0]), o[1]);
        o[2] = fmaf(f, bflo(po[i][1]), o[2]); o[3] = fmaf(f, bfhi(po[i][1]), o[3]);
      }
      *reinterpret_cast<v4f*>(&lds[L_CMB + w * 256 + lane * 4]) = o;
      if (lane == 0) *reinterpret_cast<v2f*>(&lds[L_CML + w * 2]) = v2f{mx, lw};
    } else if (w < 6) {
      const int hh = w - 4;
      for (u32 sp = 0; lflag[1] < 8u && sp < (SPIN_CAP << 4); ++sp) { }
      asm volatile("" ::: "memory");
      STAMP(5);
      float mw[8], Mh;
#pragma unroll
      for (int ww = 0; ww < 8; ++ww) mw[ww] = lds[L_WMAX + ww * 2 + hh];
      Mh = fmaxf(fmaxf(fmaxf(mw[0], mw[1]), fmaxf(mw[2], mw[3])), fmaxf(fmaxf(mw[4], mw[5]), fmaxf(mw[6], mw[7])));
      v4f o = {0.f, 0.f, 0.f, 0.f}, o2 = {0.f, 0.f, 0.f, 0.f};
      float L = 0.f;
#pragma unroll
      for (int ww = 0; ww < 8; ++ww) {
        const float f = __builtin_amdgcn_exp2f((mw[ww] - Mh) * LOG2E);
        const v4f t = *reinterpret_cast<const v4f*>(&lds[L_ORED + (ww * 2 + hh) * 256 + lane * 4]);
        if (ww & 1) o2 += f * t; else o += f * t;
        L = fmaf(f, lds[L_WSUM + ww * 2 + hh], L);
      }
      o += o2;
      const u32 c0 = cvtpk(o[0], o[1]), c1 = cvtpk(o[2], o[3]);
      stg(rs, WS_PO + (((2 * g + hh) * 32 + s) * 64 + lane) * 16, c0, c1, t0, t1);
      if (lane == 0) stg(rs, WS_ML + ((2 * g + hh) * 32 + s) * 128, __float_as_uint(Mh), __float_as_uint(L), t0, t1);
      if (hh == (oh & 1)) {
        *reinterpret_cast<v4f*>(&lds[L_CMB + 4 * 256 + lane * 4]) = v4f{bflo(c0), bfhi(c0), bflo(c1), bfhi(c1)};
        if (lane == 0) *reinterpret_cast<v2f*>(&lds[L_CML + 8]) = v2f{Mh, L};
      }
      STAMP(6);
    }
    wor[0] |= woa0; wor[1] |= woa1;
    pairup(wor[0], wor[1]);
    asm volatile("" : "+v"(wor[0]), "+v"(wor[1]));
    bar();
    {
      v2f cm[5], co[5];
#pragma unroll
      for (int i = 0; i < 5; ++i) {
        cm[i] = *reinterpret_cast<const v2f*>(&lds[L_CML + i * 2]);
        co[i] = *reinterpret_cast<const v2f*>(&lds[L_CMB + i * 256 + 32 * w + 4 * gq + 2 * (lane >> 5)]);
      }
      const float Mx = fmaxf(fmaxf(fmaxf(cm[0][0], cm[1][0]), fmaxf(cm[2][0], cm[3][0])), cm[4][0]);
      float L = 0.f;
      v2f u = {0.f, 0.f};
#pragma unroll
      for (int i = 0; i < 5; ++i) {
        const float f = __builtin_amdgcn_exp2f((cm[i][0] - Mx) * LOG2E);
        L = fmaf(f, cm[i][1], L);
        u += f * co[i];
      }
      const float rl = rcp(L);
      const u32 n = cvtpk(u[0] * rl, u[1] * rl);
      float acc[8];
#pragma unroll
      for (int j = 0; j < 8; ++j) acc[j] = 0.f;
      fmap(acc, wor[0], wor[1], n);
      float r4[4], r2[2];
#pragma unroll
      for (int j = 0; j < 4; ++j) r4[j] = sw32_sum(acc[j], acc[j + 4]);
#pragma unroll
      for (int j = 0; j < 2; ++j) r2[j] = tr_step<X7>(r4[j], r4[j + 2], (lane & 4) != 0);
      float r1 = tr_step<X2>(r2[0], r2[1], (lane & 2) != 0);
      r1 += dpp<X1>(r1);
      if ((lane & 1) == 0) lds[L_S1 + w * 32 + (sub & 3) * 8 + (lane >> 5) * 4 + ((lane >> 2) & 1) * 2 + ((lane >> 1) & 1)] = r1;
    }
    bar();
    if (w == 0 && lane < 16) {
      v2f a[8];
#pragma unroll
      for (int ww = 0; ww < 8; ++ww) a[ww] = *reinterpret_cast<const v2f*>(&lds[L_S1 + ww * 32 + lane * 2]);
      const v2f t = ((a[0] + a[1]) + (a[2] + a[3])) + ((a[4] + a[5]) + (a[6] + a[7]));
      stg(rs, WS_PH + (oh * 224 + cb * 16 + lane) * 16, __float_as_uint(t[0]), __float_as_uint(t[1]), t0, t1);
    }
    STAMP(8);
    STAMP_FLUSH();
    return;
  }

  const int f = g * 4 + (s - NSP);
  const bool ffn = f < 28;
  const int r4 = lane >> 4;
  const v4u xv = *reinterpret_cast<const v4u*>(x + (lane < 56 ? lane : 0) * 8);
  u32 xq[7];
  v4u wa[14], wf[14], wdn[8];
  {
    const int row0 = 56 * w + 14 * r4;
#pragma unroll
    for (int i = 0; i < 7; ++i) xq[i] = *reinterpret_cast<const u32*>(x + row0 + 2 * i);
    const u16* p = wq + (size_t)row0 * NQ + 128 * f + (lane & 15) * 8;
#pragma unroll
    for (int i = 0; i < 14; ++i) wa[i] = ldnt(p + (size_t)i * NQ);
    SB();
    bar();
    SB();
    const int fc = ffn ? f : 0;
    const u16* pf = (((lane >> 3) & 1) ? wu : wg) + (size_t)row0 * FF + 64 * fc + (lane & 7) * 8;
    float xf[8];
#pragma unroll
    for (int j = 0; j < 4; ++j) { xf[2 * j] = bflo(xv[j]); xf[2 * j + 1] = bfhi(xv[j]); }
    float sm = 0.f;
#pragma unroll
    for (int j = 0; j < 8; ++j) sm += xf[j];
    sm = wave_sum(lane < 56 ? sm : 0.f);
    const float mean = sm * (1.0f / D);
    float sq = 0.f;
#pragma unroll
    for (int j = 0; j < 8; ++j) { const float d = xf[j] - mean; sq += d * d; }
    sq = wave_sum(lane < 56 ? sq : 0.f);
    const float mb = rbf(mean), den = rbf(sqrtf(rbf(rbf(sq * (1.0f / D)) + 1e-5f)));
    float acc[8];
#pragma unroll
    for (int j = 0; j < 8; ++j) acc[j] = 0.f;
#pragma unroll
    for (int i = 0; i < 7; ++i) {
      const u32 n = cvtpk(rbf(bflo(xq[i]) - mb) / den, rbf(bfhi(xq[i]) - mb) / den);
      pairup(wa[2 * i], wa[2 * i + 1]);
      fmap(acc, wa[2 * i], wa[2 * i + 1], n);
    }
    float a4[4];
#pragma unroll
    for (int j = 0; j < 4; ++j) a4[j] = sw32_sum(acc[j], acc[j + 4]);
    v2f a2;
#pragma unroll
    for (int j = 0; j < 2; ++j) a2[j] = sw16_sum(a4[j], a4[j + 2]);
    *reinterpret_cast<v2f*>(&lds[L_QR + w * 128 + (lane & 15) * 8 + (lane >> 5) * 4 + ((lane >> 4) & 1) * 2]) = a2;
    bar();
    TAG();
    CODE_PF_USE();
    if (w == 0 && lane < 32) {
      v4f qv = *reinterpret_cast<const v4f*>(&lds[L_QR + lane * 4]);
#pragma unroll
      for (int ww = 1; ww < 8; ++ww) qv += *reinterpret_cast<const v4f*>(&lds[L_QR + ww * 128 + lane * 4]);
      stg(rs, WS_Q + (f * 32 + lane) * 16, cvtpk(qv[0], qv[1]), cvtpk(qv[2], qv[3]), t0, t1);
    }
    STAMP(1);
    if (!ffn) { STAMP_FLUSH(); return; }
    SB();
#pragma unroll
    for (int i = 0; i < 14; ++i) wf[i] = ldnt(pf + (size_t)i * FF);
    SB();
  }
  const int l56 = lane < 56 ? lane : 0;
  {
    const u16* pd = wd + (size_t)(64 * f + 8 * w) * D + 8 * l56;
#pragma unroll
    for (int i = 0; i < 8; ++i) wdn[i] = ldnt(pd + i * D);
    SB();
  }
#pragma unroll
  for (int i = 0; i < 7; ++i) { pairup(wf[2 * i], wf[2 * i + 1]); asm volatile("" : "+v"(wf[2 * i]), "+v"(wf[2 * i + 1])); }
#pragma unroll
  for (int i = 0; i < 4; ++i) { pairup(wdn[2 * i], wdn[2 * i + 1]); asm volatile("" : "+v"(wdn[2 * i]), "+v"(wdn[2 * i + 1])); }
  {
    float acc[8];
#pragma unroll
    for (int j = 0; j < 8; ++j) acc[j] = 0.f;
#pragma unroll
    for (int i = 0; i < 7; ++i) fmap(acc, wf[2 * i], wf[2 * i + 1], 0x3f803f80u);
    float a4[4];
#pragma unroll
    for (int j = 0; j < 4; ++j) a4[j] = sw32_sum(acc[j], acc[j + 4]);
    v2f a2;
#pragma unroll
    for (int j = 0; j < 2; ++j) a2[j] = sw16_sum(a4[j], a4[j + 2]);
    *reinterpret_cast<v2f*>(&lds[L_QR + w * 128 + (lane & 15) * 8 + (lane >> 5) * 4 + ((lane >> 4) & 1) * 2]) = a2;
  }
  bar();
  const int u = 8 * w + (lane & 7);
  float SG = 0.f, SU = 0.f;
#pragma unroll
  for (int ww = 0; ww < 8; ++ww) { SG += lds[L_QR + ww * 128 + u]; SU += lds[L_QR + ww * 128 + 64 + u]; }
  asm volatile("" : "+v"(SG), "+v"(SU));
  TAG();
  STAMP(2);
  {
    const int hsub = lane >> 5;
    const bool act = (lane & 31) < 28;
    const int pp = act ? (lane & 31) : 0;
    int off[8];
    v4u gr[8];
    const u32 xp = *reinterpret_cast<const u32*>(x + 56 * w + 2 * pp);
#pragma unroll
    for (int i = 0; i < 8; ++i) { off[i] = act ? WS_PH + ((2 * i + hsub) * 224 + 28 * w + pp) * 16 : OOB; gr[i] = ldg(rs, off[i]); }
#pragma unroll
    for (int i = 0; i < 8; ++i) mark(gr[i], !act, tag);
    poll<8, 32>(srd, off, gr, tag);
    STAMP(9);
    float h0 = ((__uint_as_float(gr[0][0]) + __uint_as_float(gr[1][0])) + (__uint_as_float(gr[2][0]) + __uint_as_float(gr[3][0]))) +
               ((__uint_as_float(gr[4][0]) + __uint_as_float(gr[5][0])) + (__uint_as_float(gr[6][0]) + __uint_as_float(gr[7][0])));
    float h1 = ((__uint_as_float(gr[0][1]) + __uint_as_float(gr[1][1])) + (__uint_as_float(gr[2][1]) + __uint_as_float(gr[3][1]))) +
               ((__uint_as_float(gr[4][1]) + __uint_as_float(gr[5][1])) + (__uint_as_float(gr[6][1]) + __uint_as_float(gr[7][1])));
    h0 = sw32_sum(h0, h0); h1 = sw32_sum(h1, h1);
    const u32 a01 = cvtpk(h0, h1);
    const u32 h01 = cvtpk(bflo(xp) + bflo(a01), bfhi(xp) + bfhi(a01));
    h0 = bflo(h01); h1 = bfhi(h01);
    LAS u32* xn = (LAS u32*)(&lds[L_XN + w * 32]);
    if (lane < 28) xn[(lane / 7) * 8 + lane % 7] = h01;
    asm volatile("s_waitcnt lgkmcnt(0)" ::: "memory");
    const v4u na = *(const LAS v4u*)(xn + r4 * 8), nb = *(const LAS v4u*)(xn + r4 * 8 + 4);
    const float lm = half_sum(act ? h0 + h1 : 0.f) * (1.0f / 56.0f);
    const float d0 = h0 - lm, d1 = h1 - lm;
    const float m2 = half_sum(act ? d0 * d0 + d1 * d1 : 0.f);
    if (lane == 0) *reinterpret_cast<v2f*>(&lds[L_ST + w * 2]) = v2f{lm, m2};
    {
      const int cp = 28 * w + pp - 8 * f;
      if (act && hsub == 0 && cp >= 0 && cp < 8) *reinterpret_cast<v2f*>(&lds[L_HS + cp * 2]) = v2f{h0, h1};
    }
    float acc[8];
#pragma unroll
    for (int j = 0; j < 8; ++j) acc[j] = 0.f;
#pragma unroll
    for (int i = 0; i < 7; ++i) fmap(acc, wf[2 * i], wf[2 * i + 1], i < 4 ? na[i] : nb[i - 4]);
    float a4[4];
#pragma unroll
    for (int j = 0; j < 4; ++j) a4[j] = sw32_sum(acc[j], acc[j + 4]);
    v2f a2;
#pragma unroll
    for (int j = 0; j < 2; ++j) a2[j] = sw16_sum(a4[j], a4[j + 2]);
    *reinterpret_cast<v2f*>(&lds[L_GU + w * 128 + (lane & 15) * 8 + (lane >> 5) * 4 + ((lane >> 4) & 1) * 2]) = a2;
  }
  bar();
  {
    float gp[8], up[8], lms[8], m2s[8];
#pragma unroll
    for (int ww = 0; ww < 8; ++ww) {
      gp[ww] = lds[L_GU + ww * 128 + u]; up[ww] = lds[L_GU + ww * 128 + 64 + u];
      const v2f v = *reinterpret_cast<const v2f*>(&lds[L_ST + ww * 2]); lms[ww] = v[0]; m2s[ww] = v[1];
    }
    const float mean = (((lms[0] + lms[1]) + (lms[2] + lms[3])) + ((lms[4] + lms[5]) + (lms[6] + lms[7]))) * 0.125f;
#pragma unroll
    for (int ww = 0; ww < 8; ++ww) { const float d = lms[ww] - mean; m2s[ww] = fmaf(56.0f * d, d, m2s[ww]); }
    const float M2 = ((m2s[0] + m2s[1]) + (m2s[2] + m2s[3])) + ((m2s[4] + m2s[5]) + (m2s[6] + m2s[7]));
    const float mb = rbf(mean), rden = rcp(rbf(__builtin_amdgcn_sqrtf(rbf(rbf(M2 * (1.0f / D)) + 1e-5f))));
    float G = ((gp[0] + gp[1]) + (gp[2] + gp[3])) + ((gp[4] + gp[5]) + (gp[6] + gp[7]));
    float U = ((up[0] + up[1]) + (up[2] + up[3])) + ((up[4] + up[5]) + (up[6] + up[7]));
    G = rbf(fmaf(-mb, SG, G) * rden); U = rbf(fmaf(-mb, SU, U) * rden);
    const float z = 0.7978845608028654f * (G + 0.044715f * G * G * G);
    const float th = 1.0f - 2.0f * rcp(__builtin_amdgcn_exp2f(z * (2.0f * LOG2E)) + 1.0f);
    const float av = rbf(0.5f * G * (1.0f + th)) * U;
    const u32 pk = cvtpk(av, dpp<X1>(av));
    float acc[8];
#pragma unroll
    for (int j = 0; j < 8; ++j) acc[j] = 0.f;
#pragma unroll
    for (int i = 0; i < 4; ++i) fmap(acc, wdn[2 * i], wdn[2 * i + 1], __builtin_amdgcn_readlane(pk, 2 * i));
    if (lane < 56) {
      *reinterpret_cast<v4f*>(&lds[L_DN + w * 448 + lane * 8]) = v4f{acc[0], acc[1], acc[2], acc[3]};
      *reinterpret_cast<v4f*>(&lds[L_DN + w * 448 + lane * 8 + 4]) = v4f{acc[4], acc[5], acc[6], acc[7]};
    }
  }
  bar();
  if (tid < 224) {
    v2f a[8];
#pragma unroll
    for (int ww = 0; ww < 8; ++ww) a[ww] = *reinterpret_cast<const v2f*>(&lds[L_DN + ww * 448 + tid * 2]);
    const v2f t = ((a[0] + a[1]) + (a[2] + a[3])) + ((a[4] + a[5]) + (a[6] + a[7]));
    stg(rs, WS_PD + (f * 224 + tid) * 16, __float_as_uint(t[0]), __float_as_uint(t[1]), t0, t1);
  }
  STAMP(10);
  if (w == 7) {
    int off[4];
    v4u gr[4];
#pragma unroll
    for (int i = 0; i < 4; ++i) {
      off[i] = (i < 3 || lane < 32) ? WS_PD + (((lane >> 3) + 8 * i) * 224 + 8 * f + (lane & 7)) * 16 : OOB;
      gr[i] = ldg(rs, off[i]);
    }
    mark(gr[3], lane >= 32, tag);
    poll<4, 64>(srd, off, gr, tag);
    STAMP(11);
    float a0 = 0.f, a1 = 0.f;
#pragma unroll
    for (int i = 0; i < 4; ++i) { a0 += __uint_as_float(gr[i][0]); a1 += __uint_as_float(gr[i][1]); }
    float r = sw32_sum(a0, a1);
    r = sw16_sum(r, r); r += dpp<X8>(r);
    const float hv = lds[L_HS + (lane & 7) * 2 + (lane >> 5)];
    const u32 ob = cvtpk(hv + rbf(r), 0.f);
    if ((lane & 0x18) == 0) out[16 * f + (lane & 7) * 2 + (lane >> 5)] = (u16)ob;
    if (f == 0 && lane == 0) __builtin_amdgcn_raw_buffer_store_b64(v2u{t0, t1}, rs, WS_EPOCH, 0, AUX_SC1);
  }
  STAMP(12);
  STAMP_FLUSH();
}

}

extern "C" void kernel_launch(void* const* d_in, const int* in_sizes, int n_in, void* d_out, int out_size, void* d_ws,
                              size_t ws_size, hipStream_t stream) {
  fused_decode_block<<<256, 512, 0, stream>>>((const u16*)d_in[0], (const u16*)d_in[1], (const u16*)d_in[2], (const u16*)d_in[3],
                                              (const u16*)d_in[4], (const u16*)d_in[6], (const u16*)d_in[7], (const u16*)d_in[8],
                                              (u16*)d_out, (unsigned char*)d_ws);
}
```

```cpp
#include <hip/hip_runtime.h>
#include <hip/hip_bf16.h>
#include <stdint.h>
#include <stdio.h>

typedef unsigned short u16;
typedef unsigned int u32;
typedef unsigned long long u64;
typedef u32 v4u __attribute__((ext_vector_type(4)));
typedef u32 v2u __attribute__((ext_vector_type(2)));
typedef float v4f __attribute__((ext_vector_type(4)));
typedef float v2f __attribute__((ext_vector_type(2)));
typedef __bf16 bf16x2 __attribute__((ext_vector_type(2)));
typedef __bf16 bf16x8 __attribute__((ext_vector_type(8)));
typedef short s16x4 __attribute__((ext_vector_type(4)));

#define LAS __attribute__((address_space(3)))
#define CODE_PF 0
#define CODE_KB 18
#define DIAG 1

namespace {

constexpr int D = 448, SK = 8192, DH = 256, FF = 1792, NQ = 4096, NSP = 28;
constexpr int WS_EPOCH = 0;
constexpr int WS_Q = 4096;
constexpr int WS_ML = 32768;
constexpr int WS_PO = 131072;
constexpr int WS_PH = WS_PO + 524288;
constexpr int WS_PD = WS_PH + 65536;
constexpr int WS_END = WS_PD + 28 * 3584;
constexpr int WS_STAMP = 1 << 20;
constexpr int AUX_SC1 = 16, AUX_NT = 2;
constexpr int OOB = 0x7ffffff0;
constexpr u32 SPIN_CAP = 1u << 20;
constexpr float LOG2E = 1.4426950408889634f;

constexpr int L_QS = 0;
constexpr int L_WMAX = 256;
constexpr int L_WSUM = 272;
constexpr int L_S1 = 288;
constexpr int L_FLAG = 544;
constexpr int L_CML = 548;
constexpr int L_ORED = 576;
constexpr int L_CMB = L_ORED + 4096;
constexpr int L_END = L_CMB + 5 * 256;
constexpr int L_QR = 0;
constexpr int L_ST = 1024;
constexpr int L_XN = 1040;
constexpr int L_HS = 1296;
constexpr int L_GU = 1312;
constexpr int L_DN = 2336;

__device__ __forceinline__ float bflo(u32 u) { return __uint_as_float(u << 16); }
__device__ __forceinline__ float bfhi(u32 u) { return __uint_as_float(u & 0xffff0000u); }
__device__ __forceinline__ u32 cvtpk(float lo, float hi) {
  v2f v = {lo, hi};
  return __builtin_bit_cast(u32, __builtin_convertvector(v, bf16x2));
}
__device__ __forceinline__ float rbf(float f) { return bflo(cvtpk(f, 0.f)); }
__device__ __forceinline__ float dot2(u32 w, u32 a, float acc) {
  return __builtin_amdgcn_fdot2_f32_bf16(__builtin_bit_cast(bf16x2, w), __builtin_bit_cast(bf16x2, a), acc, false);
}
__device__ __forceinline__ float bsel(u32 m, float a, float b) {
  return __uint_as_float((__float_as_uint(a) & m) | (__float_as_uint(b) & ~m));
}
template <int CTRL> __device__ __forceinline__ float dpp(float x) {
  return __builtin_bit_cast(float, __builtin_amdgcn_mov_dpp(__builtin_bit_cast(int, x), CTRL, 0xf, 0xf, true));
}
constexpr int X1 = 0xB1, X2 = 0x4E, X7 = 0x141, X8 = 0x128;
__device__ __forceinline__ float sw32_sum(float a, float b) {
  auto r = __builtin_amdgcn_permlane32_swap(__float_as_uint(a), __float_as_uint(b), false, false);
  return __uint_as_float(r[0]) + __uint_as_float(r[1]);
}
__device__ __forceinline__ float sw16_sum(float a, float b) {
  auto r = __builtin_amdgcn_permlane16_swap(__float_as_uint(a), __float_as_uint(b), false, false);
  return __uint_as_float(r[0]) + __uint_as_float(r[1]);
}
__device__ __forceinline__ float sw32_max(float a) {
  auto r = __builtin_amdgcn_permlane32_swap(__float_as_uint(a), __float_as_uint(a), false, false);
  return fmaxf(__uint_as_float(r[0]), __uint_as_float(r[1]));
}
__device__ __forceinline__ float sw16_max(float a) {
  auto r = __builtin_amdgcn_permlane16_swap(__float_as_uint(a), __float_as_uint(a), false, false);
  return fmaxf(__uint_as_float(r[0]), __uint_as_float(r[1]));
}
template <int CTRL> __device__ __forceinline__ float tr_step(float lo, float hi, bool sel) {
  const float t1 = lo + dpp<CTRL>(lo), t2 = hi + dpp<CTRL>(hi);
  return sel ? t2 : t1;
}
__device__ __forceinline__ float wave_sum(float x) {
  x += dpp<X1>(x); x += dpp<X2>(x); x += dpp<X7>(x); x += dpp<X8>(x);
  x = sw16_sum(x, x); return sw32_sum(x, x);
}
__device__ __forceinline__ float half_sum(float x) {
  x += dpp<X1>(x); x += dpp<X2>(x); x += dpp<X7>(x); x += dpp<X8>(x);
  return sw16_sum(x, x);
}
__device__ __forceinline__ v4u ldnt(const u16* p) { return __builtin_nontemporal_load(reinterpret_cast<const v4u*>(p)); }
__device__ __forceinline__ v4u ldb(__amdgpu_buffer_rsrc_t rs, int off) { return __builtin_amdgcn_raw_buffer_load_b128(rs, off, 0, AUX_NT); }
__device__ __forceinline__ v4u ldg(__amdgpu_buffer_rsrc_t rs, int off) { return __builtin_amdgcn_raw_buffer_load_b128(rs, off, 0, AUX_SC1); }
__device__ __forceinline__ void stg(__amdgpu_buffer_rsrc_t rs, int off, u32 a, u32 b, u32 t0, u32 t1) {
  v4u v = {a, b, t0, t1};
  __builtin_amdgcn_raw_buffer_store_b128(v, rs, off, 0, AUX_SC1);
}
__device__ __forceinline__ void bar() {
  asm volatile("s_waitcnt lgkmcnt(0)" ::: "memory");
  __builtin_amdgcn_s_barrier();
  asm volatile("" ::: "memory");
}
__device__ __forceinline__ v4f mfma(v4u a, v4u b, v4f c) {
  return __builtin_amdgcn_mfma_f32_16x16x32_bf16(__builtin_bit_cast(bf16x8, a), __builtin_bit_cast(bf16x8, b), c, 0, 0, 0);
}
__device__ __forceinline__ u32 plo(u32 r0, u32 r1) { return __builtin_amdgcn_perm(r1, r0, 0x05040100u); }
__device__ __forceinline__ u32 phi(u32 r0, u32 r1) { return __builtin_amdgcn_perm(r1, r0, 0x07060302u); }
__device__ __forceinline__ void pairup(v4u& a, v4u& b) {
  v4u x, y;
  x[0] = plo(a[0], b[0]); x[1] = phi(a[0], b[0]); x[2] = plo(a[1], b[1]); x[3] = phi(a[1], b[1]);
  y[0] = plo(a[2], b[2]); y[1] = phi(a[2], b[2]); y[2] = plo(a[3], b[3]); y[3] = phi(a[3], b[3]);
  a = x; b = y;
}
__device__ __forceinline__ void fmap(float (&acc)[8], v4u a, v4u b, u32 n) {
#pragma unroll
  for (int j = 0; j < 4; ++j) { acc[j] = dot2(a[j], n, acc[j]); acc[4 + j] = dot2(b[j], n, acc[4 + j]); }
}
__device__ __forceinline__ void fma8(float (&acc)[8], v4u wv, u32 alo, u32 ahi) {
#pragma unroll
  for (int j = 0; j < 4; ++j) { acc[2 * j] = dot2(wv[j], alo, acc[2 * j]); acc[2 * j + 1] = dot2(wv[j], ahi, acc[2 * j + 1]); }
}
__device__ __forceinline__ u64 gtag(const v4u& g) { return ((u64)g[3] << 32) | (u64)g[2]; }
__device__ __forceinline__ void mark(v4u& g, bool unused, u64 tag) {
  g[2] = unused ? (u32)tag : g[2]; g[3] = unused ? (u32)(tag >> 32) : g[3];
}
template <int N, int MANY> __device__ __forceinline__ u32 poll(v4u srd, const int (&off)[N], v4u (&gr)[N], u64 tag) {
  u32 spins = 0;
  u64 m[N], any = 0;
#pragma unroll
  for (int i = 0; i < N; ++i) { m[i] = __ballot(gtag(gr[i]) != tag); any |= m[i]; }
  if (__builtin_expect(any != 0, 0)) {
    do {
      if (MANY < 64 && __builtin_popcountll(any) > MANY) __builtin_amdgcn_s_sleep(8);
#pragma unroll
      for (int i = 0; i < N; ++i)
        asm volatile("s_mov_b64 exec, %2\n\tbuffer_load_dwordx4 %0, %1, %3, 0 offen sc1\n\ts_mov_b64 exec, -1"
                     : "+v"(gr[i]) : "v"(off[i]), "s"(m[i]), "s"(srd) : "memory");
#pragma unroll
      for (int i = 0; i < N; ++i) asm volatile("s_waitcnt vmcnt(%1)" : "+v"(gr[i]) : "i"(N - 1 - i) : "memory");
      any = 0;
#pragma unroll
      for (int i = 0; i < N; ++i) { m[i] = __ballot(gtag(gr[i]) != tag); any |= m[i]; }
    } while (any != 0 && ++spins < SPIN_CAP);
    ++spins;
  }
  return spins;
}
__device__ __forceinline__ float rcp(float x) { return __builtin_amdgcn_rcpf(x); }
#define STAMP(i) do { u64 t_; __builtin_amdgcn_sched_barrier(0); asm volatile("s_memrealtime %0\n\ts_waitcnt lgkmcnt(0)" : "=s"(t_) :: "memory"); __builtin_amdgcn_sched_barrier(0); stamps[i] = t_; } while (0)
#define STAMP_FLUSH() do { if (lane == 0) { _Pragma("unroll") for (int i_ = 0; i_ < 16; ++i_) __hip_atomic_store((u64*)(ws + WS_STAMP + ((b * 8 + w) * 16 + i_) * 8), stamps[i_], __ATOMIC_RELAXED, __HIP_MEMORY_SCOPE_AGENT); } } while (0)
#define SB() __builtin_amdgcn_sched_barrier(0)
#define TAG() const u64 tag = (((u64)__builtin_amdgcn_readfirstlane(ep[1]) << 32) | (u64)__builtin_amdgcn_readfirstlane(ep[0])) + 1; \
  const u32 t0 = (u32)tag, t1 = (u32)(tag >> 32)

__global__ void __launch_bounds__(512, 2)
fused_decode_block(const u16* __restrict__ x, const u16* __restrict__ Kc, const u16* __restrict__ Vc, const u16* __restrict__ wq,
                   const u16* __restrict__ wo, const u16* __restrict__ wg, const u16* __restrict__ wu, const u16* __restrict__ wd,
                   u16* __restrict__ out, unsigned char* ws) {
  __shared__ __attribute__((aligned(16))) float lds[L_END];
  const int tid = threadIdx.x, lane = tid & 63;
  const int w = __builtin_amdgcn_readfirstlane(tid >> 6);
  const int b = blockIdx.x, g = b & 7, s = b >> 3;
  const __amdgpu_buffer_rsrc_t rs = __builtin_amdgcn_make_buffer_rsrc((void*)ws, 0, WS_END, 0x00020000);
  const v4u srd = {(u32)(uintptr_t)ws, (u32)((uintptr_t)ws >> 32) & 0xffffu, (u32)WS_END, 0x00020000u};
  u64 stamps[16];
#pragma unroll
  for (int i_ = 0; i_ < 16; ++i_) stamps[i_] = 0;
  STAMP(0);
  const v2u ep = __builtin_amdgcn_raw_buffer_load_b64(rs, WS_EPOCH, 0, AUX_SC1);
#define CODE_PF_USE() do { } while (0)

  if (s < NSP) {
    volatile LAS u32* lflag = (volatile LAS u32*)(&lds[L_FLAG]);
    if (tid == 0) { lflag[0] = 0u; lflag[1] = 0u; }
    __builtin_amdgcn_s_setprio(1);
    bar();
    const int g4 = lane >> 4, c = lane & 15;
    const int tile0 = (256 * s) / 7, nx = (256 * (s + 1)) / 7 - tile0 - 32;
    const bool five = w < nx;
    const int key0 = (tile0 + 4 * w + (five ? w : nx)) * 8;
    const __amdgpu_buffer_rsrc_t rk = __builtin_amdgcn_make_buffer_rsrc((void*)Kc, 0, 8 * SK * DH * 2, 0x00020000);
    const __amdgpu_buffer_rsrc_t rv = __builtin_amdgcn_make_buffer_rsrc((void*)Vc, 0, 8 * SK * DH * 2, 0x00020000);
    v4u kr[20], vr[20];
    {
      const int kb = ((g * SK + key0 + (c >> 1)) * DH + (c & 1) * 32 + g4 * 8) * 2;
      const int kb5 = five ? kb + 4 * 4096 : OOB;
#pragma unroll
      for (int t = 0; t < 4; ++t)
#pragma unroll
        for (int ks = 0; ks < 4; ++ks) kr[t * 4 + ks] = ldb(rk, kb + t * 4096 + ks * 128);
#pragma unroll
      for (int ks = 0; ks < 4; ++ks) kr[16 + ks] = ldb(rk, five ? kb5 + ks * 128 : OOB);
      SB();
      const int vb = ((g * SK + key0 + 2 * g4) * DH + 8 * c) * 2;
#pragma unroll
      for (int hf = 0; hf < 2; ++hf) {
#pragma unroll
        for (int j = 0; j < 8; ++j) vr[hf * 10 + j] = ldb(rv, vb + (8 * (j >> 1) + (j & 1)) * 512 + 256 * hf);
#pragma unroll
        for (int j = 0; j < 2; ++j) vr[hf * 10 + 8 + j] = ldb(rv, five ? vb + (32 + j) * 512 + 256 * hf : OOB);
        SB();
      }
    }
    const int oh = 2 * g + s / 14, cb = s % 14;
    const int sub = lane >> 3, gq = lane & 7;
    const __amdgpu_buffer_rsrc_t ro = __builtin_amdgcn_make_buffer_rsrc((void*)wo, 0, NQ * D * 2, 0x00020000);
    const int oo = ((oh * 256 + 32 * w + 4 * gq + 2 * (lane >> 5)) * D + 32 * cb + 8 * (sub & 3)) * 2;
    const v4u woa0 = ldb(ro, w >= 4 ? oo : OOB), woa1 = ldb(ro, w >= 4 ? oo + D * 2 : OOB);
    SB();
    STAMP(1);
    TAG();
    CODE_PF_USE();
    if (w == 0) {
      __builtin_amdgcn_s_setprio(3);
      v4u qg[2];
      int qoff[2];
#pragma unroll
      for (int i = 0; i < 2; ++i) { qoff[i] = WS_Q + (g * 128 + i * 64 + lane) * 16; qg[i] = ldg(rs, qoff[i]); }
      poll<2, 64>(srd, qoff, qg, tag);
#pragma unroll
      for (int i = 0; i < 2; ++i)
        *reinterpret_cast<v2u*>(&lds[L_QS + (i * 64 + lane) * 2]) = v2u{qg[i][0], qg[i][1]};
      asm volatile("s_waitcnt lgkmcnt(0)" ::: "memory");
      lflag[0] = 1u;
      __builtin_amdgcn_s_setprio(1);
    }
    for (u32 sp = 0; lflag[0] == 0u && sp < (SPIN_CAP << 4); ++sp) __builtin_amdgcn_s_sleep(1);
    asm volatile("" ::: "memory");
    STAMP(2);
    const int hd = (c >> 2) & 1;
    const u32 meta = ((c >> 1) & 1) ? 0xffffffffu : 0u, mvar = (c & 1) ? 0xffffffffu : 0u, mhi = (g4 >> 1) ? 0xffffffffu : 0u;
    float sc[10];
    {
      v4u qf[4];
#pragma unroll
      for (int ks = 0; ks < 4; ++ks) qf[ks] = *reinterpret_cast<const v4u*>(&lds[L_QS + hd * 128 + ks * 32 + ((c >> 1) & 1) * 16 + g4 * 4]);
#pragma unroll
      for (int t = 0; t < 5; ++t) {
        v4f d = {0.f, 0.f, 0.f, 0.f};
#pragma unroll
        for (int ks = 0; ks < 4; ++ks) d = mfma(kr[t * 4 + ks], qf[ks], d);
        const float sa = bsel(meta, d[1], d[0]), sb = bsel(meta, d[3], d[2]);
        sc[2 * t] = rbf(sa + dpp<X2>(sa)) * 0.0625f;
        sc[2 * t + 1] = rbf(sb + dpp<X2>(sb)) * 0.0625f;
      }
      if (!five) { sc[8] = -INFINITY; sc[9] = -INFINITY; }
    }
    float m = sc[0];
#pragma unroll
    for (int j = 1; j < 10; ++j) m = fmaxf(m, sc[j]);
    m = sw32_max(sw16_max(m));
    v4u pa;
    v4u pa5;
    {
      float p[10], l = 0.f;
#pragma unroll
      for (int j = 0; j < 10; ++j) { p[j] = __builtin_amdgcn_exp2f((sc[j] - m) * LOG2E); l += p[j]; }
      l = sw16_sum(l, l); l = sw32_sum(l, l);
      if ((lane & 0x3b) == 0) { lds[L_WMAX + w * 2 + hd] = m; lds[L_WSUM + w * 2 + hd] = l; }
#pragma unroll
      for (int j = 0; j < 10; ++j) { const float hi = rbf(p[j]); p[j] = bsel(mvar, p[j] - hi, hi); }
#pragma unroll
      for (int t = 0; t < 4; ++t) pa[t] = cvtpk(p[2 * t], p[2 * t + 1]);
      pa5[0] = cvtpk(p[8], p[9]); pa5[1] = 0u; pa5[2] = 0u; pa5[3] = 0u;
    }
    STAMP(3);
#pragma unroll
    for (int hf = 0; hf < 2; ++hf) {
      float xo[8];
#pragma unroll
      for (int d = 0; d < 8; ++d) {
        v4u bf;
        const u32 sel = (d & 1) ? 0x07060302u : 0x05040100u;
#pragma unroll
        for (int t = 0; t < 4; ++t) bf[t] = __builtin_amdgcn_perm(vr[hf * 10 + 2 * t + 1][d >> 1], vr[hf * 10 + 2 * t][d >> 1], sel);
        v4u b5;
        b5[0] = __builtin_amdgcn_perm(vr[hf * 10 + 9][d >> 1], vr[hf * 10 + 8][d >> 1], sel); b5[1] = 0u; b5[2] = 0u; b5[3] = 0u;
        v4f dd = mfma(pa, bf, v4f{0.f, 0.f, 0.f, 0.f});
        dd = mfma(pa5, b5, dd);
        xo[d] = dd[0] + dd[1];
      }
      v4f y;
#pragma unroll
      for (int e = 0; e < 4; ++e) y[e] = bsel(mhi, xo[4 + e], xo[e]);
      *reinterpret_cast<v4f*>(&lds[L_ORED + (w * 2 + (g4 & 1)) * 256 + 128 * hf + 8 * c + 4 * (g4 >> 1)]) = y;
    }
    STAMP(4);
    __builtin_amdgcn_s_setprio(0);
    asm volatile("s_waitcnt lgkmcnt(0)" ::: "memory");
    if (lane == 0) __hip_atomic_fetch_add((LAS u32*)(&lds[L_FLAG + 1]), 1u, __ATOMIC_RELAXED, __HIP_MEMORY_SCOPE_WORKGROUP);
    v4u wor[2];
    wor[0] = ldb(ro, w < 4 ? oo : OOB); wor[1] = ldb(ro, w < 4 ? oo + D * 2 : OOB);
    SB();
    if (w < 4) {
      v4u po[7], ml = {0u, 0u, 0u, 0u};
      int poff[7];
      const int mysp = w + 4 * (lane & 7);
      const bool mlwant = lane < 7 && mysp != s;
      const int mloff = WS_ML + (oh * 32 + mysp) * 128;
      mark(ml, !mlwant, tag);
      if (!mlwant) ml[0] = 0xff800000u;
#pragma unroll
      for (int i = 0; i < 7; ++i) {
        po[i] = v4u{0u, 0u, 0u, 0u};
        mark(po[i], w + 4 * i == s, tag);
        poff[i] = WS_PO + ((oh * 32 + w + 4 * i) * 64 + lane) * 16;
      }
      u64 mm = __ballot(gtag(ml) != tag);
      for (u32 sp = 0; sp < SPIN_CAP; ++sp) {
        const bool few = __builtin_popcountll(mm) <= 1;
        u64 pm[7], pany = 0;
#pragma unroll
        for (int i = 0; i < 7; ++i) { pm[i] = __ballot(gtag(po[i]) != tag); pany |= pm[i]; }
        if ((mm | pany) == 0) break;
        if (!few && sp) __builtin_amdgcn_s_sleep(8);
        asm volatile("s_mov_b64 exec, %2\n\tbuffer_load_dwordx4 %0, %1, %3, 0 offen sc1\n\ts_mov_b64 exec, -1"
                     : "+v"(ml) : "v"(mloff), "s"(mm), "s"(srd) : "memory");
#pragma unroll
        for (int i = 0; i < 7; ++i) {
          const u64 mk = (few || !((mm >> i) & 1)) ? pm[i] : 0ull;
          asm volatile("s_mov_b64 exec, %2\n\tbuffer_load_dwordx4 %0, %1, %3, 0 offen sc1\n\ts_mov_b64 exec, -1"
                       : "+v"(po[i]) : "v"(poff[i]), "s"(mk), "s"(srd) : "memory");
        }
        asm volatile("s_waitcnt vmcnt(0)" : "+v"(ml), "+v"(po[0]), "+v"(po[1]), "+v"(po[2]), "+v"(po[3]), "+v"(po[4]), "+v"(po[5]), "+v"(po[6]) :: "memory");
        mm = __ballot(gtag(ml) != tag);
      }
      STAMP(7);
      const float mi = __uint_as_float(ml[0]);
      float mx = fmaxf(mi, dpp<X1>(mi)); mx = fmaxf(mx, dpp<X2>(mx)); mx = fmaxf(mx, dpp<X7>(mx));
      const float wt = __builtin_amdgcn_exp2f((mi - mx) * LOG2E);
      float lw = wt * __uint_as_float(ml[1]);
      lw += dpp<X1>(lw); lw += dpp<X2>(lw); lw += dpp<X7>(lw);
      v4f o = {0.f, 0.f, 0.f, 0.f};
#pragma unroll
      for (int i = 0; i < 7; ++i) {
        const float f = __builtin_bit_cast(float, __builtin_amdgcn_readlane(__builtin_bit_cast(int, wt), i));
        o[0] = fmaf(f, bflo(po[i][0]), o[0]); o[1] = fmaf(f, bfhi(po[i][0]), o[1]);
        o[2] = fmaf(f, bflo(po[i][1]), o[2]); o[3] = fmaf(f, bfhi(po[i][1]), o[3]);
      }
      *reinterpret_cast<v4f*>(&lds[L_CMB + w * 256 + lane * 4]) = o;
      if (lane == 0) *reinterpret_cast<v2f*>(&lds[L_CML + w * 2]) = v2f{mx, lw};
    } else if (w < 6) {
      const int hh = w - 4;
      for (u32 sp = 0; lflag[1] < 8u && sp < (SPIN_CAP << 4); ++sp) { }
      asm volatile("" ::: "memory");
      STAMP(5);
      float mw[8], Mh;
#pragma unroll
      for (int ww = 0; ww < 8; ++ww) mw[ww] = lds[L_WMAX + ww * 2 + hh];
      Mh = fmaxf(fmaxf(fmaxf(mw[0], mw[1]), fmaxf(mw[2], mw[3])), fmaxf(fmaxf(mw[4], mw[5]), fmaxf(mw[6], mw[7])));
      v4f o = {0.f, 0.f, 0.f, 0.f}, o2 = {0.f, 0.f, 0.f, 0.f};
      float L = 0.f;
#pragma unroll
      for (int ww = 0; ww < 8; ++ww) {
        const float f = __builtin_amdgcn_exp2f((mw[ww] - Mh) * LOG2E);
        const v4f t = *reinterpret_cast<const v4f*>(&lds[L_ORED + (ww * 2 + hh) * 256 + lane * 4]);
        if (ww & 1) o2 += f * t; else o += f * t;
        L = fmaf(f, lds[L_WSUM + ww * 2 + hh], L);
      }
      o += o2;
      const u32 c0 = cvtpk(o[0], o[1]), c1 = cvtpk(o[2], o[3]);
      stg(rs, WS_PO + (((2 * g + hh) * 32 + s) * 64 + lane) * 16, c0, c1, t0, t1);
      if (lane == 0) stg(rs, WS_ML + ((2 * g + hh) * 32 + s) * 128, __float_as_uint(Mh), __float_as_uint(L), t0, t1);
      if (hh == (oh & 1)) {
        *reinterpret_cast<v4f*>(&lds[L_CMB + 4 * 256 + lane * 4]) = v4f{bflo(c0), bfhi(c0), bflo(c1), bfhi(c1)};
        if (lane == 0) *reinterpret_cast<v2f*>(&lds[L_CML + 8]) = v2f{Mh, L};
      }
      STAMP(6);
    }
    wor[0] |= woa0; wor[1] |= woa1;
    pairup(wor[0], wor[1]);
    asm volatile("" : "+v"(wor[0]), "+v"(wor[1]));
    bar();
    {
      v2f cm[5], co[5];
#pragma unroll
      for (int i = 0; i < 5; ++i) {
        cm[i] = *reinterpret_cast<const v2f*>(&lds[L_CML + i * 2]);
        co[i] = *reinterpret_cast<const v2f*>(&lds[L_CMB + i * 256 + 32 * w + 4 * gq + 2 * (lane >> 5)]);
      }
      const float Mx = fmaxf(fmaxf(fmaxf(cm[0][0], cm[1][0]), fmaxf(cm[2][0], cm[3][0])), cm[4][0]);
      float L = 0.f;
      v2f u = {0.f, 0.f};
#pragma unroll
      for (int i = 0; i < 5; ++i) {
        const float f = __builtin_amdgcn_exp2f((cm[i][0] - Mx) * LOG2E);
        L = fmaf(f, cm[i][1], L);
        u += f * co[i];
      }
      const float rl = rcp(L);
      const u32 n = cvtpk(u[0] * rl, u[1] * rl);
      float acc[8];
#pragma unroll
      for (int j = 0; j < 8; ++j) acc[j] = 0.f;
      fmap(acc, wor[0], wor[1], n);
      float r4[4], r2[2];
#pragma unroll
      for (int j = 0; j < 4; ++j) r4[j] = sw32_sum(acc[j], acc[j + 4]);
#pragma unroll
      for (int j = 0; j < 2; ++j) r2[j] = tr_step<X7>(r4[j], r4[j + 2], (lane & 4) != 0);
      float r1 = tr_step<X2>(r2[0], r2[1], (lane & 2) != 0);
      r1 += dpp<X1>(r1);
      if ((lane & 1) == 0) lds[L_S1 + w * 32 + (sub & 3) * 8 + (lane >> 5) * 4 + ((lane >> 2) & 1) * 2 + ((lane >> 1) & 1)] = r1;
    }
    bar();
    if (w == 0 && lane < 16) {
      v2f a[8];
#pragma unroll
      for (int ww = 0; ww < 8; ++ww) a[ww] = *reinterpret_cast<const v2f*>(&lds[L_S1 + ww * 32 + lane * 2]);
      const v2f t = ((a[0] + a[1]) + (a[2] + a[3])) + ((a[4] + a[5]) + (a[6] + a[7]));
      stg(rs, WS_PH + (oh * 224 + cb * 16 + lane) * 16, __float_as_uint(t[0]), __float_as_uint(t[1]), t0, t1);
    }
    STAMP(8);
    STAMP_FLUSH();
    return;
  }

  const int f = g * 4 + (s - NSP);
  const bool ffn = f < 28;
  const int r4 = lane >> 4;
  const v4u xv = *reinterpret_cast<const v4u*>(x + (lane < 56 ? lane : 0) * 8);
  u32 xq[7];
  v4u wa[14], wf[14], wdn[8];
  {
    const int row0 = 56 * w + 14 * r4;
#pragma unroll
    for (int i = 0; i < 7; ++i) xq[i] = *reinterpret_cast<const u32*>(x + row0 + 2 * i);
    const u16* p = wq + (size_t)row0 * NQ + 128 * f + (lane & 15) * 8;
#pragma unroll
    for (int i = 0; i < 14; ++i) wa[i] = ldnt(p + (size_t)i * NQ);
    SB();
    bar();
    SB();
    const int fc = ffn ? f : 0;
    const u16* pf = (((lane >> 3) & 1) ? wu : wg) + (size_t)row0 * FF + 64 * fc + (lane & 7) * 8;
    float xf[8];
#pragma unroll
    for (int j = 0; j < 4; ++j) { xf[2 * j] = bflo(xv[j]); xf[2 * j + 1] = bfhi(xv[j]); }
    float sm = 0.f;
#pragma unroll
    for (int j = 0; j < 8; ++j) sm += xf[j];
    sm = wave_sum(lane < 56 ? sm : 0.f);
    const float mean = sm * (1.0f / D);
    float sq = 0.f;
#pragma unroll
    for (int j = 0; j < 8; ++j) { const float d = xf[j] - mean; sq += d * d; }
    sq = wave_sum(lane < 56 ? sq : 0.f);
    const float mb = rbf(mean), den = rbf(sqrtf(rbf(rbf(sq * (1.0f / D)) + 1e-5f)));
    float acc[8];
#pragma unroll
    for (int j = 0; j < 8; ++j) acc[j] = 0.f;
#pragma unroll
    for (int i = 0; i < 7; ++i) {
      const u32 n = cvtpk(rbf(bflo(xq[i]) - mb) / den, rbf(bfhi(xq[i]) - mb) / den);
      pairup(wa[2 * i], wa[2 * i + 1]);
      fmap(acc, wa[2 * i], wa[2 * i + 1], n);
    }
    float a4[4];
#pragma unroll
    for (int j = 0; j < 4; ++j) a4[j] = sw32_sum(acc[j], acc[j + 4]);
    v2f a2;
#pragma unroll
    for (int j = 0; j < 2; ++j) a2[j] = sw16_sum(a4[j], a4[j + 2]);
    *reinterpret_cast<v2f*>(&lds[L_QR + w * 128 + (lane & 15) * 8 + (lane >> 5) * 4 + ((lane >> 4) & 1) * 2]) = a2;
    bar();
    TAG();
    CODE_PF_USE();
    if (w == 0 && lane < 32) {
      v4f qv = *reinterpret_cast<const v4f*>(&lds[L_QR + lane * 4]);
#pragma unroll
      for (int ww = 1; ww < 8; ++ww) qv += *reinterpret_cast<const v4f*>(&lds[L_QR + ww * 128 + lane * 4]);
      stg(rs, WS_Q + (f * 32 + lane) * 16, cvtpk(qv[0], qv[1]), cvtpk(qv[2], qv[3]), t0, t1);
    }
    STAMP(1);
    if (!ffn) { STAMP_FLUSH(); return; }
    SB();
#pragma unroll
    for (int i = 0; i < 14; ++i) wf[i] = ldnt(pf + (size_t)i * FF);
    SB();
  }
  const int l56 = lane < 56 ? lane : 0;
  {
    const u16* pd = wd + (size_t)(64 * f + 8 * w) * D + 8 * l56;
#pragma unroll
    for (int i = 0; i < 8; ++i) wdn[i] = ldnt(pd + i * D);
    SB();
  }
#pragma unroll
  for (int i = 0; i < 7; ++i) { pairup(wf[2 * i], wf[2 * i + 1]); asm volatile("" : "+v"(wf[2 * i]), "+v"(wf[2 * i + 1])); }
#pragma unroll
  for (int i = 0; i < 4; ++i) { pairup(wdn[2 * i], wdn[2 * i + 1]); asm volatile("" : "+v"(wdn[2 * i]), "+v"(wdn[2 * i + 1])); }
  {
    float acc[8];
#pragma unroll
    for (int j = 0; j < 8; ++j) acc[j] = 0.f;
#pragma unroll
    for (int i = 0; i < 7; ++i) fmap(acc, wf[2 * i], wf[2 * i + 1], 0x3f803f80u);
    float a4[4];
#pragma unroll
    for (int j = 0; j < 4; ++j) a4[j] = sw32_sum(acc[j], acc[j + 4]);
    v2f a2;
#pragma unroll
    for (int j = 0; j < 2; ++j) a2[j] = sw16_sum(a4[j], a4[j + 2]);
    *reinterpret_cast<v2f*>(&lds[L_QR + w * 128 + (lane & 15) * 8 + (lane >> 5) * 4 + ((lane >> 4) & 1) * 2]) = a2;
  }
  bar();
  const int u = 8 * w + (lane & 7);
  float SG = 0.f, SU = 0.f;
#pragma unroll
  for (int ww = 0; ww < 8; ++ww) { SG += lds[L_QR + ww * 128 + u]; SU += lds[L_QR + ww * 128 + 64 + u]; }
  asm volatile("" : "+v"(SG), "+v"(SU));
  TAG();
  STAMP(2);
  {
    const int hsub = lane >> 5;
    const bool act = (lane & 31) < 28;
    const int pp = act ? (lane & 31) : 0;
    int off[8];
    v4u gr[8];
    const u32 xp = *reinterpret_cast<const u32*>(x + 56 * w + 2 * pp);
#pragma unroll
    for (int i = 0; i < 8; ++i) { off[i] = act ? WS_PH + ((2 * i + hsub) * 224 + 28 * w + pp) * 16 : OOB; gr[i] = ldg(rs, off[i]); }
#pragma unroll
    for (int i = 0; i < 8; ++i) mark(gr[i], !act, tag);
    poll<8, 32>(srd, off, gr, tag);
    STAMP(9);
    float h0 = ((__uint_as_float(gr[0][0]) + __uint_as_float(gr[1][0])) + (__uint_as_float(gr[2][0]) + __uint_as_float(gr[3][0]))) +
               ((__uint_as_float(gr[4][0]) + __uint_as_float(gr[5][0])) + (__uint_as_float(gr[6][0]) + __uint_as_float(gr[7][0])));
    float h1 = ((__uint_as_float(gr[0][1]) + __uint_as_float(gr[1][1])) + (__uint_as_float(gr[2][1]) + __uint_as_float(gr[3][1]))) +
               ((__uint_as_float(gr[4][1]) + __uint_as_float(gr[5][1])) + (__uint_as_float(gr[6][1]) + __uint_as_float(gr[7][1])));
    h0 = sw32_sum(h0, h0); h1 = sw32_sum(h1, h1);
    const u32 a01 = cvtpk(h0, h1);
    const u32 h01 = cvtpk(bflo(xp) + bflo(a01), bfhi(xp) + bfhi(a01));
    h0 = bflo(h01); h1 = bfhi(h01);
    LAS u32* xn = (LAS u32*)(&lds[L_XN + w * 32]);
    if (lane < 28) xn[(lane / 7) * 8 + lane % 7] = h01;
    asm volatile("s_waitcnt lgkmcnt(0)" ::: "memory");
    const v4u na = *(const LAS v4u*)(xn + r4 * 8), nb = *(const LAS v4u*)(xn + r4 * 8 + 4);
    const float lm = half_sum(act ? h0 + h1 : 0.f) * (1.0f / 56.0f);
    const float d0 = h0 - lm, d1 = h1 - lm;
    const float m2 = half_sum(act ? d0 * d0 + d1 * d1 : 0.f);
    if (lane == 0) *reinterpret_cast<v2f*>(&lds[L_ST + w * 2]) = v2f{lm, m2};
    {
      const int cp = 28 * w + pp - 8 * f;
      if (act && hsub == 0 && cp >= 0 && cp < 8) *reinterpret_cast<v2f*>(&lds[L_HS + cp * 2]) = v2f{h0, h1};
    }
    float acc[8];
#pragma unroll
    for (int j = 0; j < 8; ++j) acc[j] = 0.f;
#pragma unroll
    for (int i = 0; i < 7; ++i) fmap(acc, wf[2 * i], wf[2 * i + 1], i < 4 ? na[i] : nb[i - 4]);
    float a4[4];
#pragma unroll
    for (int j = 0; j < 4; ++j) a4[j] = sw32_sum(acc[j], acc[j + 4]);
    v2f a2;
#pragma unroll
    for (int j = 0; j < 2; ++j) a2[j] = sw16_sum(a4[j], a4[j + 2]);
    *reinterpret_cast<v2f*>(&lds[L_GU + w * 128 + (lane & 15) * 8 + (lane >> 5) * 4 + ((lane >> 4) & 1) * 2]) = a2;
  }
  bar();
  {
    float gp[8], up[8], lms[8], m2s[8];
#pragma unroll
    for (int ww = 0; ww < 8; ++ww) {
      gp[ww] = lds[L_GU + ww * 128 + u]; up[ww] = lds[L_GU + ww * 128 + 64 + u];
      const v2f v = *reinterpret_cast<const v2f*>(&lds[L_ST + ww * 2]); lms[ww] = v[0]; m2s[ww] = v[1];
    }
    const float mean = (((lms[0] + lms[1]) + (lms[2] + lms[3])) + ((lms[4] + lms[5]) + (lms[6] + lms[7]))) * 0.125f;
#pragma unroll
    for (int ww = 0; ww < 8; ++ww) { const float d = lms[ww] - mean; m2s[ww] = fmaf(56.0f * d, d, m2s[ww]); }
    const float M2 = ((m2s[0] + m2s[1]) + (m2s[2] + m2s[3])) + ((m2s[4] + m2s[5]) + (m2s[6] + m2s[7]));
    const float mb = rbf(mean), rden = rcp(rbf(__builtin_amdgcn_sqrtf(rbf(rbf(M2 * (1.0f / D)) + 1e-5f))));
    float G = ((gp[0] + gp[1]) + (gp[2] + gp[3])) + ((gp[4] + gp[5]) + (gp[6] + gp[7]));
    float U = ((up[0] + up[1]) + (up[2] + up[3])) + ((up[4] + up[5]) + (up[6] + up[7]));
    G = rbf(fmaf(-mb, SG, G) * rden); U = rbf(fmaf(-mb, SU, U) * rden);
    const float z = 0.7978845608028654f * (G + 0.044715f * G * G * G);
    const float th = 1.0f - 2.0f * rcp(__builtin_amdgcn_exp2f(z * (2.0f * LOG2E)) + 1.0f);
    const float av = rbf(0.5f * G * (1.0f + th)) * U;
    const u32 pk = cvtpk(av, dpp<X1>(av));
    float acc[8];
#pragma unroll
    for (int j = 0; j < 8; ++j) acc[j] = 0.f;
#pragma unroll
    for (int i = 0; i < 4; ++i) fmap(acc, wdn[2 * i], wdn[2 * i + 1], __builtin_amdgcn_readlane(pk, 2 * i));
    if (lane < 56) {
      *reinterpret_cast<v4f*>(&lds[L_DN + w * 448 + lane * 8]) = v4f{acc[0], acc[1], acc[2], acc[3]};
      *reinterpret_cast<v4f*>(&lds[L_DN + w * 448 + lane * 8 + 4]) = v4f{acc[4], acc[5], acc[6], acc[7]};
    }
  }
  bar();
  if (tid < 224) {
    v2f a[8];
#pragma unroll
    for (int ww = 0; ww < 8; ++ww) a[ww] = *reinterpret_cast<const v2f*>(&lds[L_DN + ww * 448 + tid * 2]);
    const v2f t = ((a[0] + a[1]) + (a[2] + a[3])) + ((a[4] + a[5]) + (a[6] + a[7]));
    stg(rs, WS_PD + (f * 224 + tid) * 16, __float_as_uint(t[0]), __float_as_uint(t[1]), t0, t1);
  }
  STAMP(10);
  if (w == 7) {
    int off[4];
    v4u gr[4];
#pragma unroll
    for (int i = 0; i < 4; ++i) {
      off[i] = (i < 3 || lane < 32) ? WS_PD + (((lane >> 3) + 8 * i) * 224 + 8 * f + (lane & 7)) * 16 : OOB;
      gr[i] = ldg(rs, off[i]);
    }
    mark(gr[3], lane >= 32, tag);
    poll<4, 64>(srd, off, gr, tag);
    STAMP(11);
    float a0 = 0.f, a1 = 0.f;
#pragma unroll
    for (int i = 0; i < 4; ++i) { a0 += __uint_as_float(gr[i][0]); a1 += __uint_as_float(gr[i][1]); }
    float r = sw32_sum(a0, a1);
    r = sw16_sum(r, r); r += dpp<X8>(r);
    const float hv = lds[L_HS + (lane & 7) * 2 + (lane >> 5)];
    const u32 ob = cvtpk(hv + rbf(r), 0.f);
    if ((lane & 0x18) == 0) out[16 * f + (lane & 7) * 2 + (lane >> 5)] = (u16)ob;
    if (f == 0 && lane == 0) __builtin_amdgcn_raw_buffer_store_b64(v2u{t0, t1}, rs, WS_EPOCH, 0, AUX_SC1);
  }
  STAMP(12);
  STAMP_FLUSH();
  if (f == 0 && tid == 448) {
    u64 ts = __builtin_amdgcn_s_memrealtime(); while (__builtin_amdgcn_s_memrealtime() - ts < 3000) __builtin_amdgcn_s_sleep(10);
    u64 t00 = ~0ull;
    for (int i = 0; i < 2048; ++i) { u64 t = __hip_atomic_load((u64*)(ws + WS_STAMP + (i * 16) * 8), __ATOMIC_RELAXED, __HIP_MEMORY_SCOPE_AGENT); if (t < t00) t00 = t; }
    for (int cls = 0; cls < 3; ++cls)
      for (int k = 0; k < 13; ++k) { u64 mn = ~0ull, mx = 0;
        for (int i = 0; i < 2048; ++i) { const int sv = (i >> 6) >= 28, lt = (i & 7) >= 4; if ((cls == 2) != (sv != 0)) continue; if (cls < 2 && lt != cls) continue;
          u64 t = __hip_atomic_load((u64*)(ws + WS_STAMP + (i * 16 + k) * 8), __ATOMIC_RELAXED, __HIP_MEMORY_SCOPE_AGENT); if (t < t00 || t - t00 > 100000) continue; if (t < mn) mn = t; if (t > mx) mx = t; }
        if (mx) printf("cls%d st%d: %d - %d\n", cls, k, (int)(mn - t00), (int)(mx - t00)); }
  }
}

}

extern "C" void kernel_launch(void* const* d_in, const int* in_sizes, int n_in, void* d_out, int out_size, void* d_ws,
                              size_t ws_size, hipStream_t stream) {
  fused_decode_block<<<256, 512, 0, stream>>>((const u16*)d_in[0], (const u16*)d_in[1], (const u16*)d_in[2], (const u16*)d_in[3],
                                              (const u16*)d_in[4], (const u16*)d_in[6], (const u16*)d_in[7], (const u16*)d_in[8],
                                              (u16*)d_out, (unsigned char*)d_ws);
}
```

```cpp
#include <hip/hip_runtime.h>
#include <hip/hip_bf16.h>
#include <stdint.h>

typedef unsigned short u16;
typedef unsigned int u32;
typedef unsigned long long u64;
typedef u32 v4u __attribute__((ext_vector_type(4)));
typedef u32 v2u __attribute__((ext_vector_type(2)));
typedef float v4f __attribute__((ext_vector_type(4)));
typedef float v2f __attribute__((ext_vector_type(2)));
typedef __bf16 bf16x2 __attribute__((ext_vector_type(2)));
typedef __bf16 bf16x8 __attribute__((ext_vector_type(8)));
typedef short s16x4 __attribute__((ext_vector_type(4)));

#define LAS __attribute__((address_space(3)))
#define CODE_PF 0
#define CODE_KB 18
#define DIAG 1

namespace {

constexpr int D = 448, SK = 8192, DH = 256, FF = 1792, NQ = 4096, NSP = 28;
constexpr int WS_EPOCH = 0;
constexpr int WS_Q = 4096;
constexpr int WS_ML = 32768;
constexpr int WS_PO = 131072;
constexpr int WS_PH = WS_PO + 524288;
constexpr int WS_PD = WS_PH + 65536;
constexpr int WS_END = WS_PD + 28 * 3584;
constexpr int WS_STAMP = 1 << 20;
constexpr int AUX_SC1 = 16, AUX_NT = 2;
constexpr int OOB = 0x7ffffff0;
constexpr u32 SPIN_CAP = 1u << 20;
constexpr float LOG2E = 1.4426950408889634f;

constexpr int L_QS = 0;
constexpr int L_WMAX = 256;
constexpr int L_WSUM = 272;
constexpr int L_S1 = 288;
constexpr int L_FLAG = 544;
constexpr int L_CML = 548;
constexpr int L_ORED = 576;
constexpr int L_CMB = L_ORED + 4096;
constexpr int L_END = L_CMB + 5 * 256;
constexpr int L_QR = 0;
constexpr int L_ST = 1024;
constexpr int L_XN = 1040;
constexpr int L_HS = 1296;
constexpr int L_GU = 1312;
constexpr int L_DN = 2336;

__device__ __forceinline__ float bflo(u32 u) { return __uint_as_float(u << 16); }
__device__ __forceinline__ float bfhi(u32 u) { return __uint_as_float(u & 0xffff0000u); }
__device__ __forceinline__ u32 cvtpk(float lo, float hi) {
  v2f v = {lo, hi};
  return __builtin_bit_cast(u32, __builtin_convertvector(v, bf16x2));
}
__device__ __forceinline__ float rbf(float f) { return bflo(cvtpk(f, 0.f)); }
__device__ __forceinline__ float dot2(u32 w, u32 a, float acc) {
  return __builtin_amdgcn_fdot2_f32_bf16(__builtin_bit_cast(bf16x2, w), __builtin_bit_cast(bf16x2, a), acc, false);
}
__device__ __forceinline__ float bsel(u32 m, float a, float b) {
  return __uint_as_float((__float_as_uint(a) & m) | (__float_as_uint(b) & ~m));
}
template <int CTRL> __device__ __forceinline__ float dpp(float x) {
  return __builtin_bit_cast(float, __builtin_amdgcn_mov_dpp(__builtin_bit_cast(int, x), CTRL, 0xf, 0xf, true));
}
constexpr int X1 = 0xB1, X2 = 0x4E, X7 = 0x141, X8 = 0x128;
__device__ __forceinline__ float sw32_sum(float a, float b) {
  auto r = __builtin_amdgcn_permlane32_swap(__float_as_uint(a), __float_as_uint(b), false, false);
  return __uint_as_float(r[0]) + __uint_as_float(r[1]);
}
__device__ __forceinline__ float sw16_sum(float a, float b) {
  auto r = __builtin_amdgcn_permlane16_swap(__float_as_uint(a), __float_as_uint(b), false, false);
  return __uint_as_float(r[0]) + __uint_as_float(r[1]);
}
__device__ __forceinline__ float sw32_max(float a) {
  auto r = __builtin_amdgcn_permlane32_swap(__float_as_uint(a), __float_as_uint(a), false, false);
  return fmaxf(__uint_as_float(r[0]), __uint_as_float(r[1]));
}
__device__ __forceinline__ float sw16_max(float a) {
  auto r = __builtin_amdgcn_permlane16_swap(__float_as_uint(a), __float_as_uint(a), false, false);
  return fmaxf(__uint_as_float(r[0]), __uint_as_float(r[1]));
}
template <int CTRL> __device__ __forceinline__ float tr_step(float lo, float hi, bool sel) {
  const float t1 = lo + dpp<CTRL>(lo), t2 = hi + dpp<CTRL>(hi);
  return sel ? t2 : t1;
}
__device__ __forceinline__ float wave_sum(float x) {
  x += dpp<X1>(x); x += dpp<X2>(x); x += dpp<X7>(x); x += dpp<X8>(x);
  x = sw16_sum(x, x); return sw32_sum(x, x);
}
__device__ __forceinline__ float half_sum(float x) {
  x += dpp<X1>(x); x += dpp<X2>(x); x += dpp<X7>(x); x += dpp<X8>(x);
  return sw16_sum(x, x);
}
__device__ __forceinline__ v4u ldnt(const u16* p) { return __builtin_nontemporal_load(reinterpret_cast<const v4u*>(p)); }
__device__ __forceinline__ v4u ldb(__amdgpu_buffer_rsrc_t rs, int off) { return __builtin_amdgcn_raw_buffer_load_b128(rs, off, 0, AUX_NT); }
__device__ __forceinline__ v4u ldg(__amdgpu_buffer_rsrc_t rs, int off) { return __builtin_amdgcn_raw_buffer_load_b128(rs, off, 0, AUX_SC1); }
__device__ __forceinline__ void stg(__amdgpu_buffer_rsrc_t rs, int off, u32 a, u32 b, u32 t0, u32 t1) {
  v4u v = {a, b, t0, t1};
  __builtin_amdgcn_raw_buffer_store_b128(v, rs, off, 0, AUX_SC1);
}
__device__ __forceinline__ void bar() {
  asm volatile("s_waitcnt lgkmcnt(0)" ::: "memory");
  __builtin_amdgcn_s_barrier();
  asm volatile("" ::: "memory");
}
__device__ __forceinline__ v4f mfma(v4u a, v4u b, v4f c) {
  return __builtin_amdgcn_mfma_f32_16x16x32_bf16(__builtin_bit_cast(bf16x8, a), __builtin_bit_cast(bf16x8, b), c, 0, 0, 0);
}
__device__ __forceinline__ u32 plo(u32 r0, u32 r1) { return __builtin_amdgcn_perm(r1, r0, 0x05040100u); }
__device__ __forceinline__ u32 phi(u32 r0, u32 r1) { return __builtin_amdgcn_perm(r1, r0, 0x07060302u); }
__device__ __forceinline__ void pairup(v4u& a, v4u& b) {
  v4u x, y;
  x[0] = plo(a[0], b[0]); x[1] = phi(a[0], b[0]); x[2] = plo(a[1], b[1]); x[3] = phi(a[1], b[1]);
  y[0] = plo(a[2], b[2]); y[1] = phi(a[2], b[2]); y[2] = plo(a[3], b[3]); y[3] = phi(a[3], b[3]);
  a = x; b = y;
}
__device__ __forceinline__ void fmap(float (&acc)[8], v4u a, v4u b, u32 n) {
#pragma unroll
  for (int j = 0; j < 4; ++j) { acc[j] = dot2(a[j], n, acc[j]); acc[4 + j] = dot2(b[j], n, acc[4 + j]); }
}
__device__ __forceinline__ void fma8(float (&acc)[8], v4u wv, u32 alo, u32 ahi) {
#pragma unroll
  for (int j = 0; j < 4; ++j) { acc[2 * j] = dot2(wv[j], alo, acc[2 * j]); acc[2 * j + 1] = dot2(wv[j], ahi, acc[2 * j + 1]); }
}
__device__ __forceinline__ u64 gtag(const v4u& g) { return ((u64)g[3] << 32) | (u64)g[2]; }
__device__ __forceinline__ void mark(v4u& g, bool unused, u64 tag) {
  g[2] = unused ? (u32)tag : g[2]; g[3] = unused ? (u32)(tag >> 32) : g[3];
}
template <int N, int MANY> __device__ __forceinline__ u32 poll(v4u srd, const int (&off)[N], v4u (&gr)[N], u64 tag) {
  u32 spins = 0;
  u64 m[N], any = 0;
#pragma unroll
  for (int i = 0; i < N; ++i) { m[i] = __ballot(gtag(gr[i]) != tag); any |= m[i]; }
  if (__builtin_expect(any != 0, 0)) {
    do {
      if (MANY < 64 && __builtin_popcountll(any) > MANY) __builtin_amdgcn_s_sleep(8);
#pragma unroll
      for (int i = 0; i < N; ++i)
        asm volatile("s_mov_b64 exec, %2\n\tbuffer_load_dwordx4 %0, %1, %3, 0 offen sc1\n\ts_mov_b64 exec, -1"
                     : "+v"(gr[i]) : "v"(off[i]), "s"(m[i]), "s"(srd) : "memory");
#pragma unroll
      for (int i = 0; i < N; ++i) asm volatile("s_waitcnt vmcnt(%1)" : "+v"(gr[i]) : "i"(N - 1 - i) : "memory");
      any = 0;
#pragma unroll
      for (int i = 0; i < N; ++i) { m[i] = __ballot(gtag(gr[i]) != tag); any |= m[i]; }
    } while (any != 0 && ++spins < SPIN_CAP);
    ++spins;
  }
  return spins;
}
__device__ __forceinline__ float rcp(float x) { return __builtin_amdgcn_rcpf(x); }
#define STAMP(i) do { u64 t_; __builtin_amdgcn_sched_barrier(0); asm volatile("s_memrealtime %0\n\ts_waitcnt lgkmcnt(0)" : "=s"(t_) :: "memory"); __builtin_amdgcn_sched_barrier(0); stamps[i] = t_; } while (0)
#define STAMP_FLUSH() do { if (lane == 0) { _Pragma("unroll") for (int i_ = 0; i_ < 16; ++i_) __hip_atomic_store((u64*)(ws + WS_STAMP + ((b * 8 + w) * 16 + i_) * 8), stamps[i_], __ATOMIC_RELAXED, __HIP_MEMORY_SCOPE_AGENT); } } while (0)
#define SB() __builtin_amdgcn_sched_barrier(0)
#define TAG() const u64 tag = (((u64)__builtin_amdgcn_readfirstlane(ep[1]) << 32) | (u64)__builtin_amdgcn_readfirstlane(ep[0])) + 1; \
  const u32 t0 = (u32)tag, t1 = (u32)(tag >> 32)

__global__ void __launch_bounds__(512, 2)
fused_decode_block(const u16* __restrict__ x, const u16* __restrict__ Kc, const u16* __restrict__ Vc, const u16* __restrict__ wq,
                   const u16* __restrict__ wo, const u16* __restrict__ wg, const u16* __restrict__ wu, const u16* __restrict__ wd,
                   u16* __restrict__ out, unsigned char* ws) {
  __shared__ __attribute__((aligned(16))) float lds[L_END];
  const int tid = threadIdx.x, lane = tid & 63;
  const int w = __builtin_amdgcn_readfirstlane(tid >> 6);
  const int b = blockIdx.x, g = b & 7, s = b >> 3;
  const __amdgpu_buffer_rsrc_t rs = __builtin_amdgcn_make_buffer_rsrc((void*)ws, 0, WS_END, 0x00020000);
  const v4u srd = {(u32)(uintptr_t)ws, (u32)((uintptr_t)ws >> 32) & 0xffffu, (u32)WS_END, 0x00020000u};
  u64 stamps[16];
#pragma unroll
  for (int i_ = 0; i_ < 16; ++i_) stamps[i_] = 0;
  STAMP(0);
  const v2u ep = __builtin_amdgcn_raw_buffer_load_b64(rs, WS_EPOCH, 0, AUX_SC1);
#define CODE_PF_USE() do { } while (0)

  if (s < NSP) {
    volatile LAS u32* lflag = (volatile LAS u32*)(&lds[L_FLAG]);
    if (tid == 0) { lflag[0] = 0u; lflag[1] = 0u; }
    __builtin_amdgcn_s_setprio(1);
    bar();
    const int g4 = lane >> 4, c = lane & 15;
    const int tile0 = (256 * s) / 7, nx = (256 * (s + 1)) / 7 - tile0 - 32;
    const bool five = w < nx;
    const int key0 = (tile0 + 4 * w + (five ? w : nx)) * 8;
    const __amdgpu_buffer_rsrc_t rk = __builtin_amdgcn_make_buffer_rsrc((void*)Kc, 0, 8 * SK * DH * 2, 0x00020000);
    const __amdgpu_buffer_rsrc_t rv = __builtin_amdgcn_make_buffer_rsrc((void*)Vc, 0, 8 * SK * DH * 2, 0x00020000);
    v4u kr[20], vr[20];
    {
      const int kb = ((g * SK + key0 + (c >> 1)) * DH + (c & 1) * 32 + g4 * 8) * 2;
      const int kb5 = five ? kb + 4 * 4096 : OOB;
#pragma unroll
      for (int t = 0; t < 4; ++t)
#pragma unroll
        for (int ks = 0; ks < 4; ++ks) kr[t * 4 + ks] = ldb(rk, kb + t * 4096 + ks * 128);
#pragma unroll
      for (int ks = 0; ks < 4; ++ks) kr[16 + ks] = ldb(rk, five ? kb5 + ks * 128 : OOB);
      SB();
      const int vb = ((g * SK + key0 + 2 * g4) * DH + 8 * c) * 2;
#pragma unroll
      for (int hf = 0; hf < 2; ++hf) {
#pragma unroll
        for (int j = 0; j < 8; ++j) vr[hf * 10 + j] = ldb(rv, vb + (8 * (j >> 1) + (j & 1)) * 512 + 256 * hf);
#pragma unroll
        for (int j = 0; j < 2; ++j) vr[hf * 10 + 8 + j] = ldb(rv, five ? vb + (32 + j) * 512 + 256 * hf : OOB);
        SB();
      }
    }
    const int oh = 2 * g + s / 14, cb = s % 14;
    const int sub = lane >> 3, gq = lane & 7;
    const __amdgpu_buffer_rsrc_t ro = __builtin_amdgcn_make_buffer_rsrc((void*)wo, 0, NQ * D * 2, 0x00020000);
    const int oo = ((oh * 256 + 32 * w + 4 * gq + 2 * (lane >> 5)) * D + 32 * cb + 8 * (sub & 3)) * 2;
    const v4u woa0 = ldb(ro, w >= 4 ? oo : OOB), woa1 = ldb(ro, w >= 4 ? oo + D * 2 : OOB);
    SB();
    STAMP(1);
    TAG();
    CODE_PF_USE();
    if (w == 0) {
      __builtin_amdgcn_s_setprio(3);
      v4u qg[2];
      int qoff[2];
#pragma unroll
      for (int i = 0; i < 2; ++i) { qoff[i] = WS_Q + (g * 128 + i * 64 + lane) * 16; qg[i] = ldg(rs, qoff[i]); }
      poll<2, 64>(srd, qoff, qg, tag);
#pragma unroll
      for (int i = 0; i < 2; ++i)
        *reinterpret_cast<v2u*>(&lds[L_QS + (i * 64 + lane) * 2]) = v2u{qg[i][0], qg[i][1]};
      asm volatile("s_waitcnt lgkmcnt(0)" ::: "memory");
      lflag[0] = 1u;
      __builtin_amdgcn_s_setprio(1);
    }
    for (u32 sp = 0; lflag[0] == 0u && sp < (SPIN_CAP << 4); ++sp) __builtin_amdgcn_s_sleep(1);
    asm volatile("" ::: "memory");
    STAMP(2);
    const int hd = (c >> 2) & 1;
    const u32 meta = ((c >> 1) & 1) ? 0xffffffffu : 0u, mvar = (c & 1) ? 0xffffffffu : 0u, mhi = (g4 >> 1) ? 0xffffffffu : 0u;
    float sc[10];
    {
      v4u qf[4];
#pragma unroll
      for (int ks = 0; ks < 4; ++ks) qf[ks] = *reinterpret_cast<const v4u*>(&lds[L_QS + hd * 128 + ks * 32 + ((c >> 1) & 1) * 16 + g4 * 4]);
#pragma unroll
      for (int t = 0; t < 5; ++t) {
        v4f d = {0.f, 0.f, 0.f, 0.f};
#pragma unroll
        for (int ks = 0; ks < 4; ++ks) d = mfma(kr[t * 4 + ks], qf[ks], d);
        const float sa = bsel(meta, d[1], d[0]), sb = bsel(meta, d[3], d[2]);
        sc[2 * t] = rbf(sa + dpp<X2>(sa)) * 0.0625f;
        sc[2 * t + 1] = rbf(sb + dpp<X2>(sb)) * 0.0625f;
      }
      if (!five) { sc[8] = -INFINITY; sc[9] = -INFINITY; }
    }
    float m = sc[0];
#pragma unroll
    for (int j = 1; j < 10; ++j) m = fmaxf(m, sc[j]);
    m = sw32_max(sw16_max(m));
    v4u pa;
    v4u pa5;
    {
      float p[10], l = 0.f;
#pragma unroll
      for (int j = 0; j < 10; ++j) { p[j] = __builtin_amdgcn_exp2f((sc[j] - m) * LOG2E); l += p[j]; }
      l = sw16_sum(l, l); l = sw32_sum(l, l);
      if ((lane & 0x3b) == 0) { lds[L_WMAX + w * 2 + hd] = m; lds[L_WSUM + w * 2 + hd] = l; }
#pragma unroll
      for (int j = 0; j < 10; ++j) { const float hi = rbf(p[j]); p[j] = bsel(mvar, p[j] - hi, hi); }
#pragma unroll
      for (int t = 0; t < 4; ++t) pa[t] = cvtpk(p[2 * t], p[2 * t + 1]);
      pa5[0] = cvtpk(p[8], p[9]); pa5[1] = 0u; pa5[2] = 0u; pa5[3] = 0u;
    }
    STAMP(3);
#pragma unroll
    for (int hf = 0; hf < 2; ++hf) {
      float xo[8];
#pragma unroll
      for (int d = 0; d < 8; ++d) {
        v4u bf;
        const u32 sel = (d & 1) ? 0x07060302u : 0x05040100u;
#pragma unroll
        for (int t = 0; t < 4; ++t) bf[t] = __builtin_amdgcn_perm(vr[hf * 10 + 2 * t + 1][d >> 1], vr[hf * 10 + 2 * t][d >> 1], sel);
        v4u b5;
        b5[0] = __builtin_amdgcn_perm(vr[hf * 10 + 9][d >> 1], vr[hf * 10 + 8][d >> 1], sel); b5[1] = 0u; b5[2] = 0u; b5[3] = 0u;
        v4f dd = mfma(pa, bf, v4f{0.f, 0.f, 0.f, 0.f});
        dd = mfma(pa5, b5, dd);
        xo[d] = dd[0] + dd[1];
      }
      v4f y;
#pragma unroll
      for (int e = 0; e < 4; ++e) y[e] = bsel(mhi, xo[4 + e], xo[e]);
      *reinterpret_cast<v4f*>(&lds[L_ORED + (w * 2 + (g4 & 1)) * 256 + 128 * hf + 8 * c + 4 * (g4 >> 1)]) = y;
    }
    STAMP(4);
    __builtin_amdgcn_s_setprio(0);
    asm volatile("s_waitcnt lgkmcnt(0)" ::: "memory");
    if (lane == 0) __hip_atomic_fetch_add((LAS u32*)(&lds[L_FLAG + 1]), 1u, __ATOMIC_RELAXED, __HIP_MEMORY_SCOPE_WORKGROUP);
    v4u wor[2];
    wor[0] = ldb(ro, w < 4 ? oo : OOB); wor[1] = ldb(ro, w < 4 ? oo + D * 2 : OOB);
    SB();
    if (w < 4) {
      v4u po[7], ml = {0u, 0u, 0u, 0u};
      int poff[7];
      const int mysp = w + 4 * (lane & 7);
      const bool mlwant = lane < 7 && mysp != s;
      const int mloff = WS_ML + (oh * 32 + mysp) * 128;
      mark(ml, !mlwant, tag);
      if (!mlwant) ml[0] = 0xff800000u;
#pragma unroll
      for (int i = 0; i < 7; ++i) {
        po[i] = v4u{0u, 0u, 0u, 0u};
        mark(po[i], w + 4 * i == s, tag);
        poff[i] = WS_PO + ((oh * 32 + w + 4 * i) * 64 + lane) * 16;
      }
      u64 mm = __ballot(gtag(ml) != tag);
      for (u32 sp = 0; sp < SPIN_CAP; ++sp) {
        const bool few = __builtin_popcountll(mm) <= 1;
        u64 pm[7], pany = 0;
#pragma unroll
        for (int i = 0; i < 7; ++i) { pm[i] = __ballot(gtag(po[i]) != tag); pany |= pm[i]; }
        if ((mm | pany) == 0) break;
        if (!few && sp) __builtin_amdgcn_s_sleep(8);
        asm volatile("s_mov_b64 exec, %2\n\tbuffer_load_dwordx4 %0, %1, %3, 0 offen sc1\n\ts_mov_b64 exec, -1"
                     : "+v"(ml) : "v"(mloff), "s"(mm), "s"(srd) : "memory");
#pragma unroll
        for (int i = 0; i < 7; ++i) {
          const u64 mk = (few || !((mm >> i) & 1)) ? pm[i] : 0ull;
          asm volatile("s_mov_b64 exec, %2\n\tbuffer_load_dwordx4 %0, %1, %3, 0 offen sc1\n\ts_mov_b64 exec, -1"
                       : "+v"(po[i]) : "v"(poff[i]), "s"(mk), "s"(srd) : "memory");
        }
        asm volatile("s_waitcnt vmcnt(0)" : "+v"(ml), "+v"(po[0]), "+v"(po[1]), "+v"(po[2]), "+v"(po[3]), "+v"(po[4]), "+v"(po[5]), "+v"(po[6]) :: "memory");
        mm = __ballot(gtag(ml) != tag);
      }
      STAMP(7);
      const float mi = __uint_as_float(ml[0]);
      float mx = fmaxf(mi, dpp<X1>(mi)); mx = fmaxf(mx, dpp<X2>(mx)); mx = fmaxf(mx, dpp<X7>(mx));
      const float wt = __builtin_amdgcn_exp2f((mi - mx) * LOG2E);
      float lw = wt * __uint_as_float(ml[1]);
      lw += dpp<X1>(lw); lw += dpp<X2>(lw); lw += dpp<X7>(lw);
      v4f o = {0.f, 0.f, 0.f, 0.f};
#pragma unroll
      for (int i = 0; i < 7; ++i) {
        const float f = __builtin_bit_cast(float, __builtin_amdgcn_readlane(__builtin_bit_cast(int, wt), i));
        o[0] = fmaf(f, bflo(po[i][0]), o[0]); o[1] = fmaf(f, bfhi(po[i][0]), o[1]);
        o[2] = fmaf(f, bflo(po[i][1]), o[2]); o[3] = fmaf(f, bfhi(po[i][1]), o[3]);
      }
      *reinterpret_cast<v4f*>(&lds[L_CMB + w * 256 + lane * 4]) = o;
      if (lane == 0) *reinterpret_cast<v2f*>(&lds[L_CML + w * 2]) = v2f{mx, lw};
    } else if (w < 6) {
      const int hh = w - 4;
      for (u32 sp = 0; lflag[1] < 8u && sp < (SPIN_CAP << 4); ++sp) { }
      asm volatile("" ::: "memory");
      STAMP(5);
      float mw[8], Mh;
#pragma unroll
      for (int ww = 0; ww < 8; ++ww) mw[ww] = lds[L_WMAX + ww * 2 + hh];
      Mh = fmaxf(fmaxf(fmaxf(mw[0], mw[1]), fmaxf(mw[2], mw[3])), fmaxf(fmaxf(mw[4], mw[5]), fmaxf(mw[6], mw[7])));
      v4f o = {0.f, 0.f, 0.f, 0.f}, o2 = {0.f, 0.f, 0.f, 0.f};
      float L = 0.f;
#pragma unroll
      for (int ww = 0; ww < 8; ++ww) {
        const float f = __builtin_amdgcn_exp2f((mw[ww] - Mh) * LOG2E);
        const v4f t = *reinterpret_cast<const v4f*>(&lds[L_ORED + (ww * 2 + hh) * 256 + lane * 4]);
        if (ww & 1) o2 += f * t; else o += f * t;
        L = fmaf(f, lds[L_WSUM + ww * 2 + hh], L);
      }
      o += o2;
      const u32 c0 = cvtpk(o[0], o[1]), c1 = cvtpk(o[2], o[3]);
      stg(rs, WS_PO + (((2 * g + hh) * 32 + s) * 64 + lane) * 16, c0, c1, t0, t1);
      if (lane == 0) stg(rs, WS_ML + ((2 * g + hh) * 32 + s) * 128, __float_as_uint(Mh), __float_as_uint(L), t0, t1);
      if (hh == (oh & 1)) {
        *reinterpret_cast<v4f*>(&lds[L_CMB + 4 * 256 + lane * 4]) = v4f{bflo(c0), bfhi(c0), bflo(c1), bfhi(c1)};
        if (lane == 0) *reinterpret_cast<v2f*>(&lds[L_CML + 8]) = v2f{Mh, L};
      }
      STAMP(6);
    }
    wor[0] |= woa0; wor[1] |= woa1;
    pairup(wor[0], wor[1]);
    asm volatile("" : "+v"(wor[0]), "+v"(wor[1]));
    bar();
    {
      v2f cm[5], co[5];
#pragma unroll
      for (int i = 0; i < 5; ++i) {
        cm[i] = *reinterpret_cast<const v2f*>(&lds[L_CML + i * 2]);
        co[i] = *reinterpret_cast<const v2f*>(&lds[L_CMB + i * 256 + 32 * w + 4 * gq + 2 * (lane >> 5)]);
      }
      const float Mx = fmaxf(fmaxf(fmaxf(cm[0][0], cm[1][0]), fmaxf(cm[2][0], cm[3][0])), cm[4][0]);
      float L = 0.f;
      v2f u = {0.f, 0.f};
#pragma unroll
      for (int i = 0; i < 5; ++i) {
        const float f = __builtin_amdgcn_exp2f((cm[i][0] - Mx) * LOG2E);
        L = fmaf(f, cm[i][1], L);
        u += f * co[i];
      }
      const float rl = rcp(L);
      const u32 n = cvtpk(u[0] * rl, u[1] * rl);
      float acc[8];
#pragma unroll
      for (int j = 0; j < 8; ++j) acc[j] = 0.f;
      fmap(acc, wor[0], wor[1], n);
      float r4[4], r2[2];
#pragma unroll
      for (int j = 0; j < 4; ++j) r4[j] = sw32_sum(acc[j], acc[j + 4]);
#pragma unroll
      for (int j = 0; j < 2; ++j) r2[j] = tr_step<X7>(r4[j], r4[j + 2], (lane & 4) != 0);
      float r1 = tr_step<X2>(r2[0], r2[1], (lane & 2) != 0);
      r1 += dpp<X1>(r1);
      if ((lane & 1) == 0) lds[L_S1 + w * 32 + (sub & 3) * 8 + (lane >> 5) * 4 + ((lane >> 2) & 1) * 2 + ((lane >> 1) & 1)] = r1;
    }
    bar();
    if (w == 0 && lane < 16) {
      v2f a[8];
#pragma unroll
      for (int ww = 0; ww < 8; ++ww) a[ww] = *reinterpret_cast<const v2f*>(&lds[L_S1 + ww * 32 + lane * 2]);
      const v2f t = ((a[0] + a[1]) + (a[2] + a[3])) + ((a[4] + a[5]) + (a[6] + a[7]));
      stg(rs, WS_PH + (oh * 224 + cb * 16 + lane) * 16, __float_as_uint(t[0]), __float_as_uint(t[1]), t0, t1);
    }
    STAMP(8);
    STAMP_FLUSH();
    return;
  }

  const int f = g * 4 + (s - NSP);
  const bool ffn = f < 28;
  const int r4 = lane >> 4;
  const v4u xv = *reinterpret_cast<const v4u*>(x + (lane < 56 ? lane : 0) * 8);
  u32 xq[7];
  v4u wa[14], wf[14], wdn[8];
  {
    const int row0 = 56 * w + 14 * r4;
#pragma unroll
    for (int i = 0; i < 7; ++i) xq[i] = *reinterpret_cast<const u32*>(x + row0 + 2 * i);
    const u16* p = wq + (size_t)row0 * NQ + 128 * f + (lane & 15) * 8;
#pragma unroll
    for (int i = 0; i < 14; ++i) wa[i] = ldnt(p + (size_t)i * NQ);
    SB();
    bar();
    SB();
    const int fc = ffn ? f : 0;
    const u16* pf = (((lane >> 3) & 1) ? wu : wg) + (size_t)row0 * FF + 64 * fc + (lane & 7) * 8;
    float xf[8];
#pragma unroll
    for (int j = 0; j < 4; ++j) { xf[2 * j] = bflo(xv[j]); xf[2 * j + 1] = bfhi(xv[j]); }
    float sm = 0.f;
#pragma unroll
    for (int j = 0; j < 8; ++j) sm += xf[j];
    sm = wave_sum(lane < 56 ? sm : 0.f);
    const float mean = sm * (1.0f / D);
    float sq = 0.f;
#pragma unroll
    for (int j = 0; j < 8; ++j) { const float d = xf[j] - mean; sq += d * d; }
    sq = wave_sum(lane < 56 ? sq : 0.f);
    const float mb = rbf(mean), den = rbf(sqrtf(rbf(rbf(sq * (1.0f / D)) + 1e-5f)));
    float acc[8];
#pragma unroll
    for (int j = 0; j < 8; ++j) acc[j] = 0.f;
#pragma unroll
    for (int i = 0; i < 7; ++i) {
      const u32 n = cvtpk(rbf(bflo(xq[i]) - mb) / den, rbf(bfhi(xq[i]) - mb) / den);
      pairup(wa[2 * i], wa[2 * i + 1]);
      fmap(acc, wa[2 * i], wa[2 * i + 1], n);
    }
    float a4[4];
#pragma unroll
    for (int j = 0; j < 4; ++j) a4[j] = sw32_sum(acc[j], acc[j + 4]);
    v2f a2;
#pragma unroll
    for (int j = 0; j < 2; ++j) a2[j] = sw16_sum(a4[j], a4[j + 2]);
    *reinterpret_cast<v2f*>(&lds[L_QR + w * 128 + (lane & 15) * 8 + (lane >> 5) * 4 + ((lane >> 4) & 1) * 2]) = a2;
    bar();
    TAG();
    CODE_PF_USE();
    if (w == 0 && lane < 32) {
      v4f qv = *reinterpret_cast<const v4f*>(&lds[L_QR + lane * 4]);
#pragma unroll
      for (int ww = 1; ww < 8; ++ww) qv += *reinterpret_cast<const v4f*>(&lds[L_QR + ww * 128 + lane * 4]);
      stg(rs, WS_Q + (f * 32 + lane) * 16, cvtpk(qv[0], qv[1]), cvtpk(qv[2], qv[3]), t0, t1);
    }
    STAMP(1);
    if (!ffn) { STAMP_FLUSH(); return; }
    SB();
#pragma unroll
    for (int i = 0; i < 14; ++i) wf[i] = ldnt(pf + (size_t)i * FF);
    SB();
  }
  const int l56 = lane < 56 ? lane : 0;
  {
    const u16* pd = wd + (size_t)(64 * f + 8 * w) * D + 8 * l56;
#pragma unroll
    for (int i = 0; i < 8; ++i) wdn[i] = ldnt(pd + i * D);
    SB();
  }
#pragma unroll
  for (int i = 0; i < 7; ++i) { pairup(wf[2 * i], wf[2 * i + 1]); asm volatile("" : "+v"(wf[2 * i]), "+v"(wf[2 * i + 1])); }
#pragma unroll
  for (int i = 0; i < 4; ++i) { pairup(wdn[2 * i], wdn[2 * i + 1]); asm volatile("" : "+v"(wdn[2 * i]), "+v"(wdn[2 * i + 1])); }
  {
    float acc[8];
#pragma unroll
    for (int j = 0; j < 8; ++j) acc[j] = 0.f;
#pragma unroll
    for (int i = 0; i < 7; ++i) fmap(acc, wf[2 * i], wf[2 * i + 1], 0x3f803f80u);
    float a4[4];
#pragma unroll
    for (int j = 0; j < 4; ++j) a4[j] = sw32_sum(acc[j], acc[j + 4]);
    v2f a2;
#pragma unroll
    for (int j = 0; j < 2; ++j) a2[j] = sw16_sum(a4[j], a4[j + 2]);
    *reinterpret_cast<v2f*>(&lds[L_QR + w * 128 + (lane & 15) * 8 + (lane >> 5) * 4 + ((lane >> 4) & 1) * 2]) = a2;
  }
  bar();
  const int u = 8 * w + (lane & 7);
  float SG = 0.f, SU = 0.f;
#pragma unroll
  for (int ww = 0; ww < 8; ++ww) { SG += lds[L_QR + ww * 128 + u]; SU += lds[L_QR + ww * 128 + 64 + u]; }
  asm volatile("" : "+v"(SG), "+v"(SU));
  TAG();
  STAMP(2);
  {
    const int hsub = lane >> 5;
    const bool act = (lane & 31) < 28;
    const int pp = act ? (lane & 31) : 0;
    int off[8];
    v4u gr[8];
    const u32 xp = *reinterpret_cast<const u32*>(x + 56 * w + 2 * pp);
#pragma unroll
    for (int i = 0; i < 8; ++i) { off[i] = act ? WS_PH + ((2 * i + hsub) * 224 + 28 * w + pp) * 16 : OOB; gr[i] = ldg(rs, off[i]); }
#pragma unroll
    for (int i = 0; i < 8; ++i) mark(gr[i], !act, tag);
    poll<8, 32>(srd, off, gr, tag);
    STAMP(9);
    float h0 = ((__uint_as_float(gr[0][0]) + __uint_as_float(gr[1][0])) + (__uint_as_float(gr[2][0]) + __uint_as_float(gr[3][0]))) +
               ((__uint_as_float(gr[4][0]) + __uint_as_float(gr[5][0])) + (__uint_as_float(gr[6][0]) + __uint_as_float(gr[7][0])));
    float h1 = ((__uint_as_float(gr[0][1]) + __uint_as_float(gr[1][1])) + (__uint_as_float(gr[2][1]) + __uint_as_float(gr[3][1]))) +
               ((__uint_as_float(gr[4][1]) + __uint_as_float(gr[5][1])) + (__uint_as_float(gr[6][1]) + __uint_as_float(gr[7][1])));
    h0 = sw32_sum(h0, h0); h1 = sw32_sum(h1, h1);
    const u32 a01 = cvtpk(h0, h1);
    const u32 h01 = cvtpk(bflo(xp) + bflo(a01), bfhi(xp) + bfhi(a01));
    h0 = bflo(h01); h1 = bfhi(h01);
    LAS u32* xn = (LAS u32*)(&lds[L_XN + w * 32]);
    if (lane < 28) xn[(lane / 7) * 8 + lane % 7] = h01;
    asm volatile("s_waitcnt lgkmcnt(0)" ::: "memory");
    const v4u na = *(const LAS v4u*)(xn + r4 * 8), nb = *(const LAS v4u*)(xn + r4 * 8 + 4);
    const float lm = half_sum(act ? h0 + h1 : 0.f) * (1.0f / 56.0f);
    const float d0 = h0 - lm, d1 = h1 - lm;
    const float m2 = half_sum(act ? d0 * d0 + d1 * d1 : 0.f);
    if (lane == 0) *reinterpret_cast<v2f*>(&lds[L_ST + w * 2]) = v2f{lm, m2};
    {
      const int cp = 28 * w + pp - 8 * f;
      if (act && hsub == 0 && cp >= 0 && cp < 8) *reinterpret_cast<v2f*>(&lds[L_HS + cp * 2]) = v2f{h0, h1};
    }
    float acc[8];
#pragma unroll
    for (int j = 0; j < 8; ++j) acc[j] = 0.f;
#pragma unroll
    for (int i = 0; i < 7; ++i) fmap(acc, wf[2 * i], wf[2 * i + 1], i < 4 ? na[i] : nb[i - 4]);
    float a4[4];
#pragma unroll
    for (int j = 0; j < 4; ++j) a4[j] = sw32_sum(acc[j], acc[j + 4]);
    v2f a2;
#pragma unroll
    for (int j = 0; j < 2; ++j) a2[j] = sw16_sum(a4[j], a4[j + 2]);
    *reinterpret_cast<v2f*>(&lds[L_GU + w * 128 + (lane & 15) * 8 + (lane >> 5) * 4 + ((lane >> 4) & 1) * 2]) = a2;
  }
  bar();
  {
    float gp[8], up[8], lms[8], m2s[8];
#pragma unroll
    for (int ww = 0; ww < 8; ++ww) {
      gp[ww] = lds[L_GU + ww * 128 + u]; up[ww] = lds[L_GU + ww * 128 + 64 + u];
      const v2f v = *reinterpret_cast<const v2f*>(&lds[L_ST + ww * 2]); lms[ww] = v[0]; m2s[ww] = v[1];
    }
    const float mean = (((lms[0] + lms[1]) + (lms[2] + lms[3])) + ((lms[4] + lms[5]) + (lms[6] + lms[7]))) * 0.125f;
#pragma unroll
    for (int ww = 0; ww < 8; ++ww) { const float d = lms[ww] - mean; m2s[ww] = fmaf(56.0f * d, d, m2s[ww]); }
    const float M2 = ((m2s[0] + m2s[1]) + (m2s[2] + m2s[3])) + ((m2s[4] + m2s[5]) + (m2s[6] + m2s[7]));
    const float mb = rbf(mean), rden = rcp(rbf(__builtin_amdgcn_sqrtf(rbf(rbf(M2 * (1.0f / D)) + 1e-5f))));
    float G = ((gp[0] + gp[1]) + (gp[2] + gp[3])) + ((gp[4] + gp[5]) + (gp[6] + gp[7]));
    float U = ((up[0] + up[1]) + (up[2] + up[3])) + ((up[4] + up[5]) + (up[6] + up[7]));
    G = rbf(fmaf(-mb, SG, G) * rden); U = rbf(fmaf(-mb, SU, U) * rden);
    const float z = 0.7978845608028654f * (G + 0.044715f * G * G * G);
    const float th = 1.0f - 2.0f * rcp(__builtin_amdgcn_exp2f(z * (2.0f * LOG2E)) + 1.0f);
    const float av = rbf(0.5f * G * (1.0f + th)) * U;
    const u32 pk = cvtpk(av, dpp<X1>(av));
    float acc[8];
#pragma unroll
    for (int j = 0; j < 8; ++j) acc[j] = 0.f;
#pragma unroll
    for (int i = 0; i < 4; ++i) fmap(acc, wdn[2 * i], wdn[2 * i + 1], __builtin_amdgcn_readlane(pk, 2 * i));
    if (lane < 56) {
      *reinterpret_cast<v4f*>(&lds[L_DN + w * 448 + lane * 8]) = v4f{acc[0], acc[1], acc[2], acc[3]};
      *reinterpret_cast<v4f*>(&lds[L_DN + w * 448 + lane * 8 + 4]) = v4f{acc[4], acc[5], acc[6], acc[7]};
    }
  }
  bar();
  if (tid < 224) {
    v2f a[8];
#pragma unroll
    for (int ww = 0; ww < 8; ++ww) a[ww] = *reinterpret_cast<const v2f*>(&lds[L_DN + ww * 448 + tid * 2]);
    const v2f t = ((a[0] + a[1]) + (a[2] + a[3])) + ((a[4] + a[5]) + (a[6] + a[7]));
    stg(rs, WS_PD + (f * 224 + tid) * 16, __float_as_uint(t[0]), __float_as_uint(t[1]), t0, t1);
  }
  STAMP(10);
  if (w == 7) {
    int off[4];
    v4u gr[4];
#pragma unroll
    for (int i = 0; i < 4; ++i) {
      off[i] = (i < 3 || lane < 32) ? WS_PD + (((lane >> 3) + 8 * i) * 224 + 8 * f + (lane & 7)) * 16 : OOB;
      gr[i] = ldg(rs, off[i]);
    }
    mark(gr[3], lane >= 32, tag);
    poll<4, 64>(srd, off, gr, tag);
    STAMP(11);
    float a0 = 0.f, a1 = 0.f;
#pragma unroll
    for (int i = 0; i < 4; ++i) { a0 += __uint_as_float(gr[i][0]); a1 += __uint_as_float(gr[i][1]); }
    float r = sw32_sum(a0, a1);
    r = sw16_sum(r, r); r += dpp<X8>(r);
    const float hv = lds[L_HS + (lane & 7) * 2 + (lane >> 5)];
    const u32 ob = cvtpk(hv + rbf(r), 0.f);
    if ((lane & 0x18) == 0) out[16 * f + (lane & 7) * 2 + (lane >> 5)] = (u16)ob;
    if (f == 0 && lane == 0) __builtin_amdgcn_raw_buffer_store_b64(v2u{t0, t1}, rs, WS_EPOCH, 0, AUX_SC1);
  }
  STAMP(12);
  STAMP_FLUSH();
  if (f == 0 && w == 7) {
    u64 ts = __builtin_amdgcn_s_memrealtime(); while (__builtin_amdgcn_s_memrealtime() - ts < 500) __builtin_amdgcn_s_sleep(10);
    u64 mn = ~0ull, mx = 0;
    const u64 ref = stamps[0];
    for (int i = lane; i < 2048; i += 64) {
      u64 t = __hip_atomic_load((u64*)(ws + WS_STAMP + (i * 16) * 8), __ATOMIC_RELAXED, __HIP_MEMORY_SCOPE_AGENT); if (t < mn) mn = t;
      u64 v = __hip_atomic_load((u64*)(ws + WS_STAMP + (i * 16 + 0) * 8), __ATOMIC_RELAXED, __HIP_MEMORY_SCOPE_AGENT); if (v + 1000 > ref && v < ref + 100000 && v > mx) mx = v; }
    for (int o = 32; o > 0; o >>= 1) { u64 a = __shfl_xor(mn, o), c2 = __shfl_xor(mx, o); mn = a < mn ? a : mn; mx = c2 > mx ? c2 : mx; }
    const u64 span = mx - mn;
    ts = __builtin_amdgcn_s_memrealtime(); while (__builtin_amdgcn_s_memrealtime() - ts < span * 50) __builtin_amdgcn_s_sleep(10);
  }
}

}

extern "C" void kernel_launch(void* const* d_in, const int* in_sizes, int n_in, void* d_out, int out_size, void* d_ws,
                              size_t ws_size, hipStream_t stream) {
  fused_decode_block<<<256, 512, 0, stream>>>((const u16*)d_in[0], (const u16*)d_in[1], (const u16*)d_in[2], (const u16*)d_in[3],
                                              (const u16*)d_in[4], (const u16*)d_in[6], (const u16*)d_in[7], (const u16*)d_in[8],
                                              (u16*)d_out, (unsigned char*)d_ws);
}
```

```cpp
#include <hip/hip_runtime.h>
#include <hip/hip_bf16.h>
#include <stdint.h>

typedef unsigned short u16;
typedef unsigned int u32;
typedef unsigned long long u64;
typedef u32 v4u __attribute__((ext_vector_type(4)));
typedef u32 v2u __attribute__((ext_vector_type(2)));
typedef float v4f __attribute__((ext_vector_type(4)));
typedef float v2f __attribute__((ext_vector_type(2)));
typedef __bf16 bf16x2 __attribute__((ext_vector_type(2)));
typedef __bf16 bf16x8 __attribute__((ext_vector_type(8)));
typedef short s16x4 __attribute__((ext_vector_type(4)));

#define LAS __attribute__((address_space(3)))
#define CODE_PF 0
#define CODE_KB 18
#define DEFER 2
#define PRETOUCH 1
#define DIAG 0

namespace {

constexpr int D = 448, SK = 8192, DH = 256, FF = 1792, NQ = 4096, NSP = 28;
constexpr int WS_EPOCH = 0;
constexpr int WS_Q = 4096;
constexpr int WS_ML = 32768;
constexpr int WS_PO = 131072;
constexpr int WS_PH = WS_PO + 524288;
constexpr int WS_PD = WS_PH + 65536;
constexpr int WS_END = WS_PD + 28 * 3584;
constexpr int WS_STAMP = 1 << 20;
constexpr int AUX_SC1 = 16, AUX_NT = 2;
constexpr int OOB = 0x7ffffff0;
constexpr u32 SPIN_CAP = 1u << 20;
constexpr float LOG2E = 1.4426950408889634f;

constexpr int L_QS = 0;
constexpr int L_WMAX = 256;
constexpr int L_WSUM = 272;
constexpr int L_S1 = 288;
constexpr int L_FLAG = 544;
constexpr int L_CML = 548;
constexpr int L_ORED = 576;
constexpr int L_CMB = L_ORED + 4096;
constexpr int L_END = L_CMB + 5 * 256;
constexpr int L_QR = 0;
constexpr int L_ST = 1024;
constexpr int L_XN = 1040;
constexpr int L_HS = 1296;
constexpr int L_GU = 1312;
constexpr int L_DN = 2336;
constexpr int L_FN = 0;
constexpr int L_FC = 5920;

__device__ __forceinline__ float bflo(u32 u) { return __uint_as_float(u << 16); }
__device__ __forceinline__ float bfhi(u32 u) { return __uint_as_float(u & 0xffff0000u); }
__device__ __forceinline__ u32 cvtpk(float lo, float hi) {
  v2f v = {lo, hi};
  return __builtin_bit_cast(u32, __builtin_convertvector(v, bf16x2));
}
__device__ __forceinline__ float rbf(float f) { return bflo(cvtpk(f, 0.f)); }
__device__ __forceinline__ float dot2(u32 w, u32 a, float acc) {
  return __builtin_amdgcn_fdot2_f32_bf16(__builtin_bit_cast(bf16x2, w), __builtin_bit_cast(bf16x2, a), acc, false);
}
__device__ __forceinline__ float bsel(u32 m, float a, float b) {
  return __uint_as_float((__float_as_uint(a) & m) | (__float_as_uint(b) & ~m));
}
template <int CTRL> __device__ __forceinline__ float dpp(float x) {
  return __builtin_bit_cast(float, __builtin_amdgcn_mov_dpp(__builtin_bit_cast(int, x), CTRL, 0xf, 0xf, true));
}
constexpr int X1 = 0xB1, X2 = 0x4E, X7 = 0x141, X8 = 0x128;
__device__ __forceinline__ float sw32_sum(float a, float b) {
  auto r = __builtin_amdgcn_permlane32_swap(__float_as_uint(a), __float_as_uint(b), false, false);
  return __uint_as_float(r[0]) + __uint_as_float(r[1]);
}
__device__ __forceinline__ float sw16_sum(float a, float b) {
  auto r = __builtin_amdgcn_permlane16_swap(__float_as_uint(a), __float_as_uint(b), false, false);
  return __uint_as_float(r[0]) + __uint_as_float(r[1]);
}
__device__ __forceinline__ float sw32_max(float a) {
  auto r = __builtin_amdgcn_permlane32_swap(__float_as_uint(a), __float_as_uint(a), false, false);
  return fmaxf(__uint_as_float(r[0]), __uint_as_float(r[1]));
}
__device__ __forceinline__ float sw16_max(float a) {
  auto r = __builtin_amdgcn_permlane16_swap(__float_as_uint(a), __float_as_uint(a), false, false);
  return fmaxf(__uint_as_float(r[0]), __uint_as_float(r[1]));
}
template <int CTRL> __device__ __forceinline__ float tr_step(float lo, float hi, bool sel) {
  const float t1 = lo + dpp<CTRL>(lo), t2 = hi + dpp<CTRL>(hi);
  return sel ? t2 : t1;
}
__device__ __forceinline__ float wave_sum(float x) {
  x += dpp<X1>(x); x += dpp<X2>(x); x += dpp<X7>(x); x += dpp<X8>(x);
  x = sw16_sum(x, x); return sw32_sum(x, x);
}
__device__ __forceinline__ float half_sum(float x) {
  x += dpp<X1>(x); x += dpp<X2>(x); x += dpp<X7>(x); x += dpp<X8>(x);
  return sw16_sum(x, x);
}
__device__ __forceinline__ v4u ldnt(const u16* p) { return __builtin_nontemporal_load(reinterpret_cast<const v4u*>(p)); }
__device__ __forceinline__ v4u ldb(__amdgpu_buffer_rsrc_t rs, int off) { return __builtin_amdgcn_raw_buffer_load_b128(rs, off, 0, AUX_NT); }
__device__ __forceinline__ v4u ldg(__amdgpu_buffer_rsrc_t rs, int off) { return __builtin_amdgcn_raw_buffer_load_b128(rs, off, 0, AUX_SC1); }
__device__ __forceinline__ void stg(__amdgpu_buffer_rsrc_t rs, int off, u32 a, u32 b, u32 t0, u32 t1) {
  v4u v = {a, b, t0, t1};
  __builtin_amdgcn_raw_buffer_store_b128(v, rs, off, 0, AUX_SC1);
}
__device__ __forceinline__ void bar() {
  asm volatile("s_waitcnt lgkmcnt(0)" ::: "memory");
  __builtin_amdgcn_s_barrier();
  asm volatile("" ::: "memory");
}
__device__ __forceinline__ v4f mfma(v4u a, v4u b, v4f c) {
  return __builtin_amdgcn_mfma_f32_16x16x32_bf16(__builtin_bit_cast(bf16x8, a), __builtin_bit_cast(bf16x8, b), c, 0, 0, 0);
}
__device__ __forceinline__ v4f mfma16(v2u a, v2u b, v4f c) {
  return __builtin_amdgcn_mfma_f32_16x16x16bf16_1k(__builtin_bit_cast(s16x4, a), __builtin_bit_cast(s16x4, b), c, 0, 0, 0);
}
__device__ __forceinline__ u32 plo(u32 r0, u32 r1) { return __builtin_amdgcn_perm(r1, r0, 0x05040100u); }
__device__ __forceinline__ u32 phi(u32 r0, u32 r1) { return __builtin_amdgcn_perm(r1, r0, 0x07060302u); }
__device__ __forceinline__ void pairup(v4u& a, v4u& b) {
  v4u x, y;
  x[0] = plo(a[0], b[0]); x[1] = phi(a[0], b[0]); x[2] = plo(a[1], b[1]); x[3] = phi(a[1], b[1]);
  y[0] = plo(a[2], b[2]); y[1] = phi(a[2], b[2]); y[2] = plo(a[3], b[3]); y[3] = phi(a[3], b[3]);
  a = x; b = y;
}
__device__ __forceinline__ void fmap(float (&acc)[8], v4u a, v4u b, u32 n) {
#pragma unroll
  for (int j = 0; j < 4; ++j) { acc[j] = dot2(a[j], n, acc[j]); acc[4 + j] = dot2(b[j], n, acc[4 + j]); }
}
__device__ __forceinline__ void fma8(float (&acc)[8], v4u wv, u32 alo, u32 ahi) {
#pragma unroll
  for (int j = 0; j < 4; ++j) { acc[2 * j] = dot2(wv[j], alo, acc[2 * j]); acc[2 * j + 1] = dot2(wv[j], ahi, acc[2 * j + 1]); }
}
__device__ __forceinline__ u64 gtag(const v4u& g) { return ((u64)g[3] << 32) | (u64)g[2]; }
__device__ __forceinline__ void mark(v4u& g, bool unused, u64 tag) {
  g[2] = unused ? (u32)tag : g[2]; g[3] = unused ? (u32)(tag >> 32) : g[3];
}
__device__ __forceinline__ void reload(v4u (&g)[1], const int (&o)[1], const u64 (&m)[1], v4u srd) {
  asm volatile("s_mov_b64 exec, %2\n\tbuffer_load_dwordx4 %0, %1, %3, 0 offen sc1\n\ts_mov_b64 exec, -1\n\ts_waitcnt vmcnt(0)"
               : "+v"(g[0]) : "v"(o[0]), "s"(m[0]), "s"(srd) : "memory");
}
__device__ __forceinline__ void reload(v4u (&g)[2], const int (&o)[2], const u64 (&m)[2], v4u srd) {
  asm volatile("s_mov_b64 exec, %4\n\tbuffer_load_dwordx4 %0, %2, %6, 0 offen sc1\n\t"
               "s_mov_b64 exec, %5\n\tbuffer_load_dwordx4 %1, %3, %6, 0 offen sc1\n\t"
               "s_mov_b64 exec, -1\n\ts_waitcnt vmcnt(0)"
               : "+v"(g[0]), "+v"(g[1])
               : "v"(o[0]), "v"(o[1]), "s"(m[0]), "s"(m[1]), "s"(srd) : "memory");
}
__device__ __forceinline__ void reload(v4u (&g)[4], const int (&o)[4], const u64 (&m)[4], v4u srd) {
  asm volatile("s_mov_b64 exec, %8\n\tbuffer_load_dwordx4 %0, %4, %12, 0 offen sc1\n\t"
               "s_mov_b64 exec, %9\n\tbuffer_load_dwordx4 %1, %5, %12, 0 offen sc1\n\t"
               "s_mov_b64 exec, %10\n\tbuffer_load_dwordx4 %2, %6, %12, 0 offen sc1\n\t"
               "s_mov_b64 exec, %11\n\tbuffer_load_dwordx4 %3, %7, %12, 0 offen sc1\n\t"
               "s_mov_b64 exec, -1\n\ts_waitcnt vmcnt(0)"
               : "+v"(g[0]), "+v"(g[1]), "+v"(g[2]), "+v"(g[3])
               : "v"(o[0]), "v"(o[1]), "v"(o[2]), "v"(o[3]), "s"(m[0]), "s"(m[1]), "s"(m[2]), "s"(m[3]), "s"(srd) : "memory");
}
__device__ __forceinline__ void reload(v4u (&g)[8], const int (&o)[8], const u64 (&m)[8], v4u srd) {
  asm volatile("s_mov_b64 exec, %16\n\tbuffer_load_dwordx4 %0, %8, %24, 0 offen sc1\n\t"
               "s_mov_b64 exec, %17\n\tbuffer_load_dwordx4 %1, %9, %24, 0 offen sc1\n\t"
               "s_mov_b64 exec, %18\n\tbuffer_load_dwordx4 %2, %10, %24, 0 offen sc1\n\t"
               "s_mov_b64 exec, %19\n\tbuffer_load_dwordx4 %3, %11, %24, 0 offen sc1\n\t"
               "s_mov_b64 exec, %20\n\tbuffer_load_dwordx4 %4, %12, %24, 0 offen sc1\n\t"
               "s_mov_b64 exec, %21\n\tbuffer_load_dwordx4 %5, %13, %24, 0 offen sc1\n\t"
               "s_mov_b64 exec, %22\n\tbuffer_load_dwordx4 %6, %14, %24, 0 offen sc1\n\t"
               "s_mov_b64 exec, %23\n\tbuffer_load_dwordx4 %7, %15, %24, 0 offen sc1\n\t"
               "s_mov_b64 exec, -1\n\ts_waitcnt vmcnt(0)"
               : "+v"(g[0]), "+v"(g[1]), "+v"(g[2]), "+v"(g[3]), "+v"(g[4]), "+v"(g[5]), "+v"(g[6]), "+v"(g[7])
               : "v"(o[0]), "v"(o[1]), "v"(o[2]), "v"(o[3]), "v"(o[4]), "v"(o[5]), "v"(o[6]), "v"(o[7]), "s"(m[0]), "s"(m[1]), "s"(m[2]), "s"(m[3]), "s"(m[4]), "s"(m[5]), "s"(m[6]), "s"(m[7]), "s"(srd) : "memory");
}
template <int N, int MANY> __device__ __forceinline__ u32 poll(v4u srd, const int (&off)[N], v4u (&gr)[N], u64 tag) {
  u32 spins = 0;
  u64 m[N], any = 0;
#pragma unroll
  for (int i = 0; i < N; ++i) { m[i] = __ballot(gtag(gr[i]) != tag); any |= m[i]; }
  if (__builtin_expect(any != 0, 0)) {
    do {
      if (MANY < 64 && __builtin_popcountll(any) > MANY) __builtin_amdgcn_s_sleep(8);
      reload(gr, off, m, srd);
      any = 0;
#pragma unroll
      for (int i = 0; i < N; ++i) { m[i] = __ballot(gtag(gr[i]) != tag); any |= m[i]; }
    } while (any != 0 && ++spins < SPIN_CAP);
    ++spins;
  }
  return spins;
}
__device__ __forceinline__ float rcp(float x) { return __builtin_amdgcn_rcpf(x); }
#define STAMP(i) do { } while (0)
#define STAMP_FLUSH() do { } while (0)
#define SB() __builtin_amdgcn_sched_barrier(0)
#define TAG() const u64 tag = (((u64)__builtin_amdgcn_readfirstlane(ep[1]) << 32) | (u64)__builtin_amdgcn_readfirstlane(ep[0])) + 1; \
  const u32 t0 = (u32)tag, t1 = (u32)(tag >> 32)

__global__ void __launch_bounds__(512, 2)
fused_decode_block(const u16* __restrict__ x, const u16* __restrict__ Kc, const u16* __restrict__ Vc, const u16* __restrict__ wq,
                   const u16* __restrict__ wo, const u16* __restrict__ wg, const u16* __restrict__ wu, const u16* __restrict__ wd,
                   u16* __restrict__ out, unsigned char* ws) {
  __shared__ __attribute__((aligned(16))) float lds[L_END];
  asm volatile("" :: "s"(x), "s"(Kc), "s"(Vc), "s"(wq), "s"(wo), "s"(wg), "s"(wu), "s"(wd), "s"(out), "s"(ws));
  const int tid = threadIdx.x, lane = tid & 63;
  const int w = __builtin_amdgcn_readfirstlane(tid >> 6);
  const int b = blockIdx.x, g = b & 7, s = b >> 3;
  const __amdgpu_buffer_rsrc_t rs = __builtin_amdgcn_make_buffer_rsrc((void*)ws, 0, WS_END, 0x00020000);
  const v4u srd = {(u32)(uintptr_t)ws, (u32)((uintptr_t)ws >> 32) & 0xffffu, (u32)WS_END, 0x00020000u};
  STAMP(0);
  const v2u ep = __builtin_amdgcn_raw_buffer_load_b64(rs, WS_EPOCH, 0, AUX_SC1);
#define CODE_PF_USE() do { } while (0)

  if (s < NSP) {
    volatile LAS u32* lflag = (volatile LAS u32*)(&lds[L_FLAG]);
    if (tid == 0) { lflag[0] = 0u; lflag[1] = 0u; }
    __builtin_amdgcn_s_setprio(1);
    bar();
    const int g4 = lane >> 4, c = lane & 15;
    const int tile0 = (256 * s) / 7, nx = (256 * (s + 1)) / 7 - tile0 - 32;
    const bool five = w < nx;
    const int key0 = (tile0 + 4 * w + (five ? w : nx)) * 8;
    const __amdgpu_buffer_rsrc_t rk = __builtin_amdgcn_make_buffer_rsrc((void*)Kc, 0, 8 * SK * DH * 2, 0x00020000);
    const __amdgpu_buffer_rsrc_t rv = __builtin_amdgcn_make_buffer_rsrc((void*)Vc, 0, 8 * SK * DH * 2, 0x00020000);
    v4u kr[20], vr[20];
    {
      const int kb = ((g * SK + key0 + (c >> 1)) * DH + (c & 1) * 32 + g4 * 8) * 2;
      const int kb5 = five ? kb + 4 * 4096 : OOB;
#pragma unroll
      for (int t = 0; t < 4; ++t)
#pragma unroll
        for (int ks = 0; ks < 4; ++ks) kr[t * 4 + ks] = ldb(rk, kb + t * 4096 + ks * 128);
#pragma unroll
      for (int ks = 0; ks < 4; ++ks) kr[16 + ks] = ldb(rk, five ? kb5 + ks * 128 : OOB);
      SB();
      const int vb = ((g * SK + key0 + 2 * g4) * DH + 8 * c) * 2;
#pragma unroll
      for (int hf = 0; hf < 2; ++hf) {
#pragma unroll
        for (int j = 0; j < 8; ++j) vr[hf * 10 + j] = ldb(rv, vb + (8 * (j >> 1) + (j & 1)) * 512 + 256 * hf);
#pragma unroll
        for (int j = 0; j < 2; ++j) vr[hf * 10 + 8 + j] = ldb(rv, five ? vb + (32 + j) * 512 + 256 * hf : OOB);
        SB();
      }
    }
    const int oh = 2 * g + s / 14, cb = s % 14;
    const int sub = lane >> 3, gq = lane & 7;
    STAMP(1);
    TAG();
    CODE_PF_USE();
    if (w == 0) {
      __builtin_amdgcn_s_setprio(3);
      v4u qg[2];
      int qoff[2];
#pragma unroll
      for (int i = 0; i < 2; ++i) { qoff[i] = WS_Q + (g * 128 + i * 64 + lane) * 16; qg[i] = ldg(rs, qoff[i]); }
      poll<2, 64>(srd, qoff, qg, tag);
#pragma unroll
      for (int i = 0; i < 2; ++i)
        *reinterpret_cast<v2u*>(&lds[L_QS + (i * 64 + lane) * 2]) = v2u{qg[i][0], qg[i][1]};
      asm volatile("s_waitcnt lgkmcnt(0)" ::: "memory");
      lflag[0] = 1u;
      __builtin_amdgcn_s_setprio(1);
    }
    for (u32 sp = 0; lflag[0] == 0u && sp < (SPIN_CAP << 4); ++sp) __builtin_amdgcn_s_sleep(1);
    asm volatile("" ::: "memory");
    STAMP(2);
    const int hd = (c >> 2) & 1;
    const u32 meta = ((c >> 1) & 1) ? 0xffffffffu : 0u, mvar = (c & 1) ? 0xffffffffu : 0u, mhi = (g4 >> 1) ? 0xffffffffu : 0u;
    float sc[10];
    {
      v4u qf[4];
#pragma unroll
      for (int ks = 0; ks < 4; ++ks) qf[ks] = *reinterpret_cast<const v4u*>(&lds[L_QS + hd * 128 + ks * 32 + ((c >> 1) & 1) * 16 + g4 * 4]);
#pragma unroll
      for (int t = 0; t < 5; ++t) {
        v4f d = {0.f, 0.f, 0.f, 0.f};
#pragma unroll
        for (int ks = 0; ks < 4; ++ks) d = mfma(kr[t * 4 + ks], qf[ks], d);
        const float sa = bsel(meta, d[1], d[0]), sb = bsel(meta, d[3], d[2]);
        sc[2 * t] = rbf(sa + dpp<X2>(sa)) * 0.0625f;
        sc[2 * t + 1] = rbf(sb + dpp<X2>(sb)) * 0.0625f;
      }
      if (!five) { sc[8] = -INFINITY; sc[9] = -INFINITY; }
    }
    float m = sc[0];
#pragma unroll
    for (int j = 1; j < 10; ++j) m = fmaxf(m, sc[j]);
    m = sw32_max(sw16_max(m));
    v4u pa;
    v2u pa5;
    {
      float p[10], l = 0.f;
#pragma unroll
      for (int j = 0; j < 10; ++j) { p[j] = __builtin_amdgcn_exp2f((sc[j] - m) * LOG2E); l += p[j]; }
      l = sw16_sum(l, l); l = sw32_sum(l, l);
      if ((lane & 0x3b) == 0) { lds[L_WMAX + w * 2 + hd] = m; lds[L_WSUM + w * 2 + hd] = l; }
#pragma unroll
      for (int j = 0; j < 10; ++j) { const float hi = rbf(p[j]); p[j] = bsel(mvar, p[j] - hi, hi); }
#pragma unroll
      for (int t = 0; t < 4; ++t) pa[t] = cvtpk(p[2 * t], p[2 * t + 1]);
      pa5[0] = cvtpk(p[8], p[9]); pa5[1] = 0u;
    }
    STAMP(3);
#pragma unroll
    for (int hf = 0; hf < 2; ++hf) {
      v4u bf[8];
      v2u b5[8];
#pragma unroll
      for (int t = 0; t < 4; ++t)
#pragma unroll
        for (int d = 0; d < 8; ++d)
          bf[d][t] = __builtin_amdgcn_perm(vr[hf * 10 + 2 * t + 1][d >> 1], vr[hf * 10 + 2 * t][d >> 1], (d & 1) ? 0x07060302u : 0x05040100u);
#pragma unroll
      for (int d = 0; d < 8; ++d) {
        b5[d][0] = __builtin_amdgcn_perm(vr[hf * 10 + 9][d >> 1], vr[hf * 10 + 8][d >> 1], (d & 1) ? 0x07060302u : 0x05040100u);
        b5[d][1] = 0u;
      }
      SB();
      v4f d1[8], d2[8];
#pragma unroll
      for (int d = 0; d < 8; ++d) d1[d] = mfma(pa, bf[d], v4f{0.f, 0.f, 0.f, 0.f});
#pragma unroll
      for (int d = 0; d < 8; ++d) d2[d] = mfma16(pa5, b5[d], v4f{0.f, 0.f, 0.f, 0.f});
      SB();
      v4f y;
#pragma unroll
      for (int e = 0; e < 4; ++e) {
        const float lo = (d1[e][0] + d1[e][1]) + (d2[e][0] + d2[e][1]);
        const float hi = (d1[4 + e][0] + d1[4 + e][1]) + (d2[4 + e][0] + d2[4 + e][1]);
        y[e] = bsel(mhi, hi, lo);
      }
      *reinterpret_cast<v4f*>(&lds[L_ORED + (w * 2 + (g4 & 1)) * 256 + 128 * hf + 8 * c + 4 * (g4 >> 1)]) = y;
    }
    STAMP(4);
    __builtin_amdgcn_s_setprio(0);
    asm volatile("s_waitcnt lgkmcnt(0)" ::: "memory");
    if (lane == 0) __hip_atomic_fetch_add((LAS u32*)(&lds[L_FLAG + 1]), 1u, __ATOMIC_RELAXED, __HIP_MEMORY_SCOPE_WORKGROUP);
    const __amdgpu_buffer_rsrc_t ro = __builtin_amdgcn_make_buffer_rsrc((void*)wo, 0, NQ * D * 2, 0x00020000);
    const int orow = 64 * (w & 3) + 4 * gq + 2 * (lane >> 5);
    const int oo = w >= 4 ? ((oh * 256 + orow) * D + 32 * cb + 8 * (sub & 3)) * 2 : OOB;
    v4u wor[4];
    wor[0] = ldb(ro, oo); wor[1] = ldb(ro, w >= 4 ? oo + D * 2 : OOB);
    wor[2] = ldb(ro, w >= 4 ? oo + 32 * D * 2 : OOB); wor[3] = ldb(ro, w >= 4 ? oo + 33 * D * 2 : OOB);
    SB();
    if (w < 4) {
      v4u po[8];
      int poff[8];
      const int mysp = w + 4 * (lane & 7);
      const bool mlwant = lane < 7 && mysp != s;
      poff[7] = WS_ML + (oh * 32 + mysp) * 128;
      po[7] = v4u{mlwant ? 0u : 0xff800000u, 0u, 0u, 0u};
      mark(po[7], !mlwant, tag);
#pragma unroll
      for (int i = 0; i < 7; ++i) {
        po[i] = v4u{0u, 0u, 0u, 0u};
        mark(po[i], w + 4 * i == s, tag);
        poff[i] = WS_PO + ((oh * 32 + w + 4 * i) * 64 + lane) * 16;
      }
      for (u32 sp = 0; sp < SPIN_CAP; ++sp) {
        u64 pm[8], pany = 0;
#pragma unroll
        for (int i = 0; i < 8; ++i) { pm[i] = __ballot(gtag(po[i]) != tag); pany |= pm[i]; }
        if (pany == 0) break;
        const bool few = __builtin_popcountll(pm[7]) <= 3;
        if (!few && sp) __builtin_amdgcn_s_sleep(4);
#pragma unroll
        for (int i = 0; i < 7; ++i)
          if (!few && ((pm[7] >> i) & 1)) pm[i] = 0ull;
        reload(po, poff, pm, srd);
      }
      const v4u ml = po[7];
      STAMP(7);
      const float mi = __uint_as_float(ml[0]);
      float mx = fmaxf(mi, dpp<X1>(mi)); mx = fmaxf(mx, dpp<X2>(mx)); mx = fmaxf(mx, dpp<X7>(mx));
      const float wt = __builtin_amdgcn_exp2f((mi - mx) * LOG2E);
      float lw = wt * __uint_as_float(ml[1]);
      lw += dpp<X1>(lw); lw += dpp<X2>(lw); lw += dpp<X7>(lw);
      v4f o = {0.f, 0.f, 0.f, 0.f};
#pragma unroll
      for (int i = 0; i < 7; ++i) {
        const float f = __builtin_bit_cast(float, __builtin_amdgcn_readlane(__builtin_bit_cast(int, wt), i));
        o[0] = fmaf(f, bflo(po[i][0]), o[0]); o[1] = fmaf(f, bfhi(po[i][0]), o[1]);
        o[2] = fmaf(f, bflo(po[i][1]), o[2]); o[3] = fmaf(f, bfhi(po[i][1]), o[3]);
      }
      *reinterpret_cast<v4f*>(&lds[L_CMB + w * 256 + lane * 4]) = o;
      if (lane == 0) *reinterpret_cast<v2f*>(&lds[L_CML + w * 2]) = v2f{mx, lw};
    } else if (w < 6) {
      const int hh = w - 4;
      for (u32 sp = 0; lflag[1] < 8u && sp < (SPIN_CAP << 4); ++sp) { }
      asm volatile("" ::: "memory");
      STAMP(5);
      float mw[8], Mh;
#pragma unroll
      for (int ww = 0; ww < 8; ++ww) mw[ww] = lds[L_WMAX + ww * 2 + hh];
      Mh = fmaxf(fmaxf(fmaxf(mw[0], mw[1]), fmaxf(mw[2], mw[3])), fmaxf(fmaxf(mw[4], mw[5]), fmaxf(mw[6], mw[7])));
      v4f o = {0.f, 0.f, 0.f, 0.f}, o2 = {0.f, 0.f, 0.f, 0.f};
      float L = 0.f;
#pragma unroll
      for (int ww = 0; ww < 8; ++ww) {
        const float f = __builtin_amdgcn_exp2f((mw[ww] - Mh) * LOG2E);
        const v4f t = *reinterpret_cast<const v4f*>(&lds[L_ORED + (ww * 2 + hh) * 256 + lane * 4]);
        if (ww & 1) o2 += f * t; else o += f * t;
        L = fmaf(f, lds[L_WSUM + ww * 2 + hh], L);
      }
      o += o2;
      const u32 c0 = cvtpk(o[0], o[1]), c1 = cvtpk(o[2], o[3]);
      stg(rs, WS_PO + (((2 * g + hh) * 32 + s) * 64 + lane) * 16, c0, c1, t0, t1);
      if (lane == 0) stg(rs, WS_ML + ((2 * g + hh) * 32 + s) * 128, __float_as_uint(Mh), __float_as_uint(L), t0, t1);
      if (hh == (oh & 1)) {
        *reinterpret_cast<v4f*>(&lds[L_CMB + 4 * 256 + lane * 4]) = v4f{bflo(c0), bfhi(c0), bflo(c1), bfhi(c1)};
        if (lane == 0) *reinterpret_cast<v2f*>(&lds[L_CML + 8]) = v2f{Mh, L};
      }
      STAMP(6);
    }
    pairup(wor[0], wor[1]); pairup(wor[2], wor[3]);
    asm volatile("" : "+v"(wor[0]), "+v"(wor[1]), "+v"(wor[2]), "+v"(wor[3]));
    bar();
    if (w >= 4) {
      v2f cm[5], co[5], c2[5];
#pragma unroll
      for (int i = 0; i < 5; ++i) {
        cm[i] = *reinterpret_cast<const v2f*>(&lds[L_CML + i * 2]);
        co[i] = *reinterpret_cast<const v2f*>(&lds[L_CMB + i * 256 + orow]);
        c2[i] = *reinterpret_cast<const v2f*>(&lds[L_CMB + i * 256 + orow + 32]);
      }
      const float Mx = fmaxf(fmaxf(fmaxf(cm[0][0], cm[1][0]), fmaxf(cm[2][0], cm[3][0])), cm[4][0]);
      float L = 0.f;
      v2f u = {0.f, 0.f}, u2 = {0.f, 0.f};
#pragma unroll
      for (int i = 0; i < 5; ++i) {
        const float f = __builtin_amdgcn_exp2f((cm[i][0] - Mx) * LOG2E);
        L = fmaf(f, cm[i][1], L);
        u += f * co[i]; u2 += f * c2[i];
      }
      const float rl = rcp(L);
      float acc[8];
#pragma unroll
      for (int j = 0; j < 8; ++j) acc[j] = 0.f;
      fmap(acc, wor[0], wor[1], cvtpk(u[0] * rl, u[1] * rl));
      fmap(acc, wor[2], wor[3], cvtpk(u2[0] * rl, u2[1] * rl));
      float r4[4], r2[2];
#pragma unroll
      for (int j = 0; j < 4; ++j) r4[j] = sw32_sum(acc[j], acc[j + 4]);
#pragma unroll
      for (int j = 0; j < 2; ++j) r2[j] = tr_step<X7>(r4[j], r4[j + 2], (lane & 4) != 0);
      float r1 = tr_step<X2>(r2[0], r2[1], (lane & 2) != 0);
      r1 += dpp<X1>(r1);
      if ((lane & 1) == 0) lds[L_S1 + (w - 4) * 32 + (sub & 3) * 8 + (lane >> 5) * 4 + ((lane >> 2) & 1) * 2 + ((lane >> 1) & 1)] = r1;
    }
    bar();
    if (w == 0 && lane < 16) {
      v2f a[4];
#pragma unroll
      for (int ww = 0; ww < 4; ++ww) a[ww] = *reinterpret_cast<const v2f*>(&lds[L_S1 + ww * 32 + lane * 2]);
      const v2f t = (a[0] + a[1]) + (a[2] + a[3]);
      stg(rs, WS_PH + (oh * 224 + cb * 16 + lane) * 16, __float_as_uint(t[0]), __float_as_uint(t[1]), t0, t1);
    }
    STAMP(8);
    STAMP_FLUSH();
    return;
  }

  const int f = g * 4 + (s - NSP);
  const u64 tstart = __builtin_amdgcn_s_memrealtime();
  const bool ffn = f < 28;
  if (tid == 0) *(volatile LAS u32*)(&lds[L_FC]) = 0u;
  const int r4 = lane >> 4;
  const v4u xv = *reinterpret_cast<const v4u*>(x + (lane < 56 ? lane : 0) * 8);
  u32 xq[7];
  v4u wa[14], wf[14], wdn[8];
  {
    const int row0 = 56 * w + 14 * r4;
#pragma unroll
    for (int i = 0; i < 7; ++i) xq[i] = *reinterpret_cast<const u32*>(x + row0 + 2 * i);
    const u16* p = wq + (size_t)row0 * NQ + 128 * f + (lane & 15) * 8;
#pragma unroll
    for (int i = 0; i < 14; ++i) wa[i] = ldnt(p + (size_t)i * NQ);
    SB();
    bar();
    SB();
    const int fc = ffn ? f : 0;
    const u16* pf = (((lane >> 3) & 1) ? wu : wg) + (size_t)row0 * FF + 64 * fc + (lane & 7) * 8;
    float xf[8];
#pragma unroll
    for (int j = 0; j < 4; ++j) { xf[2 * j] = bflo(xv[j]); xf[2 * j + 1] = bfhi(xv[j]); }
    float sm = 0.f;
#pragma unroll
    for (int j = 0; j < 8; ++j) sm += xf[j];
    sm = wave_sum(lane < 56 ? sm : 0.f);
    const float mean = sm * (1.0f / D);
    float sq = 0.f;
#pragma unroll
    for (int j = 0; j < 8; ++j) { const float d = xf[j] - mean; sq += d * d; }
    sq = wave_sum(lane < 56 ? sq : 0.f);
    const float mb = rbf(mean), den = rbf(sqrtf(rbf(rbf(sq * (1.0f / D)) + 1e-5f)));
    float acc[8];
#pragma unroll
    for (int j = 0; j < 8; ++j) acc[j] = 0.f;
#pragma unroll
    for (int i = 0; i < 7; ++i) {
      const u32 n = cvtpk(rbf(bflo(xq[i]) - mb) / den, rbf(bfhi(xq[i]) - mb) / den);
      pairup(wa[2 * i], wa[2 * i + 1]);
      fmap(acc, wa[2 * i], wa[2 * i + 1], n);
    }
    float a4[4];
#pragma unroll
    for (int j = 0; j < 4; ++j) a4[j] = sw32_sum(acc[j], acc[j + 4]);
    v2f a2;
#pragma unroll
    for (int j = 0; j < 2; ++j) a2[j] = sw16_sum(a4[j], a4[j + 2]);
    *reinterpret_cast<v2f*>(&lds[L_QR + w * 128 + (lane & 15) * 8 + (lane >> 5) * 4 + ((lane >> 4) & 1) * 2]) = a2;
    bar();
    TAG();
    CODE_PF_USE();
    if (w == 0 && lane < 32) {
      v4f qv = *reinterpret_cast<const v4f*>(&lds[L_QR + lane * 4]);
#pragma unroll
      for (int ww = 1; ww < 8; ++ww) qv += *reinterpret_cast<const v4f*>(&lds[L_QR + ww * 128 + lane * 4]);
      stg(rs, WS_Q + (f * 32 + lane) * 16, cvtpk(qv[0], qv[1]), cvtpk(qv[2], qv[3]), t0, t1);
    }
    STAMP(1);
    if (!ffn) { STAMP_FLUSH(); return; }
    {
      const u64 tq = __builtin_amdgcn_s_memrealtime();
      const u64 hold = ((tq - tstart) * DEFER) >> 3;
      while (__builtin_amdgcn_s_memrealtime() - tq < hold) __builtin_amdgcn_s_sleep(4);
    }
    SB();
#pragma unroll
    for (int i = 0; i < 14; ++i) wf[i] = ldnt(pf + (size_t)i * FF);
    SB();
  }
  const int l56 = lane < 56 ? lane : 0;
  {
    const u16* pd = wd + (size_t)(64 * f + 8 * w) * D + 8 * l56;
#pragma unroll
    for (int i = 0; i < 8; ++i) wdn[i] = ldnt(pd + i * D);
    SB();
  }
#pragma unroll
  for (int i = 0; i < 7; ++i) { pairup(wf[2 * i], wf[2 * i + 1]); asm volatile("" : "+v"(wf[2 * i]), "+v"(wf[2 * i + 1])); }
#pragma unroll
  for (int i = 0; i < 4; ++i) { pairup(wdn[2 * i], wdn[2 * i + 1]); asm volatile("" : "+v"(wdn[2 * i]), "+v"(wdn[2 * i + 1])); }
  {
    float acc[8];
#pragma unroll
    for (int j = 0; j < 8; ++j) acc[j] = 0.f;
#pragma unroll
    for (int i = 0; i < 7; ++i) fmap(acc, wf[2 * i], wf[2 * i + 1], 0x3f803f80u);
    float a4[4];
#pragma unroll
    for (int j = 0; j < 4; ++j) a4[j] = sw32_sum(acc[j], acc[j + 4]);
    v2f a2;
#pragma unroll
    for (int j = 0; j < 2; ++j) a2[j] = sw16_sum(a4[j], a4[j + 2]);
    *reinterpret_cast<v2f*>(&lds[L_QR + w * 128 + (lane & 15) * 8 + (lane >> 5) * 4 + ((lane >> 4) & 1) * 2]) = a2;
  }
  bar();
  const int u = 8 * w + (lane & 7);
  float SG = 0.f, SU = 0.f;
#pragma unroll
  for (int ww = 0; ww < 8; ++ww) { SG += lds[L_QR + ww * 128 + u]; SU += lds[L_QR + ww * 128 + 64 + u]; }
  asm volatile("" : "+v"(SG), "+v"(SU));
  {
    const u32 pt0 = __builtin_amdgcn_raw_buffer_load_b32(rs, WS_PD + (f * 224 + (tid < 224 ? tid : 0)) * 16, 0, AUX_SC1);
    const u32 pt1 = *reinterpret_cast<const volatile u32*>(out + 16 * f);
    asm volatile("" :: "v"(pt0), "v"(pt1));
  }
  TAG();
  STAMP(2);
  {
    const int hsub = lane >> 5;
    const bool act = (lane & 31) < 28;
    const int pp = act ? (lane & 31) : 0;
    int off[8];
    v4u gr[8];
    const u32 xp = *reinterpret_cast<const u32*>(x + 56 * w + 2 * pp);
#pragma unroll
    for (int i = 0; i < 8; ++i) { off[i] = act ? WS_PH + ((2 * i + hsub) * 224 + 28 * w + pp) * 16 : OOB; gr[i] = ldg(rs, off[i]); }
#pragma unroll
    for (int i = 0; i < 8; ++i) mark(gr[i], !act, tag);
    poll<8, 64>(srd, off, gr, tag);
    STAMP(9);
    float h0 = ((__uint_as_float(gr[0][0]) + __uint_as_float(gr[1][0])) + (__uint_as_float(gr[2][0]) + __uint_as_float(gr[3][0]))) +
               ((__uint_as_float(gr[4][0]) + __uint_as_float(gr[5][0])) + (__uint_as_float(gr[6][0]) + __uint_as_float(gr[7][0])));
    float h1 = ((__uint_as_float(gr[0][1]) + __uint_as_float(gr[1][1])) + (__uint_as_float(gr[2][1]) + __uint_as_float(gr[3][1]))) +
               ((__uint_as_float(gr[4][1]) + __uint_as_float(gr[5][1])) + (__uint_as_float(gr[6][1]) + __uint_as_float(gr[7][1])));
    h0 = sw32_sum(h0, h0); h1 = sw32_sum(h1, h1);
    const u32 a01 = cvtpk(h0, h1);
    const u32 h01 = cvtpk(bflo(xp) + bflo(a01), bfhi(xp) + bfhi(a01));
    h0 = bflo(h01); h1 = bfhi(h01);
    LAS u32* xn = (LAS u32*)(&lds[L_XN + w * 32]);
    if (lane < 28) xn[(lane / 7) * 8 + lane % 7] = h01;
    asm volatile("s_waitcnt lgkmcnt(0)" ::: "memory");
    const v4u na = *(const LAS v4u*)(xn + r4 * 8), nb = *(const LAS v4u*)(xn + r4 * 8 + 4);
    STAMP(8);
    const float lm = half_sum(act ? h0 + h1 : 0.f) * (1.0f / 56.0f);
    const float d0 = h0 - lm, d1 = h1 - lm;
    const float m2 = half_sum(act ? d0 * d0 + d1 * d1 : 0.f);
    if (lane == 0) *reinterpret_cast<v2f*>(&lds[L_ST + w * 2]) = v2f{lm, m2};
    {
      const int cp = 28 * w + pp - 8 * f;
      if (act && hsub == 0 && cp >= 0 && cp < 8) *reinterpret_cast<v2f*>(&lds[L_HS + cp * 2]) = v2f{h0, h1};
    }
    float acc[8];
#pragma unroll
    for (int j = 0; j < 8; ++j) acc[j] = 0.f;
#pragma unroll
    for (int i = 0; i < 7; ++i) fmap(acc, wf[2 * i], wf[2 * i + 1], i < 4 ? na[i] : nb[i - 4]);
    float a4[4];
#pragma unroll
    for (int j = 0; j < 4; ++j) a4[j] = sw32_sum(acc[j], acc[j + 4]);
    v2f a2;
#pragma unroll
    for (int j = 0; j < 2; ++j) a2[j] = sw16_sum(a4[j], a4[j + 2]);
    *reinterpret_cast<v2f*>(&lds[L_GU + w * 128 + (lane & 15) * 8 + (lane >> 5) * 4 + ((lane >> 4) & 1) * 2]) = a2;
  }
  STAMP(3);
  bar();
  STAMP(4);
  {
    float gp[8], up[8], lms[8], m2s[8];
#pragma unroll
    for (int ww = 0; ww < 8; ++ww) {
      gp[ww] = lds[L_GU + ww * 128 + u]; up[ww] = lds[L_GU + ww * 128 + 64 + u];
      const v2f v = *reinterpret_cast<const v2f*>(&lds[L_ST + ww * 2]); lms[ww] = v[0]; m2s[ww] = v[1];
    }
    const float mean = (((lms[0] + lms[1]) + (lms[2] + lms[3])) + ((lms[4] + lms[5]) + (lms[6] + lms[7]))) * 0.125f;
#pragma unroll
    for (int ww = 0; ww < 8; ++ww) { const float d = lms[ww] - mean; m2s[ww] = fmaf(56.0f * d, d, m2s[ww]); }
    const float M2 = ((m2s[0] + m2s[1]) + (m2s[2] + m2s[3])) + ((m2s[4] + m2s[5]) + (m2s[6] + m2s[7]));
    const float mb = rbf(mean), rden = rcp(rbf(__builtin_amdgcn_sqrtf(rbf(rbf(M2 * (1.0f / D)) + 1e-5f))));
    float G = ((gp[0] + gp[1]) + (gp[2] + gp[3])) + ((gp[4] + gp[5]) + (gp[6] + gp[7]));
    float U = ((up[0] + up[1]) + (up[2] + up[3])) + ((up[4] + up[5]) + (up[6] + up[7]));
    G = rbf(fmaf(-mb, SG, G) * rden); U = rbf(fmaf(-mb, SU, U) * rden);
    const float z = 0.7978845608028654f * (G + 0.044715f * G * G * G);
    const float th = 1.0f - 2.0f * rcp(__builtin_amdgcn_exp2f(z * (2.0f * LOG2E)) + 1.0f);
    const float av = rbf(0.5f * G * (1.0f + th)) * U;
    const u32 pk = cvtpk(av, dpp<X1>(av));
    STAMP(5);
    float acc[8];
#pragma unroll
    for (int j = 0; j < 8; ++j) acc[j] = 0.f;
#pragma unroll
    for (int i = 0; i < 4; ++i) fmap(acc, wdn[2 * i], wdn[2 * i + 1], __builtin_amdgcn_readlane(pk, 2 * i));
    if (lane < 56) {
      *reinterpret_cast<v4f*>(&lds[L_DN + w * 448 + lane * 8]) = v4f{acc[0], acc[1], acc[2], acc[3]};
      *reinterpret_cast<v4f*>(&lds[L_DN + w * 448 + lane * 8 + 4]) = v4f{acc[4], acc[5], acc[6], acc[7]};
    }
  }
  STAMP(6);
  bar();
  STAMP(7);
  if (tid < 224) {
    v2f a[8];
#pragma unroll
    for (int ww = 0; ww < 8; ++ww) a[ww] = *reinterpret_cast<const v2f*>(&lds[L_DN + ww * 448 + tid * 2]);
    const v2f t = ((a[0] + a[1]) + (a[2] + a[3])) + ((a[4] + a[5]) + (a[6] + a[7]));
    stg(rs, WS_PD + (f * 224 + tid) * 16, __float_as_uint(t[0]), __float_as_uint(t[1]), t0, t1);
  }
  STAMP(10);
  if (w < 7) {
    int off[1];
    v4u gr[1];
    off[0] = lane < 32 ? WS_PD + ((4 * w + (lane >> 3)) * 224 + 8 * f + (lane & 7)) * 16 : OOB;
    gr[0] = ldg(rs, off[0]);
    mark(gr[0], lane >= 32, tag);
    poll<1, 64>(srd, off, gr, tag);
    STAMP(11);
    float a0 = __uint_as_float(gr[0][0]), a1 = __uint_as_float(gr[0][1]);
    float r = sw16_sum(a0, a1);
    r += dpp<X8>(r);
    if (lane < 32 && (lane & 8) == 0) lds[L_FN + w * 16 + (lane & 7) * 2 + (lane >> 4)] = r;
    asm volatile("s_waitcnt lgkmcnt(0)" ::: "memory");
    u32 old = 0;
    if (lane == 0) old = __hip_atomic_fetch_add((LAS u32*)(&lds[L_FC]), 1u, __ATOMIC_RELAXED, __HIP_MEMORY_SCOPE_WORKGROUP);
    if (__builtin_amdgcn_readfirstlane(old) == 6u) {
      asm volatile("" ::: "memory");
      if (lane < 8) {
        v2f a[7];
#pragma unroll
        for (int ww = 0; ww < 7; ++ww) a[ww] = *reinterpret_cast<const v2f*>(&lds[L_FN + ww * 16 + lane * 2]);
        const v2f t = ((a[0] + a[1]) + (a[2] + a[3])) + ((a[4] + a[5]) + a[6]);
        const v2f hv = *reinterpret_cast<const v2f*>(&lds[L_HS + lane * 2]);
        const u32 tb = cvtpk(t[0], t[1]);
        *reinterpret_cast<u32*>(out + 16 * f + lane * 2) = cvtpk(hv[0] + bflo(tb), hv[1] + bfhi(tb));
        if (f == 0 && lane == 0) __builtin_amdgcn_raw_buffer_store_b64(v2u{t0, t1}, rs, WS_EPOCH, 0, AUX_SC1);
      }
    }
  }
  STAMP(12);
  STAMP_FLUSH();
}

}

extern "C" void kernel_launch(void* const* d_in, const int* in_sizes, int n_in, void* d_out, int out_size, void* d_ws,
                              size_t ws_size, hipStream_t stream) {
  fused_decode_block<<<256, 512, 0, stream>>>((const u16*)d_in[0], (const u16*)d_in[1], (const u16*)d_in[2], (const u16*)d_in[3],
                                              (const u16*)d_in[4], (const u16*)d_in[6], (const u16*)d_in[7], (const u16*)d_in[8],
                                              (u16*)d_out, (unsigned char*)d_ws);
}
```

```cpp
#include <hip/hip_runtime.h>
#include <hip/hip_bf16.h>
#include <stdint.h>

typedef unsigned short u16;
typedef unsigned int u32;
typedef unsigned long long u64;
typedef u32 v4u __attribute__((ext_vector_type(4)));
typedef u32 v2u __attribute__((ext_vector_type(2)));
typedef float v4f __attribute__((ext_vector_type(4)));
typedef float v2f __attribute__((ext_vector_type(2)));
typedef __bf16 bf16x2 __attribute__((ext_vector_type(2)));
typedef __bf16 bf16x8 __attribute__((ext_vector_type(8)));
typedef short s16x4 __attribute__((ext_vector_type(4)));

#define LAS __attribute__((address_space(3)))
#define DEFER 2
#define PRETOUCH 1
#define QPOLL 4
#define DIAG 0

namespace {

constexpr int D = 448, SK = 8192, DH = 256, FF = 1792, NQ = 4096, NSP = 28;
constexpr int WS_EPOCH = 0;
constexpr int WS_Q = 4096;
constexpr int WS_ML = 32768;
constexpr int WS_PO = 131072;
constexpr int WS_PH = WS_PO + 524288;
constexpr int WS_PD = WS_PH + 65536;
constexpr int WS_END = WS_PD + 28 * 3584;
constexpr int WS_STAMP = 1 << 20;
constexpr int AUX_SC1 = 16, AUX_NT = 2;
constexpr int OOB = 0x7ffffff0;
constexpr u32 SPIN_CAP = 1u << 17;
constexpr float LOG2E = 1.4426950408889634f;

constexpr int L_QS = 0;
constexpr int L_WMAX = 256;
constexpr int L_WSUM = 272;
constexpr int L_S1 = 288;
constexpr int L_FLAG = 544;
constexpr int L_CML = 548;
constexpr int L_ORED = 576;
constexpr int L_CMB = L_ORED + 4096;
constexpr int L_END = L_CMB + 5 * 256;
constexpr int L_QR = 0;
constexpr int L_ST = 1024;
constexpr int L_XN = 1040;
constexpr int L_HS = 1296;
constexpr int L_GU = 1312;
constexpr int L_DN = 2336;
constexpr int L_FN = 0;
constexpr int L_FC = 5920;

__device__ __forceinline__ float bflo(u32 u) { return __uint_as_float(u << 16); }
__device__ __forceinline__ float bfhi(u32 u) { return __uint_as_float(u & 0xffff0000u); }
__device__ __forceinline__ u32 cvtpk(float lo, float hi) {
  v2f v = {lo, hi};
  return __builtin_bit_cast(u32, __builtin_convertvector(v, bf16x2));
}
__device__ __forceinline__ float rbf(float f) { return bflo(cvtpk(f, 0.f)); }
__device__ __forceinline__ float dot2(u32 w, u32 a, float acc) {
  return __builtin_amdgcn_fdot2_f32_bf16(__builtin_bit_cast(bf16x2, w), __builtin_bit_cast(bf16x2, a), acc, false);
}
__device__ __forceinline__ float bsel(u32 m, float a, float b) {
  return __uint_as_float((__float_as_uint(a) & m) | (__float_as_uint(b) & ~m));
}
template <int CTRL> __device__ __forceinline__ float dpp(float x) {
  return __builtin_bit_cast(float, __builtin_amdgcn_mov_dpp(__builtin_bit_cast(int, x), CTRL, 0xf, 0xf, true));
}
constexpr int X1 = 0xB1, X2 = 0x4E, X7 = 0x141, X8 = 0x128;
__device__ __forceinline__ float sw32_sum(float a, float b) {
  auto r = __builtin_amdgcn_permlane32_swap(__float_as_uint(a), __float_as_uint(b), false, false);
  return __uint_as_float(r[0]) + __uint_as_float(r[1]);
}
__device__ __forceinline__ float sw16_sum(float a, float b) {
  auto r = __builtin_amdgcn_permlane16_swap(__float_as_uint(a), __float_as_uint(b), false, false);
  return __uint_as_float(r[0]) + __uint_as_float(r[1]);
}
__device__ __forceinline__ float sw32_max(float a) {
  auto r = __builtin_amdgcn_permlane32_swap(__float_as_uint(a), __float_as_uint(a), false, false);
  return fmaxf(__uint_as_float(r[0]), __uint_as_float(r[1]));
}
__device__ __forceinline__ float sw16_max(float a) {
  auto r = __builtin_amdgcn_permlane16_swap(__float_as_uint(a), __float_as_uint(a), false, false);
  return fmaxf(__uint_as_float(r[0]), __uint_as_float(r[1]));
}
template <int CTRL> __device__ __forceinline__ float tr_step(float lo, float hi, bool sel) {
  const float t1 = lo + dpp<CTRL>(lo), t2 = hi + dpp<CTRL>(hi);
  return sel ? t2 : t1;
}
__device__ __forceinline__ float wave_sum(float x) {
  x += dpp<X1>(x); x += dpp<X2>(x); x += dpp<X7>(x); x += dpp<X8>(x);
  x = sw16_sum(x, x); return sw32_sum(x, x);
}
__device__ __forceinline__ float half_sum(float x) {
  x += dpp<X1>(x); x += dpp<X2>(x); x += dpp<X7>(x); x += dpp<X8>(x);
  return sw16_sum(x, x);
}
__device__ __forceinline__ v4u ldnt(const u16* p) { return __builtin_nontemporal_load(reinterpret_cast<const v4u*>(p)); }
__device__ __forceinline__ v4u ldb(__amdgpu_buffer_rsrc_t rs, int off) { return __builtin_amdgcn_raw_buffer_load_b128(rs, off, 0, AUX_NT); }
__device__ __forceinline__ v4u ldg(__amdgpu_buffer_rsrc_t rs, int off) { return __builtin_amdgcn_raw_buffer_load_b128(rs, off, 0, AUX_SC1); }
__device__ __forceinline__ void stg(__amdgpu_buffer_rsrc_t rs, int off, u32 a, u32 b, u32 t0, u32 t1) {
  v4u v = {a, b, t0, t1};
  __builtin_amdgcn_raw_buffer_store_b128(v, rs, off, 0, AUX_SC1);
}
__device__ __forceinline__ void bar() {
  asm volatile("s_waitcnt lgkmcnt(0)" ::: "memory");
  __builtin_amdgcn_s_barrier();
  asm volatile("" ::: "memory");
}
__device__ __forceinline__ v4f mfma(v4u a, v4u b, v4f c) {
  return __builtin_amdgcn_mfma_f32_16x16x32_bf16(__builtin_bit_cast(bf16x8, a), __builtin_bit_cast(bf16x8, b), c, 0, 0, 0);
}
__device__ __forceinline__ v4f mfma16(v2u a, v2u b, v4f c) {
  return __builtin_amdgcn_mfma_f32_16x16x16bf16_1k(__builtin_bit_cast(s16x4, a), __builtin_bit_cast(s16x4, b), c, 0, 0, 0);
}
__device__ __forceinline__ u32 plo(u32 r0, u32 r1) { return __builtin_amdgcn_perm(r1, r0, 0x05040100u); }
__device__ __forceinline__ u32 phi(u32 r0, u32 r1) { return __builtin_amdgcn_perm(r1, r0, 0x07060302u); }
__device__ __forceinline__ void pairup(v4u& a, v4u& b) {
  v4u x, y;
  x[0] = plo(a[0], b[0]); x[1] = phi(a[0], b[0]); x[2] = plo(a[1], b[1]); x[3] = phi(a[1], b[1]);
  y[0] = plo(a[2], b[2]); y[1] = phi(a[2], b[2]); y[2] = plo(a[3], b[3]); y[3] = phi(a[3], b[3]);
  a = x; b = y;
}
__device__ __forceinline__ void fmap(float (&acc)[8], v4u a, v4u b, u32 n) {
#pragma unroll
  for (int j = 0; j < 4; ++j) { acc[j] = dot2(a[j], n, acc[j]); acc[4 + j] = dot2(b[j], n, acc[4 + j]); }
}
__device__ __forceinline__ void fma8(float (&acc)[8], v4u wv, u32 alo, u32 ahi) {
#pragma unroll
  for (int j = 0; j < 4; ++j) { acc[2 * j] = dot2(wv[j], alo, acc[2 * j]); acc[2 * j + 1] = dot2(wv[j], ahi, acc[2 * j + 1]); }
}
__device__ __forceinline__ u64 gtag(const v4u& g) { return ((u64)g[3] << 32) | (u64)g[2]; }
__device__ __forceinline__ void mark(v4u& g, bool unused, u64 tag) {
  g[2] = unused ? (u32)tag : g[2]; g[3] = unused ? (u32)(tag >> 32) : g[3];
}
__device__ __forceinline__ void reload(v4u (&g)[1], const int (&o)[1], const u64 (&m)[1], v4u srd) {
  asm volatile("s_mov_b64 exec, %2\n\tbuffer_load_dwordx4 %0, %1, %3, 0 offen sc1\n\ts_mov_b64 exec, -1\n\ts_waitcnt vmcnt(0)"
               : "+v"(g[0]) : "v"(o[0]), "s"(m[0]), "s"(srd) : "memory");
}
__device__ __forceinline__ void reload(v4u (&g)[2], const int (&o)[2], const u64 (&m)[2], v4u srd) {
  asm volatile("s_mov_b64 exec, %4\n\tbuffer_load_dwordx4 %0, %2, %6, 0 offen sc1\n\t"
               "s_mov_b64 exec, %5\n\tbuffer_load_dwordx4 %1, %3, %6, 0 offen sc1\n\t"
               "s_mov_b64 exec, -1\n\ts_waitcnt vmcnt(0)"
               : "+v"(g[0]), "+v"(g[1])
               : "v"(o[0]), "v"(o[1]), "s"(m[0]), "s"(m[1]), "s"(srd) : "memory");
}
__device__ __forceinline__ void reload(v4u (&g)[4], const int (&o)[4], const u64 (&m)[4], v4u srd) {
  asm volatile("s_mov_b64 exec, %8\n\tbuffer_load_dwordx4 %0, %4, %12, 0 offen sc1\n\t"
               "s_mov_b64 exec, %9\n\tbuffer_load_dwordx4 %1, %5, %12, 0 offen sc1\n\t"
               "s_mov_b64 exec, %10\n\tbuffer_load_dwordx4 %2, %6, %12, 0 offen sc1\n\t"
               "s_mov_b64 exec, %11\n\tbuffer_load_dwordx4 %3, %7, %12, 0 offen sc1\n\t"
               "s_mov_b64 exec, -1\n\ts_waitcnt vmcnt(0)"
               : "+v"(g[0]), "+v"(g[1]), "+v"(g[2]), "+v"(g[3])
               : "v"(o[0]), "v"(o[1]), "v"(o[2]), "v"(o[3]), "s"(m[0]), "s"(m[1]), "s"(m[2]), "s"(m[3]), "s"(srd) : "memory");
}
__device__ __forceinline__ void reload(v4u (&g)[8], const int (&o)[8], const u64 (&m)[8], v4u srd) {
  asm volatile("s_mov_b64 exec, %16\n\tbuffer_load_dwordx4 %0, %8, %24, 0 offen sc1\n\t"
               "s_mov_b64 exec, %17\n\tbuffer_load_dwordx4 %1, %9, %24, 0 offen sc1\n\t"
               "s_mov_b64 exec, %18\n\tbuffer_load_dwordx4 %2, %10, %24, 0 offen sc1\n\t"
               "s_mov_b64 exec, %19\n\tbuffer_load_dwordx4 %3, %11, %24, 0 offen sc1\n\t"
               "s_mov_b64 exec, %20\n\tbuffer_load_dwordx4 %4, %12, %24, 0 offen sc1\n\t"
               "s_mov_b64 exec, %21\n\tbuffer_load_dwordx4 %5, %13, %24, 0 offen sc1\n\t"
               "s_mov_b64 exec, %22\n\tbuffer_load_dwordx4 %6, %14, %24, 0 offen sc1\n\t"
               "s_mov_b64 exec, %23\n\tbuffer_load_dwordx4 %7, %15, %24, 0 offen sc1\n\t"
               "s_mov_b64 exec, -1\n\ts_waitcnt vmcnt(0)"
               : "+v"(g[0]), "+v"(g[1]), "+v"(g[2]), "+v"(g[3]), "+v"(g[4]), "+v"(g[5]), "+v"(g[6]), "+v"(g[7])
               : "v"(o[0]), "v"(o[1]), "v"(o[2]), "v"(o[3]), "v"(o[4]), "v"(o[5]), "v"(o[6]), "v"(o[7]), "s"(m[0]), "s"(m[1]), "s"(m[2]), "s"(m[3]), "s"(m[4]), "s"(m[5]), "s"(m[6]), "s"(m[7]), "s"(srd) : "memory");
}
template <int N, int MANY> __device__ __forceinline__ u32 poll(v4u srd, const int (&off)[N], v4u (&gr)[N], u64 tag) {
  u32 spins = 0;
  u64 m[N], any = 0;
#pragma unroll
  for (int i = 0; i < N; ++i) { m[i] = __ballot(gtag(gr[i]) != tag); any |= m[i]; }
  if (__builtin_expect(any != 0, 0)) {
    do {
      if (MANY < 64 && __builtin_popcountll(any) > MANY) __builtin_amdgcn_s_sleep(8);
      reload(gr, off, m, srd);
      any = 0;
#pragma unroll
      for (int i = 0; i < N; ++i) { m[i] = __ballot(gtag(gr[i]) != tag); any |= m[i]; }
    } while (any != 0 && ++spins < SPIN_CAP);
    ++spins;
  }
  return spins;
}
__device__ __forceinline__ float rcp(float x) { return __builtin_amdgcn_rcpf(x); }
#define STAMP(i) do { } while (0)
#define STAMP_FLUSH() do { } while (0)
#define SB() __builtin_amdgcn_sched_barrier(0)
#define TAG() const u64 tag = (((u64)__builtin_amdgcn_readfirstlane(ep[1]) << 32) | (u64)__builtin_amdgcn_readfirstlane(ep[0])) + 1; \
  const u32 t0 = (u32)tag, t1 = (u32)(tag >> 32)

__global__ void __launch_bounds__(512, 2)
fused_decode_block(const u16* __restrict__ x, const u16* __restrict__ Kc, const u16* __restrict__ Vc, const u16* __restrict__ wq,
                   const u16* __restrict__ wo, const u16* __restrict__ wg, const u16* __restrict__ wu, const u16* __restrict__ wd,
                   u16* __restrict__ out, unsigned char* ws) {
  __shared__ __attribute__((aligned(16))) float lds[L_END];
  asm volatile("" :: "s"(x), "s"(Kc), "s"(Vc), "s"(wq), "s"(wo), "s"(wg), "s"(wu), "s"(wd), "s"(out), "s"(ws));
  const int tid = threadIdx.x, lane = tid & 63;
  const int w = __builtin_amdgcn_readfirstlane(tid >> 6);
  const int b = blockIdx.x, g = b & 7, s = b >> 3;
  const __amdgpu_buffer_rsrc_t rs = __builtin_amdgcn_make_buffer_rsrc((void*)ws, 0, WS_END, 0x00020000);
  const v4u srd = {(u32)(uintptr_t)ws, (u32)((uintptr_t)ws >> 32) & 0xffffu, (u32)WS_END, 0x00020000u};
  STAMP(0);
  const v2u ep = __builtin_amdgcn_raw_buffer_load_b64(rs, WS_EPOCH, 0, AUX_SC1);

  if (s < NSP) {
    volatile LAS u32* lflag = (volatile LAS u32*)(&lds[L_FLAG]);
    if (tid == 0) { lflag[0] = 0u; lflag[1] = 0u; }
    __builtin_amdgcn_s_setprio(1);
    bar();
    const int g4 = lane >> 4, c = lane & 15;
    const int tile0 = (256 * s) / 7, nx = (256 * (s + 1)) / 7 - tile0 - 32;
    const bool five = w < nx;
    const int key0 = (tile0 + 4 * w + (five ? w : nx)) * 8;
    const __amdgpu_buffer_rsrc_t rk = __builtin_amdgcn_make_buffer_rsrc((void*)Kc, 0, 8 * SK * DH * 2, 0x00020000);
    const __amdgpu_buffer_rsrc_t rv = __builtin_amdgcn_make_buffer_rsrc((void*)Vc, 0, 8 * SK * DH * 2, 0x00020000);
    v4u kr[20], vr[20];
    {
      const int kb = ((g * SK + key0 + (c >> 1)) * DH + (c & 1) * 32 + g4 * 8) * 2;
      const int kb5 = five ? kb + 4 * 4096 : OOB;
#pragma unroll
      for (int t = 0; t < 4; ++t)
#pragma unroll
        for (int ks = 0; ks < 4; ++ks) kr[t * 4 + ks] = ldb(rk, kb + t * 4096 + ks * 128);
#pragma unroll
      for (int ks = 0; ks < 4; ++ks) kr[16 + ks] = ldb(rk, five ? kb5 + ks * 128 : OOB);
      SB();
      const int vb = ((g * SK + key0 + 2 * g4) * DH + 8 * c) * 2;
#pragma unroll
      for (int hf = 0; hf < 2; ++hf) {
#pragma unroll
        for (int j = 0; j < 8; ++j) vr[hf * 10 + j] = ldb(rv, vb + (8 * (j >> 1) + (j & 1)) * 512 + 256 * hf);
#pragma unroll
        for (int j = 0; j < 2; ++j) vr[hf * 10 + 8 + j] = ldb(rv, five ? vb + (32 + j) * 512 + 256 * hf : OOB);
        SB();
      }
    }
    const int oh = 2 * g + s / 14, cb = s % 14;
    const int sub = lane >> 3, gq = lane & 7;
    STAMP(1);
    TAG();
    if (w < QPOLL) {
      __builtin_amdgcn_s_setprio(3);
      v4u qg[2] = {v4u{0u, 0u, 0u, 0u}, v4u{0u, 0u, 0u, 0u}};
      int qoff[2];
      u64 qm[2] = {~0ull, ~0ull};
#pragma unroll
      for (int i = 0; i < 2; ++i) qoff[i] = WS_Q + (g * 128 + i * 64 + lane) * 16;
      for (u32 sp = 0; lflag[0] == 0u && sp < SPIN_CAP; ++sp) {
        reload(qg, qoff, qm, srd);
        qm[0] = __ballot(gtag(qg[0]) != tag); qm[1] = __ballot(gtag(qg[1]) != tag);
        if ((qm[0] | qm[1]) == 0) {
#pragma unroll
          for (int i = 0; i < 2; ++i)
            *reinterpret_cast<v2u*>(&lds[L_QS + (i * 64 + lane) * 2]) = v2u{qg[i][0], qg[i][1]};
          asm volatile("s_waitcnt lgkmcnt(0)" ::: "memory");
          lflag[0] = 1u;
          break;
        }
      }
      __builtin_amdgcn_s_setprio(1);
    }
    for (u32 sp = 0; lflag[0] == 0u && sp < (SPIN_CAP << 4); ++sp) __builtin_amdgcn_s_sleep(1);
    asm volatile("" ::: "memory");
    STAMP(2);
    const int hd = (c >> 2) & 1;
    const u32 meta = ((c >> 1) & 1) ? 0xffffffffu : 0u, mvar = (c & 1) ? 0xffffffffu : 0u, mhi = (g4 >> 1) ? 0xffffffffu : 0u;
    float sc[10];
    {
      v4u qf[4];
#pragma unroll
      for (int ks = 0; ks < 4; ++ks) qf[ks] = *reinterpret_cast<const v4u*>(&lds[L_QS + hd * 128 + ks * 32 + ((c >> 1) & 1) * 16 + g4 * 4]);
#pragma unroll
      for (int t = 0; t < 5; ++t) {
        v4f d = {0.f, 0.f, 0.f, 0.f};
#pragma unroll
        for (int ks = 0; ks < 4; ++ks) d = mfma(kr[t * 4 + ks], qf[ks], d);
        const float sa = bsel(meta, d[1], d[0]), sb = bsel(meta, d[3], d[2]);
        sc[2 * t] = rbf(sa + dpp<X2>(sa)) * 0.0625f;
        sc[2 * t + 1] = rbf(sb + dpp<X2>(sb)) * 0.0625f;
      }
      if (!five) { sc[8] = -INFINITY; sc[9] = -INFINITY; }
    }
    float m = sc[0];
#pragma unroll
    for (int j = 1; j < 10; ++j) m = fmaxf(m, sc[j]);
    m = sw32_max(sw16_max(m));
    v4u pa;
    v2u pa5;
    {
      float p[10], l = 0.f;
#pragma unroll
      for (int j = 0; j < 10; ++j) { p[j] = __builtin_amdgcn_exp2f((sc[j] - m) * LOG2E); l += p[j]; }
      l = sw16_sum(l, l); l = sw32_sum(l, l);
      if ((lane & 0x3b) == 0) { lds[L_WMAX + w * 2 + hd] = m; lds[L_WSUM + w * 2 + hd] = l; }
#pragma unroll
      for (int j = 0; j < 10; ++j) { const float hi = rbf(p[j]); p[j] = bsel(mvar, p[j] - hi, hi); }
#pragma unroll
      for (int t = 0; t < 4; ++t) pa[t] = cvtpk(p[2 * t], p[2 * t + 1]);
      pa5[0] = cvtpk(p[8], p[9]); pa5[1] = 0u;
    }
    STAMP(3);
#pragma unroll
    for (int hf = 0; hf < 2; ++hf) {
      v4u bf[8];
      v2u b5[8];
#pragma unroll
      for (int t = 0; t < 4; ++t)
#pragma unroll
        for (int d = 0; d < 8; ++d)
          bf[d][t] = __builtin_amdgcn_perm(vr[hf * 10 + 2 * t + 1][d >> 1], vr[hf * 10 + 2 * t][d >> 1], (d & 1) ? 0x07060302u : 0x05040100u);
#pragma unroll
      for (int d = 0; d < 8; ++d) {
        b5[d][0] = __builtin_amdgcn_perm(vr[hf * 10 + 9][d >> 1], vr[hf * 10 + 8][d >> 1], (d & 1) ? 0x07060302u : 0x05040100u);
        b5[d][1] = 0u;
      }
      SB();
      v4f d1[8], d2[8];
#pragma unroll
      for (int d = 0; d < 8; ++d) d1[d] = mfma(pa, bf[d], v4f{0.f, 0.f, 0.f, 0.f});
#pragma unroll
      for (int d = 0; d < 8; ++d) d2[d] = mfma16(pa5, b5[d], v4f{0.f, 0.f, 0.f, 0.f});
      SB();
      v4f y;
#pragma unroll
      for (int e = 0; e < 4; ++e) {
        const float lo = (d1[e][0] + d1[e][1]) + (d2[e][0] + d2[e][1]);
        const float hi = (d1[4 + e][0] + d1[4 + e][1]) + (d2[4 + e][0] + d2[4 + e][1]);
        y[e] = bsel(mhi, hi, lo);
      }
      *reinterpret_cast<v4f*>(&lds[L_ORED + (w * 2 + (g4 & 1)) * 256 + 128 * hf + 8 * c + 4 * (g4 >> 1)]) = y;
    }
    STAMP(4);
    __builtin_amdgcn_s_setprio(0);
    asm volatile("s_waitcnt lgkmcnt(0)" ::: "memory");
    if (lane == 0) __hip_atomic_fetch_add((LAS u32*)(&lds[L_FLAG + 1]), 1u, __ATOMIC_RELAXED, __HIP_MEMORY_SCOPE_WORKGROUP);
    const __amdgpu_buffer_rsrc_t ro = __builtin_amdgcn_make_buffer_rsrc((void*)wo, 0, NQ * D * 2, 0x00020000);
    const int orow = 64 * (w & 3) + 4 * gq + 2 * (lane >> 5);
    const int oo = w >= 4 ? ((oh * 256 + orow) * D + 32 * cb + 8 * (sub & 3)) * 2 : OOB;
    v4u wor[4];
    wor[0] = ldb(ro, oo); wor[1] = ldb(ro, w >= 4 ? oo + D * 2 : OOB);
    wor[2] = ldb(ro, w >= 4 ? oo + 32 * D * 2 : OOB); wor[3] = ldb(ro, w >= 4 ? oo + 33 * D * 2 : OOB);
    SB();
    if (w < 4) {
      v4u po[8];
      int poff[8];
      const int mysp = w + 4 * (lane & 7);
      const bool mlwant = lane < 7 && mysp != s;
      poff[7] = WS_ML + (oh * 32 + mysp) * 128;
      po[7] = v4u{mlwant ? 0u : 0xff800000u, 0u, 0u, 0u};
      mark(po[7], !mlwant, tag);
#pragma unroll
      for (int i = 0; i < 7; ++i) {
        po[i] = v4u{0u, 0u, 0u, 0u};
        mark(po[i], w + 4 * i == s, tag);
        poff[i] = WS_PO + ((oh * 32 + w + 4 * i) * 64 + lane) * 16;
      }
      for (u32 sp = 0; sp < SPIN_CAP; ++sp) {
        u64 pm[8], pany = 0;
#pragma unroll
        for (int i = 0; i < 8; ++i) { pm[i] = __ballot(gtag(po[i]) != tag); pany |= pm[i]; }
        if (pany == 0) break;
        const bool few = __builtin_popcountll(pm[7]) <= 3;
        if (!few && sp) __builtin_amdgcn_s_sleep(4);
#pragma unroll
        for (int i = 0; i < 7; ++i)
          if (!few && ((pm[7] >> i) & 1)) pm[i] = 0ull;
        reload(po, poff, pm, srd);
      }
      const v4u ml = po[7];
      STAMP(7);
      const float mi = __uint_as_float(ml[0]);
      float mx = fmaxf(mi, dpp<X1>(mi)); mx = fmaxf(mx, dpp<X2>(mx)); mx = fmaxf(mx, dpp<X7>(mx));
      const float wt = __builtin_amdgcn_exp2f((mi - mx) * LOG2E);
      float lw = wt * __uint_as_float(ml[1]);
      lw += dpp<X1>(lw); lw += dpp<X2>(lw); lw += dpp<X7>(lw);
      v4f o = {0.f, 0.f, 0.f, 0.f};
#pragma unroll
      for (int i = 0; i < 7; ++i) {
        const float f = __builtin_bit_cast(float, __builtin_amdgcn_readlane(__builtin_bit_cast(int, wt), i));
        o[0] = fmaf(f, bflo(po[i][0]), o[0]); o[1] = fmaf(f, bfhi(po[i][0]), o[1]);
        o[2] = fmaf(f, bflo(po[i][1]), o[2]); o[3] = fmaf(f, bfhi(po[i][1]), o[3]);
      }
      *reinterpret_cast<v4f*>(&lds[L_CMB + w * 256 + lane * 4]) = o;
      if (lane == 0) *reinterpret_cast<v2f*>(&lds[L_CML + w * 2]) = v2f{mx, lw};
    } else if (w < 6) {
      const int hh = w - 4;
      for (u32 sp = 0; lflag[1] < 8u && sp < (SPIN_CAP << 4); ++sp) { }
      asm volatile("" ::: "memory");
      STAMP(5);
      float mw[8], Mh;
#pragma unroll
      for (int ww = 0; ww < 8; ++ww) mw[ww] = lds[L_WMAX + ww * 2 + hh];
      Mh = fmaxf(fmaxf(fmaxf(mw[0], mw[1]), fmaxf(mw[2], mw[3])), fmaxf(fmaxf(mw[4], mw[5]), fmaxf(mw[6], mw[7])));
      v4f o = {0.f, 0.f, 0.f, 0.f}, o2 = {0.f, 0.f, 0.f, 0.f};
      float L = 0.f;
#pragma unroll
      for (int ww = 0; ww < 8; ++ww) {
        const float f = __builtin_amdgcn_exp2f((mw[ww] - Mh) * LOG2E);
        const v4f t = *reinterpret_cast<const v4f*>(&lds[L_ORED + (ww * 2 + hh) * 256 + lane * 4]);
        if (ww & 1) o2 += f * t; else o += f * t;
        L = fmaf(f, lds[L_WSUM + ww * 2 + hh], L);
      }
      o += o2;
      const u32 c0 = cvtpk(o[0], o[1]), c1 = cvtpk(o[2], o[3]);
      stg(rs, WS_PO + (((2 * g + hh) * 32 + s) * 64 + lane) * 16, c0, c1, t0, t1);
      if (lane == 0) stg(rs, WS_ML + ((2 * g + hh) * 32 + s) * 128, __float_as_uint(Mh), __float_as_uint(L), t0, t1);
      if (hh == (oh & 1)) {
        *reinterpret_cast<v4f*>(&lds[L_CMB + 4 * 256 + lane * 4]) = v4f{bflo(c0), bfhi(c0), bflo(c1), bfhi(c1)};
        if (lane == 0) *reinterpret_cast<v2f*>(&lds[L_CML + 8]) = v2f{Mh, L};
      }
      STAMP(6);
    }
    pairup(wor[0], wor[1]); pairup(wor[2], wor[3]);
    asm volatile("" : "+v"(wor[0]), "+v"(wor[1]), "+v"(wor[2]), "+v"(wor[3]));
    bar();
    if (w >= 4) {
      v2f cm[5], co[5], c2[5];
#pragma unroll
      for (int i = 0; i < 5; ++i) {
        cm[i] = *reinterpret_cast<const v2f*>(&lds[L_CML + i * 2]);
        co[i] = *reinterpret_cast<const v2f*>(&lds[L_CMB + i * 256 + orow]);
        c2[i] = *reinterpret_cast<const v2f*>(&lds[L_CMB + i * 256 + orow + 32]);
      }
      const float Mx = fmaxf(fmaxf(fmaxf(cm[0][0], cm[1][0]), fmaxf(cm[2][0], cm[3][0])), cm[4][0]);
      float L = 0.f;
      v2f u = {0.f, 0.f}, u2 = {0.f, 0.f};
#pragma unroll
      for (int i = 0; i < 5; ++i) {
        const float f = __builtin_amdgcn_exp2f((cm[i][0] - Mx) * LOG2E);
        L = fmaf(f, cm[i][1], L);
        u += f * co[i]; u2 += f * c2[i];
      }
      const float rl = rcp(L);
      float acc[8];
#pragma unroll
      for (int j = 0; j < 8; ++j) acc[j] = 0.f;
      fmap(acc, wor[0], wor[1], cvtpk(u[0] * rl, u[1] * rl));
      fmap(acc, wor[2], wor[3], cvtpk(u2[0] * rl, u2[1] * rl));
      float r4[4], r2[2];
#pragma unroll
      for (int j = 0; j < 4; ++j) r4[j] = sw32_sum(acc[j], acc[j + 4]);
#pragma unroll
      for (int j = 0; j < 2; ++j) r2[j] = tr_step<X7>(r4[j], r4[j + 2], (lane & 4) != 0);
      float r1 = tr_step<X2>(r2[0], r2[1], (lane & 2) != 0);
      r1 += dpp<X1>(r1);
      if ((lane & 1) == 0) lds[L_S1 + (w - 4) * 32 + (sub & 3) * 8 + (lane >> 5) * 4 + ((lane >> 2) & 1) * 2 + ((lane >> 1) & 1)] = r1;
    }
    bar();
    if (w == 0 && lane < 16) {
      v2f a[4];
#pragma unroll
      for (int ww = 0; ww < 4; ++ww) a[ww] = *reinterpret_cast<const v2f*>(&lds[L_S1 + ww * 32 + lane * 2]);
      const v2f t = (a[0] + a[1]) + (a[2] + a[3]);
      stg(rs, WS_PH + (oh * 224 + cb * 16 + lane) * 16, __float_as_uint(t[0]), __float_as_uint(t[1]), t0, t1);
    }
    STAMP(8);
    STAMP_FLUSH();
    return;
  }

  const int f = g * 4 + (s - NSP);
  const u64 tstart = __builtin_amdgcn_s_memrealtime();
  const bool ffn = f < 28;
  if (tid == 0) *(volatile LAS u32*)(&lds[L_FC]) = 0u;
  const int r4 = lane >> 4;
  const v4u xv = *reinterpret_cast<const v4u*>(x + (lane < 56 ? lane : 0) * 8);
  u32 xq[7];
  v4u wa[14], wf[14], wdn[8];
  {
    const int row0 = 56 * w + 14 * r4;
#pragma unroll
    for (int i = 0; i < 7; ++i) xq[i] = *reinterpret_cast<const u32*>(x + row0 + 2 * i);
    const u16* p = wq + (size_t)row0 * NQ + 128 * f + (lane & 15) * 8;
#pragma unroll
    for (int i = 0; i < 14; ++i) wa[i] = ldnt(p + (size_t)i * NQ);
    SB();
    bar();
    SB();
    const int fc = ffn ? f : 0;
    const u16* pf = (((lane >> 3) & 1) ? wu : wg) + (size_t)row0 * FF + 64 * fc + (lane & 7) * 8;
    float xf[8];
#pragma unroll
    for (int j = 0; j < 4; ++j) { xf[2 * j] = bflo(xv[j]); xf[2 * j + 1] = bfhi(xv[j]); }
    float sm = 0.f;
#pragma unroll
    for (int j = 0; j < 8; ++j) sm += xf[j];
    sm = wave_sum(lane < 56 ? sm : 0.f);
    const float mean = sm * (1.0f / D);
    float sq = 0.f;
#pragma unroll
    for (int j = 0; j < 8; ++j) { const float d = xf[j] - mean; sq += d * d; }
    sq = wave_sum(lane < 56 ? sq : 0.f);
    const float mb = rbf(mean), den = rbf(sqrtf(rbf(rbf(sq * (1.0f / D)) + 1e-5f)));
    float acc[8];
#pragma unroll
    for (int j = 0; j < 8; ++j) acc[j] = 0.f;
#pragma unroll
    for (int i = 0; i < 7; ++i) {
      const u32 n = cvtpk(rbf(bflo(xq[i]) - mb) / den, rbf(bfhi(xq[i]) - mb) / den);
      pairup(wa[2 * i], wa[2 * i + 1]);
      fmap(acc, wa[2 * i], wa[2 * i + 1], n);
    }
    float a4[4];
#pragma unroll
    for (int j = 0; j < 4; ++j) a4[j] = sw32_sum(acc[j], acc[j + 4]);
    v2f a2;
#pragma unroll
    for (int j = 0; j < 2; ++j) a2[j] = sw16_sum(a4[j], a4[j + 2]);
    *reinterpret_cast<v2f*>(&lds[L_QR + w * 128 + (lane & 15) * 8 + (lane >> 5) * 4 + ((lane >> 4) & 1) * 2]) = a2;
    bar();
    TAG();
    if (w == 0 && lane < 32) {
      v4f qv = *reinterpret_cast<const v4f*>(&lds[L_QR + lane * 4]);
#pragma unroll
      for (int ww = 1; ww < 8; ++ww) qv += *reinterpret_cast<const v4f*>(&lds[L_QR + ww * 128 + lane * 4]);
      stg(rs, WS_Q + (f * 32 + lane) * 16, cvtpk(qv[0], qv[1]), cvtpk(qv[2], qv[3]), t0, t1);
    }
    STAMP(1);
    if (!ffn) { STAMP_FLUSH(); return; }
    {
      const u64 tq = __builtin_amdgcn_s_memrealtime();
      const u64 hold = ((tq - tstart) * DEFER) >> 3;
      while (__builtin_amdgcn_s_memrealtime() - tq < hold) __builtin_amdgcn_s_sleep(4);
    }
    SB();
#pragma unroll
    for (int i = 0; i < 14; ++i) wf[i] = ldnt(pf + (size_t)i * FF);
    SB();
  }
  const int l56 = lane < 56 ? lane : 0;
  {
    const u16* pd = wd + (size_t)(64 * f + 8 * w) * D + 8 * l56;
#pragma unroll
    for (int i = 0; i < 8; ++i) wdn[i] = ldnt(pd + i * D);
    SB();
  }
#pragma unroll
  for (int i = 0; i < 7; ++i) { pairup(wf[2 * i], wf[2 * i + 1]); asm volatile("" : "+v"(wf[2 * i]), "+v"(wf[2 * i + 1])); }
#pragma unroll
  for (int i = 0; i < 4; ++i) { pairup(wdn[2 * i], wdn[2 * i + 1]); asm volatile("" : "+v"(wdn[2 * i]), "+v"(wdn[2 * i + 1])); }
  {
    float acc[8];
#pragma unroll
    for (int j = 0; j < 8; ++j) acc[j] = 0.f;
#pragma unroll
    for (int i = 0; i < 7; ++i) fmap(acc, wf[2 * i], wf[2 * i + 1], 0x3f803f80u);
    float a4[4];
#pragma unroll
    for (int j = 0; j < 4; ++j) a4[j] = sw32_sum(acc[j], acc[j + 4]);
    v2f a2;
#pragma unroll
    for (int j = 0; j < 2; ++j) a2[j] = sw16_sum(a4[j], a4[j + 2]);
    *reinterpret_cast<v2f*>(&lds[L_QR + w * 128 + (lane & 15) * 8 + (lane >> 5) * 4 + ((lane >> 4) & 1) * 2]) = a2;
  }
  bar();
  const int u = 8 * w + (lane & 7);
  float SG = 0.f, SU = 0.f;
#pragma unroll
  for (int ww = 0; ww < 8; ++ww) { SG += lds[L_QR + ww * 128 + u]; SU += lds[L_QR + ww * 128 + 64 + u]; }
  asm volatile("" : "+v"(SG), "+v"(SU));
  {
    const u32 pt0 = __builtin_amdgcn_raw_buffer_load_b32(rs, WS_PD + (f * 224 + (tid < 224 ? tid : 0)) * 16, 0, AUX_SC1);
    const u32 pt1 = *reinterpret_cast<const volatile u32*>(out + 16 * f);
    asm volatile("" :: "v"(pt0), "v"(pt1));
  }
  TAG();
  STAMP(2);
  {
    const int hsub = lane >> 5;
    const bool act = (lane & 31) < 28;
    const int pp = act ? (lane & 31) : 0;
    int off[8];
    v4u gr[8];
    const u32 xp = *reinterpret_cast<const u32*>(x + 56 * w + 2 * pp);
#pragma unroll
    for (int i = 0; i < 8; ++i) { off[i] = act ? WS_PH + ((2 * i + hsub) * 224 + 28 * w + pp) * 16 : OOB; gr[i] = ldg(rs, off[i]); }
#pragma unroll
    for (int i = 0; i < 8; ++i) mark(gr[i], !act, tag);
    poll<8, 64>(srd, off, gr, tag);
    STAMP(9);
    float h0 = ((__uint_as_float(gr[0][0]) + __uint_as_float(gr[1][0])) + (__uint_as_float(gr[2][0]) + __uint_as_float(gr[3][0]))) +
               ((__uint_as_float(gr[4][0]) + __uint_as_float(gr[5][0])) + (__uint_as_float(gr[6][0]) + __uint_as_float(gr[7][0])));
    float h1 = ((__uint_as_float(gr[0][1]) + __uint_as_float(gr[1][1])) + (__uint_as_float(gr[2][1]) + __uint_as_float(gr[3][1]))) +
               ((__uint_as_float(gr[4][1]) + __uint_as_float(gr[5][1])) + (__uint_as_float(gr[6][1]) + __uint_as_float(gr[7][1])));
    h0 = sw32_sum(h0, h0); h1 = sw32_sum(h1, h1);
    const u32 a01 = cvtpk(h0, h1);
    const u32 h01 = cvtpk(bflo(xp) + bflo(a01), bfhi(xp) + bfhi(a01));
    h0 = bflo(h01); h1 = bfhi(h01);
    LAS u32* xn = (LAS u32*)(&lds[L_XN + w * 32]);
    if (lane < 28) xn[(lane / 7) * 8 + lane % 7] = h01;
    asm volatile("s_waitcnt lgkmcnt(0)" ::: "memory");
    const v4u na = *(const LAS v4u*)(xn + r4 * 8), nb = *(const LAS v4u*)(xn + r4 * 8 + 4);
    STAMP(8);
    const float lm = half_sum(act ? h0 + h1 : 0.f) * (1.0f / 56.0f);
    const float d0 = h0 - lm, d1 = h1 - lm;
    const float m2 = half_sum(act ? d0 * d0 + d1 * d1 : 0.f);
    if (lane == 0) *reinterpret_cast<v2f*>(&lds[L_ST + w * 2]) = v2f{lm, m2};
    {
      const int cp = 28 * w + pp - 8 * f;
      if (act && hsub == 0 && cp >= 0 && cp < 8) *reinterpret_cast<v2f*>(&lds[L_HS + cp * 2]) = v2f{h0, h1};
    }
    float acc[8];
#pragma unroll
    for (int j = 0; j < 8; ++j) acc[j] = 0.f;
#pragma unroll
    for (int i = 0; i < 7; ++i) fmap(acc, wf[2 * i], wf[2 * i + 1], i < 4 ? na[i] : nb[i - 4]);
    float a4[4];
#pragma unroll
    for (int j = 0; j < 4; ++j) a4[j] = sw32_sum(acc[j], acc[j + 4]);
    v2f a2;
#pragma unroll
    for (int j = 0; j < 2; ++j) a2[j] = sw16_sum(a4[j], a4[j + 2]);
    *reinterpret_cast<v2f*>(&lds[L_GU + w * 128 + (lane & 15) * 8 + (lane >> 5) * 4 + ((lane >> 4) & 1) * 2]) = a2;
  }
  STAMP(3);
  bar();
  STAMP(4);
  {
    float gp[8], up[8], lms[8], m2s[8];
#pragma unroll
    for (int ww = 0; ww < 8; ++ww) {
      gp[ww] = lds[L_GU + ww * 128 + u]; up[ww] = lds[L_GU + ww * 128 + 64 + u];
      const v2f v = *reinterpret_cast<const v2f*>(&lds[L_ST + ww * 2]); lms[ww] = v[0]; m2s[ww] = v[1];
    }
    const float mean = (((lms[0] + lms[1]) + (lms[2] + lms[3])) + ((lms[4] + lms[5]) + (lms[6] + lms[7]))) * 0.125f;
#pragma unroll
    for (int ww = 0; ww < 8; ++ww) { const float d = lms[ww] - mean; m2s[ww] = fmaf(56.0f * d, d, m2s[ww]); }
    const float M2 = ((m2s[0] + m2s[1]) + (m2s[2] + m2s[3])) + ((m2s[4] + m2s[5]) + (m2s[6] + m2s[7]));
    const float mb = rbf(mean), rden = rcp(rbf(__builtin_amdgcn_sqrtf(rbf(rbf(M2 * (1.0f / D)) + 1e-5f))));
    float G = ((gp[0] + gp[1]) + (gp[2] + gp[3])) + ((gp[4] + gp[5]) + (gp[6] + gp[7]));
    float U = ((up[0] + up[1]) + (up[2] + up[3])) + ((up[4] + up[5]) + (up[6] + up[7]));
    G = rbf(fmaf(-mb, SG, G) * rden); U = rbf(fmaf(-mb, SU, U) * rden);
    const float z = 0.7978845608028654f * (G + 0.044715f * G * G * G);
    const float th = 1.0f - 2.0f * rcp(__builtin_amdgcn_exp2f(z * (2.0f * LOG2E)) + 1.0f);
    const float av = rbf(0.5f * G * (1.0f + th)) * U;
    const u32 pk = cvtpk(av, dpp<X1>(av));
    STAMP(5);
    float acc[8];
#pragma unroll
    for (int j = 0; j < 8; ++j) acc[j] = 0.f;
#pragma unroll
    for (int i = 0; i < 4; ++i) fmap(acc, wdn[2 * i], wdn[2 * i + 1], __builtin_amdgcn_readlane(pk, 2 * i));
    if (lane < 56) {
      *reinterpret_cast<v4f*>(&lds[L_DN + w * 448 + lane * 8]) = v4f{acc[0], acc[1], acc[2], acc[3]};
      *reinterpret_cast<v4f*>(&lds[L_DN + w * 448 + lane * 8 + 4]) = v4f{acc[4], acc[5], acc[6], acc[7]};
    }
  }
  STAMP(6);
  bar();
  STAMP(7);
  if (tid < 224) {
    v2f a[8];
#pragma unroll
    for (int ww = 0; ww < 8; ++ww) a[ww] = *reinterpret_cast<const v2f*>(&lds[L_DN + ww * 448 + tid * 2]);
    const v2f t = ((a[0] + a[1]) + (a[2] + a[3])) + ((a[4] + a[5]) + (a[6] + a[7]));
    stg(rs, WS_PD + (f * 224 + tid) * 16, __float_as_uint(t[0]), __float_as_uint(t[1]), t0, t1);
  }
  STAMP(10);
  if (w < 7) {
    int off[1];
    v4u gr[1];
    off[0] = lane < 32 ? WS_PD + ((4 * w + (lane >> 3)) * 224 + 8 * f + (lane & 7)) * 16 : OOB;
    gr[0] = ldg(rs, off[0]);
    mark(gr[0], lane >= 32, tag);
    poll<1, 64>(srd, off, gr, tag);
    STAMP(11);
    float a0 = __uint_as_float(gr[0][0]), a1 = __uint_as_float(gr[0][1]);
    float r = sw16_sum(a0, a1);
    r += dpp<X8>(r);
    if (lane < 32 && (lane & 8) == 0) lds[L_FN + w * 16 + (lane & 7) * 2 + (lane >> 4)] = r;
    asm volatile("s_waitcnt lgkmcnt(0)" ::: "memory");
    u32 old = 0;
    if (lane == 0) old = __hip_atomic_fetch_add((LAS u32*)(&lds[L_FC]), 1u, __ATOMIC_RELAXED, __HIP_MEMORY_SCOPE_WORKGROUP);
    if (__builtin_amdgcn_readfirstlane(old) == 6u) {
      asm volatile("" ::: "memory");
      if (lane < 8) {
        v2f a[7];
#pragma unroll
        for (int ww = 0; ww < 7; ++ww) a[ww] = *reinterpret_cast<const v2f*>(&lds[L_FN + ww * 16 + lane * 2]);
        const v2f t = ((a[0] + a[1]) + (a[2] + a[3])) + ((a[4] + a[5]) + a[6]);
        const v2f hv = *reinterpret_cast<const v2f*>(&lds[L_HS + lane * 2]);
        const u32 tb = cvtpk(t[0], t[1]);
        *reinterpret_cast<u32*>(out + 16 * f + lane * 2) = cvtpk(hv[0] + bflo(tb), hv[1] + bfhi(tb));
        if (f == 0 && lane == 0) __builtin_amdgcn_raw_buffer_store_b64(v2u{t0, t1}, rs, WS_EPOCH, 0, AUX_SC1);
      }
    }
  }
  STAMP(12);
  STAMP_FLUSH();
}

}

extern "C" void kernel_launch(void* const* d_in, const int* in_sizes, int n_in, void* d_out, int out_size, void* d_ws,
                              size_t ws_size, hipStream_t stream) {
  fused_decode_block<<<256, 512, 0, stream>>>((const u16*)d_in[0], (const u16*)d_in[1], (const u16*)d_in[2], (const u16*)d_in[3],
                                              (const u16*)d_in[4], (const u16*)d_in[6], (const u16*)d_in[7], (const u16*)d_in[8],
                                              (u16*)d_out, (unsigned char*)d_ws);
}
```

```cpp
#include <hip/hip_runtime.h>
#include <hip/hip_bf16.h>
#include <stdint.h>

typedef unsigned short u16;
typedef unsigned int u32;
typedef unsigned long long u64;
typedef u32 v4u __attribute__((ext_vector_type(4)));
typedef u32 v2u __attribute__((ext_vector_type(2)));
typedef float v4f __attribute__((ext_vector_type(4)));
typedef float v2f __attribute__((ext_vector_type(2)));
typedef __bf16 bf16x2 __attribute__((ext_vector_type(2)));
typedef __bf16 bf16x8 __attribute__((ext_vector_type(8)));
typedef short s16x4 __attribute__((ext_vector_type(4)));

#define LAS __attribute__((address_space(3)))
#define DEFER 2
#define PRETOUCH 1
#define QPOLL 4
#define FEW 4
#define GSL 4
#define WOEARLY 1
#define DIAG 0

namespace {

constexpr int D = 448, SK = 8192, DH = 256, FF = 1792, NQ = 4096, NSP = 28;
constexpr int WS_EPOCH = 0;
constexpr int WS_Q = 4096;
constexpr int WS_ML = 32768;
constexpr int WS_PO = 131072;
constexpr int WS_PH = WS_PO + 524288;
constexpr int WS_PD = WS_PH + 65536;
constexpr int WS_END = WS_PD + 28 * 3584;
constexpr int WS_STAMP = 1 << 20;
constexpr int AUX_SC1 = 16, AUX_NT = 2;
constexpr int OOB = 0x7ffffff0;
constexpr u32 SPIN_CAP = 1u << 17;
constexpr float LOG2E = 1.4426950408889634f;

constexpr int L_QS = 0;
constexpr int L_WMAX = 256;
constexpr int L_WSUM = 272;
constexpr int L_S1 = 288;
constexpr int L_FLAG = 544;
constexpr int L_CML = 548;
constexpr int L_ORED = 576;
constexpr int L_CMB = L_ORED + 4096;
constexpr int L_END = L_CMB + 5 * 256;
constexpr int L_QR = 0;
constexpr int L_ST = 1024;
constexpr int L_XN = 1040;
constexpr int L_HS = 1296;
constexpr int L_GU = 1312;
constexpr int L_DN = 2336;
constexpr int L_FN = 0;
constexpr int L_FC = 5920;

__device__ __forceinline__ float bflo(u32 u) { return __uint_as_float(u << 16); }
__device__ __forceinline__ float bfhi(u32 u) { return __uint_as_float(u & 0xffff0000u); }
__device__ __forceinline__ u32 cvtpk(float lo, float hi) {
  v2f v = {lo, hi};
  return __builtin_bit_cast(u32, __builtin_convertvector(v, bf16x2));
}
__device__ __forceinline__ float rbf(float f) { return bflo(cvtpk(f, 0.f)); }
__device__ __forceinline__ float dot2(u32 w, u32 a, float acc) {
  return __builtin_amdgcn_fdot2_f32_bf16(__builtin_bit_cast(bf16x2, w), __builtin_bit_cast(bf16x2, a), acc, false);
}
__device__ __forceinline__ float bsel(u32 m, float a, float b) {
  return __uint_as_float((__float_as_uint(a) & m) | (__float_as_uint(b) & ~m));
}
template <int CTRL> __device__ __forceinline__ float dpp(float x) {
  return __builtin_bit_cast(float, __builtin_amdgcn_mov_dpp(__builtin_bit_cast(int, x), CTRL, 0xf, 0xf, true));
}
constexpr int X1 = 0xB1, X2 = 0x4E, X7 = 0x141, X8 = 0x128;
__device__ __forceinline__ float sw32_sum(float a, float b) {
  auto r = __builtin_amdgcn_permlane32_swap(__float_as_uint(a), __float_as_uint(b), false, false);
  return __uint_as_float(r[0]) + __uint_as_float(r[1]);
}
__device__ __forceinline__ float sw16_sum(float a, float b) {
  auto r = __builtin_amdgcn_permlane16_swap(__float_as_uint(a), __float_as_uint(b), false, false);
  return __uint_as_float(r[0]) + __uint_as_float(r[1]);
}
__device__ __forceinline__ float sw32_max(float a) {
  auto r = __builtin_amdgcn_permlane32_swap(__float_as_uint(a), __float_as_uint(a), false, false);
  return fmaxf(__uint_as_float(r[0]), __uint_as_float(r[1]));
}
__device__ __forceinline__ float sw16_max(float a) {
  auto r = __builtin_amdgcn_permlane16_swap(__float_as_uint(a), __float_as_uint(a), false, false);
  return fmaxf(__uint_as_float(r[0]), __uint_as_float(r[1]));
}
template <int CTRL> __device__ __forceinline__ float tr_step(float lo, float hi, bool sel) {
  const float t1 = lo + dpp<CTRL>(lo), t2 = hi + dpp<CTRL>(hi);
  return sel ? t2 : t1;
}
__device__ __forceinline__ float wave_sum(float x) {
  x += dpp<X1>(x); x += dpp<X2>(x); x += dpp<X7>(x); x += dpp<X8>(x);
  x = sw16_sum(x, x); return sw32_sum(x, x);
}
__device__ __forceinline__ float half_sum(float x) {
  x += dpp<X1>(x); x += dpp<X2>(x); x += dpp<X7>(x); x += dpp<X8>(x);
  return sw16_sum(x, x);
}
__device__ __forceinline__ v4u ldnt(const u16* p) { return __builtin_nontemporal_load(reinterpret_cast<const v4u*>(p)); }
__device__ __forceinline__ v4u ldb(__amdgpu_buffer_rsrc_t rs, int off) { return __builtin_amdgcn_raw_buffer_load_b128(rs, off, 0, AUX_NT); }
__device__ __forceinline__ v4u ldg(__amdgpu_buffer_rsrc_t rs, int off) { return __builtin_amdgcn_raw_buffer_load_b128(rs, off, 0, AUX_SC1); }
__device__ __forceinline__ void stg(__amdgpu_buffer_rsrc_t rs, int off, u32 a, u32 b, u32 t0, u32 t1) {
  v4u v = {a, b, t0, t1};
  __builtin_amdgcn_raw_buffer_store_b128(v, rs, off, 0, AUX_SC1);
}
__device__ __forceinline__ void bar() {
  asm volatile("s_waitcnt lgkmcnt(0)" ::: "memory");
  __builtin_amdgcn_s_barrier();
  asm volatile("" ::: "memory");
}
__device__ __forceinline__ v4f mfma(v4u a, v4u b, v4f c) {
  return __builtin_amdgcn_mfma_f32_16x16x32_bf16(__builtin_bit_cast(bf16x8, a), __builtin_bit_cast(bf16x8, b), c, 0, 0, 0);
}
__device__ __forceinline__ v4f mfma16(v2u a, v2u b, v4f c) {
  return __builtin_amdgcn_mfma_f32_16x16x16bf16_1k(__builtin_bit_cast(s16x4, a), __builtin_bit_cast(s16x4, b), c, 0, 0, 0);
}
__device__ __forceinline__ u32 plo(u32 r0, u32 r1) { return __builtin_amdgcn_perm(r1, r0, 0x05040100u); }
__device__ __forceinline__ u32 phi(u32 r0, u32 r1) { return __builtin_amdgcn_perm(r1, r0, 0x07060302u); }
__device__ __forceinline__ void pairup(v4u& a, v4u& b) {
  v4u x, y;
  x[0] = plo(a[0], b[0]); x[1] = phi(a[0], b[0]); x[2] = plo(a[1], b[1]); x[3] = phi(a[1], b[1]);
  y[0] = plo(a[2], b[2]); y[1] = phi(a[2], b[2]); y[2] = plo(a[3], b[3]); y[3] = phi(a[3], b[3]);
  a = x; b = y;
}
__device__ __forceinline__ void fmap(float (&acc)[8], v4u a, v4u b, u32 n) {
#pragma unroll
  for (int j = 0; j < 4; ++j) { acc[j] = dot2(a[j], n, acc[j]); acc[4 + j] = dot2(b[j], n, acc[4 + j]); }
}
__device__ __forceinline__ void fma8(float (&acc)[8], v4u wv, u32 alo, u32 ahi) {
#pragma unroll
  for (int j = 0; j < 4; ++j) { acc[2 * j] = dot2(wv[j], alo, acc[2 * j]); acc[2 * j + 1] = dot2(wv[j], ahi, acc[2 * j + 1]); }
}
__device__ __forceinline__ u64 gtag(const v4u& g) { return ((u64)g[3] << 32) | (u64)g[2]; }
__device__ __forceinline__ void mark(v4u& g, bool unused, u64 tag) {
  g[2] = unused ? (u32)tag : g[2]; g[3] = unused ? (u32)(tag >> 32) : g[3];
}
__device__ __forceinline__ void reload(v4u (&g)[1], const int (&o)[1], const u64 (&m)[1], v4u srd) {
  asm volatile("s_mov_b64 exec, %2\n\tbuffer_load_dwordx4 %0, %1, %3, 0 offen sc1\n\ts_mov_b64 exec, -1\n\ts_waitcnt vmcnt(0)"
               : "+v"(g[0]) : "v"(o[0]), "s"(m[0]), "s"(srd) : "memory");
}
__device__ __forceinline__ void reload(v4u (&g)[2], const int (&o)[2], const u64 (&m)[2], v4u srd) {
  asm volatile("s_mov_b64 exec, %4\n\tbuffer_load_dwordx4 %0, %2, %6, 0 offen sc1\n\t"
               "s_mov_b64 exec, %5\n\tbuffer_load_dwordx4 %1, %3, %6, 0 offen sc1\n\t"
               "s_mov_b64 exec, -1\n\ts_waitcnt vmcnt(0)"
               : "+v"(g[0]), "+v"(g[1])
               : "v"(o[0]), "v"(o[1]), "s"(m[0]), "s"(m[1]), "s"(srd) : "memory");
}
__device__ __forceinline__ void reload(v4u (&g)[4], const int (&o)[4], const u64 (&m)[4], v4u srd) {
  asm volatile("s_mov_b64 exec, %8\n\tbuffer_load_dwordx4 %0, %4, %12, 0 offen sc1\n\t"
               "s_mov_b64 exec, %9\n\tbuffer_load_dwordx4 %1, %5, %12, 0 offen sc1\n\t"
               "s_mov_b64 exec, %10\n\tbuffer_load_dwordx4 %2, %6, %12, 0 offen sc1\n\t"
               "s_mov_b64 exec, %11\n\tbuffer_load_dwordx4 %3, %7, %12, 0 offen sc1\n\t"
               "s_mov_b64 exec, -1\n\ts_waitcnt vmcnt(0)"
               : "+v"(g[0]), "+v"(g[1]), "+v"(g[2]), "+v"(g[3])
               : "v"(o[0]), "v"(o[1]), "v"(o[2]), "v"(o[3]), "s"(m[0]), "s"(m[1]), "s"(m[2]), "s"(m[3]), "s"(srd) : "memory");
}
__device__ __forceinline__ void reload(v4u (&g)[8], const int (&o)[8], const u64 (&m)[8], v4u srd) {
  asm volatile("s_mov_b64 exec, %16\n\tbuffer_load_dwordx4 %0, %8, %24, 0 offen sc1\n\t"
               "s_mov_b64 exec, %17\n\tbuffer_load_dwordx4 %1, %9, %24, 0 offen sc1\n\t"
               "s_mov_b64 exec, %18\n\tbuffer_load_dwordx4 %2, %10, %24, 0 offen sc1\n\t"
               "s_mov_b64 exec, %19\n\tbuffer_load_dwordx4 %3, %11, %24, 0 offen sc1\n\t"
               "s_mov_b64 exec, %20\n\tbuffer_load_dwordx4 %4, %12, %24, 0 offen sc1\n\t"
               "s_mov_b64 exec, %21\n\tbuffer_load_dwordx4 %5, %13, %24, 0 offen sc1\n\t"
               "s_mov_b64 exec, %22\n\tbuffer_load_dwordx4 %6, %14, %24, 0 offen sc1\n\t"
               "s_mov_b64 exec, %23\n\tbuffer_load_dwordx4 %7, %15, %24, 0 offen sc1\n\t"
               "s_mov_b64 exec, -1\n\ts_waitcnt vmcnt(0)"
               : "+v"(g[0]), "+v"(g[1]), "+v"(g[2]), "+v"(g[3]), "+v"(g[4]), "+v"(g[5]), "+v"(g[6]), "+v"(g[7])
               : "v"(o[0]), "v"(o[1]), "v"(o[2]), "v"(o[3]), "v"(o[4]), "v"(o[5]), "v"(o[6]), "v"(o[7]), "s"(m[0]), "s"(m[1]), "s"(m[2]), "s"(m[3]), "s"(m[4]), "s"(m[5]), "s"(m[6]), "s"(m[7]), "s"(srd) : "memory");
}
template <int N, int MANY> __device__ __forceinline__ u32 poll(v4u srd, const int (&off)[N], v4u (&gr)[N], u64 tag) {
  u32 spins = 0;
  u64 m[N], any = 0;
#pragma unroll
  for (int i = 0; i < N; ++i) { m[i] = __ballot(gtag(gr[i]) != tag); any |= m[i]; }
  if (__builtin_expect(any != 0, 0)) {
    do {
      if (MANY < 64 && __builtin_popcountll(any) > MANY) __builtin_amdgcn_s_sleep(8);
      reload(gr, off, m, srd);
      any = 0;
#pragma unroll
      for (int i = 0; i < N; ++i) { m[i] = __ballot(gtag(gr[i]) != tag); any |= m[i]; }
    } while (any != 0 && ++spins < SPIN_CAP);
    ++spins;
  }
  return spins;
}
__device__ __forceinline__ float rcp(float x) { return __builtin_amdgcn_rcpf(x); }
#define STAMP(i) do { } while (0)
#define STAMP_FLUSH() do { } while (0)
#define SB() __builtin_amdgcn_sched_barrier(0)
#define TAG() const u64 tag = (((u64)(u32)__builtin_amdgcn_readfirstlane(ep[1]) << 32) | (u64)(u32)__builtin_amdgcn_readfirstlane(ep[0])) + 1; \
  const u32 t0 = (u32)tag, t1 = (u32)(tag >> 32)

__global__ void __launch_bounds__(512, 2)
fused_decode_block(const u16* __restrict__ x, const u16* __restrict__ Kc, const u16* __restrict__ Vc, const u16* __restrict__ wq,
                   const u16* __restrict__ wo, const u16* __restrict__ wg, const u16* __restrict__ wu, const u16* __restrict__ wd,
                   u16* __restrict__ out, unsigned char* ws) {
  __shared__ __attribute__((aligned(16))) float lds[L_END];
  asm volatile("" :: "s"(x), "s"(Kc), "s"(Vc), "s"(wq), "s"(wo), "s"(wg), "s"(wu), "s"(wd), "s"(out), "s"(ws));
  const int tid = threadIdx.x, lane = tid & 63;
  const int w = __builtin_amdgcn_readfirstlane(tid >> 6);
  const int b = blockIdx.x, g = b & 7, s = b >> 3;
  const __amdgpu_buffer_rsrc_t rs = __builtin_amdgcn_make_buffer_rsrc((void*)ws, 0, WS_END, 0x00020000);
  const v4u srd = {(u32)(uintptr_t)ws, (u32)((uintptr_t)ws >> 32) & 0xffffu, (u32)WS_END, 0x00020000u};
  STAMP(0);
  const v2u ep = __builtin_amdgcn_raw_buffer_load_b64(rs, WS_EPOCH, 0, AUX_SC1);

  if (s < NSP) {
    volatile LAS u32* lflag = (volatile LAS u32*)(&lds[L_FLAG]);
    if (tid == 0) { lflag[0] = 0u; lflag[1] = 0u; lflag[2] = 0u; }
    __builtin_amdgcn_s_setprio(1);
    bar();
    const int g4 = lane >> 4, c = lane & 15;
    const int tile0 = (256 * s) / 7, nx = (256 * (s + 1)) / 7 - tile0 - 32;
    const bool five = w < nx;
    const int key0 = (tile0 + 4 * w + (five ? w : nx)) * 8;
    const __amdgpu_buffer_rsrc_t rk = __builtin_amdgcn_make_buffer_rsrc((void*)Kc, 0, 8 * SK * DH * 2, 0x00020000);
    const __amdgpu_buffer_rsrc_t rv = __builtin_amdgcn_make_buffer_rsrc((void*)Vc, 0, 8 * SK * DH * 2, 0x00020000);
    v4u kr[20], vr[20];
    {
      const int kb = ((g * SK + key0 + (c >> 1)) * DH + (c & 1) * 32 + g4 * 8) * 2;
      const int kb5 = five ? kb + 4 * 4096 : OOB;
#pragma unroll
      for (int t = 0; t < 4; ++t)
#pragma unroll
        for (int ks = 0; ks < 4; ++ks) kr[t * 4 + ks] = ldb(rk, kb + t * 4096 + ks * 128);
#pragma unroll
      for (int ks = 0; ks < 4; ++ks) kr[16 + ks] = ldb(rk, five ? kb5 + ks * 128 : OOB);
      SB();
      const int vb = ((g * SK + key0 + 2 * g4) * DH + 8 * c) * 2;
#pragma unroll
      for (int hf = 0; hf < 2; ++hf) {
#pragma unroll
        for (int j = 0; j < 8; ++j) vr[hf * 10 + j] = ldb(rv, vb + (8 * (j >> 1) + (j & 1)) * 512 + 256 * hf);
#pragma unroll
        for (int j = 0; j < 2; ++j) vr[hf * 10 + 8 + j] = ldb(rv, five ? vb + (32 + j) * 512 + 256 * hf : OOB);
        SB();
      }
    }
    const int oh = 2 * g + s / 14, cb = s % 14;
    const int sub = lane >> 3, gq = lane & 7;
    const __amdgpu_buffer_rsrc_t ro = __builtin_amdgcn_make_buffer_rsrc((void*)wo, 0, NQ * D * 2, 0x00020000);
    const int orow = 64 * (w & 3) + 4 * gq + 2 * (lane >> 5);
    const int oo = ((oh * 256 + orow) * D + 32 * cb + 8 * (sub & 3)) * 2;
    v4u wor[4];
#define WO_LOADS() do { wor[0] = ldb(ro, w >= 4 ? oo : OOB); wor[1] = ldb(ro, w >= 4 ? oo + D * 2 : OOB); \
    wor[2] = ldb(ro, w >= 4 ? oo + 32 * D * 2 : OOB); wor[3] = ldb(ro, w >= 4 ? oo + 33 * D * 2 : OOB); SB(); } while (0)
    WO_LOADS();
    STAMP(1);
    TAG();
    if (w < QPOLL) {
      __builtin_amdgcn_s_setprio(3);
      v4u qg[2] = {v4u{0u, 0u, ~t0, 0u}, v4u{0u, 0u, ~t0, 0u}};
      int qoff[2];
      u64 qm[2] = {~0ull, ~0ull};
#pragma unroll
      for (int i = 0; i < 2; ++i) qoff[i] = WS_Q + (g * 128 + i * 64 + lane) * 16;
      for (u32 sp = 0; lflag[0] == 0u && sp < SPIN_CAP; ++sp) {
        reload(qg, qoff, qm, srd);
        qm[0] = __ballot(gtag(qg[0]) != tag); qm[1] = __ballot(gtag(qg[1]) != tag);
        if ((qm[0] | qm[1]) == 0) {
#pragma unroll
          for (int i = 0; i < 2; ++i)
            *reinterpret_cast<v2u*>(&lds[L_QS + (i * 64 + lane) * 2]) = v2u{qg[i][0], qg[i][1]};
          asm volatile("s_waitcnt lgkmcnt(0)" ::: "memory");
          lflag[0] = 1u;
          break;
        }
      }
      __builtin_amdgcn_s_setprio(1);
    }
    for (u32 sp = 0; lflag[0] == 0u && sp < (SPIN_CAP << 4); ++sp) __builtin_amdgcn_s_sleep(1);
    asm volatile("" ::: "memory");
    STAMP(2);
    const int hd = (c >> 2) & 1;
    const u32 meta = ((c >> 1) & 1) ? 0xffffffffu : 0u, mvar = (c & 1) ? 0xffffffffu : 0u, mhi = (g4 >> 1) ? 0xffffffffu : 0u;
    float sc[10];
    {
      v4u qf[4];
#pragma unroll
      for (int ks = 0; ks < 4; ++ks) qf[ks] = *reinterpret_cast<const v4u*>(&lds[L_QS + hd * 128 + ks * 32 + ((c >> 1) & 1) * 16 + g4 * 4]);
#pragma unroll
      for (int t = 0; t < 5; ++t) {
        v4f d = {0.f, 0.f, 0.f, 0.f};
#pragma unroll
        for (int ks = 0; ks < 4; ++ks) d = mfma(kr[t * 4 + ks], qf[ks], d);
        const float sa = bsel(meta, d[1], d[0]), sb = bsel(meta, d[3], d[2]);
        const u32 sp = cvtpk(sa + dpp<X2>(sa), sb + dpp<X2>(sb));
        sc[2 * t] = bflo(sp) * (0.0625f * LOG2E);
        sc[2 * t + 1] = bfhi(sp) * (0.0625f * LOG2E);
      }
      if (!five) { sc[8] = -INFINITY; sc[9] = -INFINITY; }
    }
    float m = fmaxf(fmaxf(fmaxf(fmaxf(sc[0], sc[1]), fmaxf(sc[2], sc[3])), fmaxf(fmaxf(sc[4], sc[5]), fmaxf(sc[6], sc[7]))), fmaxf(sc[8], sc[9]));
    m = sw32_max(sw16_max(m));
    v4u pa;
    v2u pa5;
    {
      float p[10];
#pragma unroll
      for (int j = 0; j < 10; ++j) p[j] = __builtin_amdgcn_exp2f(sc[j] - m);
      float l = (((p[0] + p[1]) + (p[2] + p[3])) + ((p[4] + p[5]) + (p[6] + p[7]))) + (p[8] + p[9]);
      l = sw16_sum(l, l); l = sw32_sum(l, l);
      if ((lane & 0x3b) == 0) { lds[L_WMAX + w * 2 + hd] = m; lds[L_WSUM + w * 2 + hd] = l; }
      asm volatile("s_waitcnt lgkmcnt(0)" ::: "memory");
      if (lane == 0) __hip_atomic_fetch_add((LAS u32*)(&lds[L_FLAG + 2]), 1u, __ATOMIC_RELAXED, __HIP_MEMORY_SCOPE_WORKGROUP);
      u32 pk[5];
#pragma unroll
      for (int t = 0; t < 5; ++t) {
        const u32 hi = cvtpk(p[2 * t], p[2 * t + 1]);
        const u32 lo = cvtpk(p[2 * t] - bflo(hi), p[2 * t + 1] - bfhi(hi));
        pk[t] = (lo & mvar) | (hi & ~mvar);
      }
      pa = v4u{pk[0], pk[1], pk[2], pk[3]};
      pa5 = v2u{pk[4], 0u};
    }
    STAMP(3);
    float fw[8] = {0.f, 0.f, 0.f, 0.f, 0.f, 0.f, 0.f, 0.f}, Mh = 0.f, L = 0.f;
#pragma unroll
    for (int hf = 0; hf < 2; ++hf) {
      v4u bf[8];
      v2u b5[8];
#pragma unroll
      for (int t = 0; t < 4; ++t)
#pragma unroll
        for (int d = 0; d < 8; ++d)
          bf[d][t] = __builtin_amdgcn_perm(vr[hf * 10 + 2 * t + 1][d >> 1], vr[hf * 10 + 2 * t][d >> 1], (d & 1) ? 0x07060302u : 0x05040100u);
#pragma unroll
      for (int d = 0; d < 8; ++d) {
        b5[d][0] = __builtin_amdgcn_perm(vr[hf * 10 + 9][d >> 1], vr[hf * 10 + 8][d >> 1], (d & 1) ? 0x07060302u : 0x05040100u);
        b5[d][1] = 0u;
      }
      SB();
      v4f d1[8], d2[8];
#pragma unroll
      for (int d = 0; d < 8; ++d) d1[d] = mfma(pa, bf[d], v4f{0.f, 0.f, 0.f, 0.f});
#pragma unroll
      for (int d = 0; d < 8; ++d) d2[d] = mfma16(pa5, b5[d], v4f{0.f, 0.f, 0.f, 0.f});
      SB();
      v4f y;
#pragma unroll
      for (int e = 0; e < 4; ++e) {
        const float lo = (d1[e][0] + d1[e][1]) + (d2[e][0] + d2[e][1]);
        const float hi = (d1[4 + e][0] + d1[4 + e][1]) + (d2[4 + e][0] + d2[4 + e][1]);
        y[e] = bsel(mhi, hi, lo);
      }
      *reinterpret_cast<v4f*>(&lds[L_ORED + (w * 2 + (g4 & 1)) * 256 + 128 * hf + 8 * c + 4 * (g4 >> 1)]) = y;
      if (hf == 0 && (w >> 1) == 2) {
        for (u32 sp = 0; lflag[2] < 8u && sp < (SPIN_CAP << 4); ++sp) { }
        asm volatile("" ::: "memory");
#pragma unroll
        for (int ww = 0; ww < 8; ++ww) fw[ww] = lds[L_WMAX + ww * 2 + (w & 1)];
        Mh = fmaxf(fmaxf(fmaxf(fw[0], fw[1]), fmaxf(fw[2], fw[3])), fmaxf(fmaxf(fw[4], fw[5]), fmaxf(fw[6], fw[7])));
#pragma unroll
        for (int ww = 0; ww < 8; ++ww) {
          fw[ww] = __builtin_amdgcn_exp2f(fw[ww] - Mh);
          L = fmaf(fw[ww], lds[L_WSUM + ww * 2 + (w & 1)], L);
        }
      }
    }
    STAMP(4);
    __builtin_amdgcn_s_setprio(0);
    asm volatile("s_waitcnt lgkmcnt(0)" ::: "memory");
    if (lane == 0) __hip_atomic_fetch_add((LAS u32*)(&lds[L_FLAG + 1]), 1u, __ATOMIC_RELAXED, __HIP_MEMORY_SCOPE_WORKGROUP);
    if (w < 4) {
      v4u po[8];
      int poff[8];
      const int mysp = w + 4 * (lane & 7);
      const bool mlwant = lane < 7 && mysp != s;
      poff[7] = WS_ML + (oh * 32 + mysp) * 128;
      po[7] = v4u{mlwant ? 0u : 0xff800000u, 0u, ~t0, 0u};
      mark(po[7], !mlwant, tag);
#pragma unroll
      for (int i = 0; i < 7; ++i) {
        po[i] = v4u{0u, 0u, ~t0, 0u};
        mark(po[i], w + 4 * i == s, tag);
        poff[i] = WS_PO + ((oh * 32 + w + 4 * i) * 64 + lane) * 16;
      }
      for (u32 sp = 0; sp < SPIN_CAP; ++sp) {
        u64 pm[8], pany = 0;
#pragma unroll
        for (int i = 0; i < 8; ++i) { pm[i] = __ballot(gtag(po[i]) != tag); pany |= pm[i]; }
        if (pany == 0) break;
        const bool few = __builtin_popcountll(pm[7]) <= FEW;
        if (GSL && !few && sp) __builtin_amdgcn_s_sleep(GSL);
#pragma unroll
        for (int i = 0; i < 7; ++i)
          if (!few && ((pm[7] >> i) & 1)) pm[i] = 0ull;
        reload(po, poff, pm, srd);
      }
      const v4u ml = po[7];
      STAMP(7);
      const float mi = __uint_as_float(ml[0]);
      float mx = fmaxf(mi, dpp<X1>(mi)); mx = fmaxf(mx, dpp<X2>(mx)); mx = fmaxf(mx, dpp<X7>(mx));
      const float wt = __builtin_amdgcn_exp2f(mi - mx);
      float lw = wt * __uint_as_float(ml[1]);
      lw += dpp<X1>(lw); lw += dpp<X2>(lw); lw += dpp<X7>(lw);
      v4f o = {0.f, 0.f, 0.f, 0.f};
#pragma unroll
      for (int i = 0; i < 7; ++i) {
        const float f = __builtin_bit_cast(float, __builtin_amdgcn_readlane(__builtin_bit_cast(int, wt), i));
        o[0] = fmaf(f, bflo(po[i][0]), o[0]); o[1] = fmaf(f, bfhi(po[i][0]), o[1]);
        o[2] = fmaf(f, bflo(po[i][1]), o[2]); o[3] = fmaf(f, bfhi(po[i][1]), o[3]);
      }
      *reinterpret_cast<v4f*>(&lds[L_CMB + w * 256 + lane * 4]) = o;
      if (lane == 0) *reinterpret_cast<v2f*>(&lds[L_CML + w * 2]) = v2f{mx, lw};
    } else if (w < 6) {
      const int hh = w - 4;
      for (u32 sp = 0; lflag[1] < 8u && sp < (SPIN_CAP << 4); ++sp) { }
      asm volatile("" ::: "memory");
      STAMP(5);
      v4f o = {0.f, 0.f, 0.f, 0.f}, o2 = {0.f, 0.f, 0.f, 0.f};
#pragma unroll
      for (int ww = 0; ww < 8; ++ww) {
        const v4f t = *reinterpret_cast<const v4f*>(&lds[L_ORED + (ww * 2 + hh) * 256 + lane * 4]);
        if (ww & 1) o2 += fw[ww] * t; else o += fw[ww] * t;
      }
      o += o2;
      const u32 c0 = cvtpk(o[0], o[1]), c1 = cvtpk(o[2], o[3]);
      stg(rs, WS_PO + (((2 * g + hh) * 32 + s) * 64 + lane) * 16, c0, c1, t0, t1);
      if (lane == 0) stg(rs, WS_ML + ((2 * g + hh) * 32 + s) * 128, __float_as_uint(Mh), __float_as_uint(L), t0, t1);
      if (hh == (oh & 1)) {
        *reinterpret_cast<v4f*>(&lds[L_CMB + 4 * 256 + lane * 4]) = v4f{bflo(c0), bfhi(c0), bflo(c1), bfhi(c1)};
        if (lane == 0) *reinterpret_cast<v2f*>(&lds[L_CML + 8]) = v2f{Mh, L};
      }
      STAMP(6);
    }
    pairup(wor[0], wor[1]); pairup(wor[2], wor[3]);
    asm volatile("" : "+v"(wor[0]), "+v"(wor[1]), "+v"(wor[2]), "+v"(wor[3]));
    bar();
    if (w >= 4) {
      v2f cm[5], co[5], c2[5];
#pragma unroll
      for (int i = 0; i < 5; ++i) {
        cm[i] = *reinterpret_cast<const v2f*>(&lds[L_CML + i * 2]);
        co[i] = *reinterpret_cast<const v2f*>(&lds[L_CMB + i * 256 + orow]);
        c2[i] = *reinterpret_cast<const v2f*>(&lds[L_CMB + i * 256 + orow + 32]);
      }
      const float Mx = fmaxf(fmaxf(fmaxf(cm[0][0], cm[1][0]), fmaxf(cm[2][0], cm[3][0])), cm[4][0]);
      float L = 0.f;
      v2f u = {0.f, 0.f}, u2 = {0.f, 0.f};
#pragma unroll
      for (int i = 0; i < 5; ++i) {
        const float f = __builtin_amdgcn_exp2f(cm[i][0] - Mx);
        L = fmaf(f, cm[i][1], L);
        u += f * co[i]; u2 += f * c2[i];
      }
      const float rl = rcp(L);
      float acc[8];
#pragma unroll
      for (int j = 0; j < 8; ++j) acc[j] = 0.f;
      fmap(acc, wor[0], wor[1], cvtpk(u[0] * rl, u[1] * rl));
      fmap(acc, wor[2], wor[3], cvtpk(u2[0] * rl, u2[1] * rl));
      float r4[4], r2[2];
#pragma unroll
      for (int j = 0; j < 4; ++j) r4[j] = sw32_sum(acc[j], acc[j + 4]);
#pragma unroll
      for (int j = 0; j < 2; ++j) r2[j] = tr_step<X7>(r4[j], r4[j + 2], (lane & 4) != 0);
      float r1 = tr_step<X2>(r2[0], r2[1], (lane & 2) != 0);
      r1 += dpp<X1>(r1);
      if ((lane & 1) == 0) lds[L_S1 + (w - 4) * 32 + (sub & 3) * 8 + (lane >> 5) * 4 + ((lane >> 2) & 1) * 2 + ((lane >> 1) & 1)] = r1;
    }
    bar();
    if (w == 0 && lane < 16) {
      v2f a[4];
#pragma unroll
      for (int ww = 0; ww < 4; ++ww) a[ww] = *reinterpret_cast<const v2f*>(&lds[L_S1 + ww * 32 + lane * 2]);
      const v2f t = (a[0] + a[1]) + (a[2] + a[3]);
      stg(rs, WS_PH + (oh * 224 + cb * 16 + lane) * 16, __float_as_uint(t[0]), __float_as_uint(t[1]), t0, t1);
    }
    STAMP(8);
    STAMP_FLUSH();
    return;
  }

  const int f = g * 4 + (s - NSP);
  const u64 tstart = __builtin_amdgcn_s_memrealtime();
  const bool ffn = f < 28;
  if (tid == 0) *(volatile LAS u32*)(&lds[L_FC]) = 0u;
  const int r4 = lane >> 4;
  const v4u xv = *reinterpret_cast<const v4u*>(x + (lane < 56 ? lane : 0) * 8);
  u32 xq[7];
  v4u wa[14], wf[14], wdn[8];
  {
    const int row0 = 56 * w + 14 * r4;
#pragma unroll
    for (int i = 0; i < 7; ++i) xq[i] = *reinterpret_cast<const u32*>(x + row0 + 2 * i);
    const u16* p = wq + (size_t)row0 * NQ + 128 * f + (lane & 15) * 8;
#pragma unroll
    for (int i = 0; i < 14; ++i) wa[i] = ldnt(p + (size_t)i * NQ);
    SB();
    bar();
    SB();
    const int fc = ffn ? f : 0;
    const u16* pf = (((lane >> 3) & 1) ? wu : wg) + (size_t)row0 * FF + 64 * fc + (lane & 7) * 8;
    float xf[8];
#pragma unroll
    for (int j = 0; j < 4; ++j) { xf[2 * j] = bflo(xv[j]); xf[2 * j + 1] = bfhi(xv[j]); }
    float sm = 0.f;
#pragma unroll
    for (int j = 0; j < 8; ++j) sm += xf[j];
    sm = wave_sum(lane < 56 ? sm : 0.f);
    const float mean = sm * (1.0f / D);
    float sq = 0.f;
#pragma unroll
    for (int j = 0; j < 8; ++j) { const float d = xf[j] - mean; sq += d * d; }
    sq = wave_sum(lane < 56 ? sq : 0.f);
    const float mb = rbf(mean), den = rbf(sqrtf(rbf(rbf(sq * (1.0f / D)) + 1e-5f)));
    float acc[8];
#pragma unroll
    for (int j = 0; j < 8; ++j) acc[j] = 0.f;
#pragma unroll
    for (int i = 0; i < 7; ++i) {
      const u32 n = cvtpk(rbf(bflo(xq[i]) - mb) / den, rbf(bfhi(xq[i]) - mb) / den);
      pairup(wa[2 * i], wa[2 * i + 1]);
      fmap(acc, wa[2 * i], wa[2 * i + 1], n);
    }
    float a4[4];
#pragma unroll
    for (int j = 0; j < 4; ++j) a4[j] = sw32_sum(acc[j], acc[j + 4]);
    v2f a2;
#pragma unroll
    for (int j = 0; j < 2; ++j) a2[j] = sw16_sum(a4[j], a4[j + 2]);
    *reinterpret_cast<v2f*>(&lds[L_QR + w * 128 + (lane & 15) * 8 + (lane >> 5) * 4 + ((lane >> 4) & 1) * 2]) = a2;
    bar();
    TAG();
    if (w == 0 && lane < 32) {
      v4f qv = *reinterpret_cast<const v4f*>(&lds[L_QR + lane * 4]);
#pragma unroll
      for (int ww = 1; ww < 8; ++ww) qv += *reinterpret_cast<const v4f*>(&lds[L_QR + ww * 128 + lane * 4]);
      stg(rs, WS_Q + (f * 32 + lane) * 16, cvtpk(qv[0], qv[1]), cvtpk(qv[2], qv[3]), t0, t1);
    }
    STAMP(1);
    if (!ffn) { STAMP_FLUSH(); return; }
    {
      const u64 tq = __builtin_amdgcn_s_memrealtime();
      const u64 hold = ((tq - tstart) * DEFER) >> 3;
      while (__builtin_amdgcn_s_memrealtime() - tq < hold) __builtin_amdgcn_s_sleep(4);
    }
    SB();
#pragma unroll
    for (int i = 0; i < 14; ++i) wf[i] = ldnt(pf + (size_t)i * FF);
    SB();
  }
  const int l56 = lane < 56 ? lane : 0;
  {
    const u16* pd = wd + (size_t)(64 * f + 8 * w) * D + 8 * l56;
#pragma unroll
    for (int i = 0; i < 8; ++i) wdn[i] = ldnt(pd + i * D);
    SB();
  }
#pragma unroll
  for (int i = 0; i < 7; ++i) { pairup(wf[2 * i], wf[2 * i + 1]); asm volatile("" : "+v"(wf[2 * i]), "+v"(wf[2 * i + 1])); }
#pragma unroll
  for (int i = 0; i < 4; ++i) { pairup(wdn[2 * i], wdn[2 * i + 1]); asm volatile("" : "+v"(wdn[2 * i]), "+v"(wdn[2 * i + 1])); }
  const int cu = (lane & 7) * 8 + (lane >> 5) * 4 + ((lane >> 4) & 1) * 2, mt = (lane >> 3) & 1;
  {
    float acc[8];
#pragma unroll
    for (int j = 0; j < 8; ++j) acc[j] = 0.f;
#pragma unroll
    for (int i = 0; i < 7; ++i) fmap(acc, wf[2 * i], wf[2 * i + 1], 0x3f803f80u);
    float a4[4];
#pragma unroll
    for (int j = 0; j < 4; ++j) a4[j] = sw32_sum(acc[j], acc[j + 4]);
#pragma unroll
    for (int j = 0; j < 2; ++j) lds[L_QR + w * 128 + (cu + j) * 2 + mt] = sw16_sum(a4[j], a4[j + 2]);
  }
  bar();
  const int u = 8 * w + (lane & 7);
  v2f SGU = {0.f, 0.f};
#pragma unroll
  for (int ww = 0; ww < 8; ++ww) SGU += *reinterpret_cast<const v2f*>(&lds[L_QR + ww * 128 + u * 2]);
  asm volatile("" : "+v"(SGU));
  {
    const u32 pt0 = __builtin_amdgcn_raw_buffer_load_b32(rs, WS_PD + (f * 224 + (tid < 224 ? tid : 0)) * 16, 0, AUX_SC1);
    const u32 pt1 = *reinterpret_cast<const volatile u32*>(out + 16 * f);
    asm volatile("" :: "v"(pt0), "v"(pt1));
  }
  TAG();
  STAMP(2);
  {
    const int hsub = lane >> 5;
    const bool act = (lane & 31) < 28;
    const int pp = act ? (lane & 31) : 0;
    int off[8];
    v4u gr[8];
    const u32 xp = *reinterpret_cast<const u32*>(x + 56 * w + 2 * pp);
    LAS u32* const xw = (LAS u32*)(&lds[L_XN + w * 32 + (pp / 7) * 8 + pp % 7]);
    const LAS v4u* const xr = (const LAS v4u*)(&lds[L_XN + w * 32 + r4 * 8]);
    const int cp = 28 * w + pp - 8 * f;
    const bool keep = lane < 28 && cp >= 0 && cp < 8;
    float acc[10];
#pragma unroll
    for (int j = 0; j < 10; ++j) { acc[j] = 0.f; asm volatile("" : "+v"(acc[j])); }
#pragma unroll
    for (int i = 0; i < 8; ++i) { off[i] = act ? WS_PH + ((2 * i + hsub) * 224 + 28 * w + pp) * 16 : OOB; gr[i] = ldg(rs, off[i]); }
#pragma unroll
    for (int i = 0; i < 8; ++i) mark(gr[i], !act, tag);
    poll<8, 64>(srd, off, gr, tag);
    STAMP(9);
    v2f hp[8];
#pragma unroll
    for (int i = 0; i < 8; ++i) hp[i] = v2f{__uint_as_float(gr[i][0]), __uint_as_float(gr[i][1])};
    const v2f hs = ((hp[0] + hp[1]) + (hp[2] + hp[3])) + ((hp[4] + hp[5]) + (hp[6] + hp[7]));
    const u32 a01 = cvtpk(sw32_sum(hs[0], hs[0]), sw32_sum(hs[1], hs[1]));
    const u32 h01 = cvtpk(bflo(xp) + bflo(a01), bfhi(xp) + bfhi(a01));
    if (lane < 28) *xw = h01;
    asm volatile("s_waitcnt lgkmcnt(0)" ::: "memory");
    const v4u na = xr[0], nb = xr[1];
    if (keep) reinterpret_cast<u32*>(&lds[L_HS])[cp] = h01;
#pragma unroll
    for (int i = 0; i < 7; ++i) {
      const u32 n = i < 4 ? na[i] : nb[i - 4];
#pragma unroll
      for (int j = 0; j < 4; ++j) { acc[j] = dot2(wf[2 * i][j], n, acc[j]); acc[4 + j] = dot2(wf[2 * i + 1][j], n, acc[4 + j]); }
      acc[8] = dot2(n, 0x3f803f80u, acc[8]);
      acc[9] = dot2(n, n, acc[9]);
    }
    float a4[4];
#pragma unroll
    for (int j = 0; j < 4; ++j) a4[j] = sw32_sum(acc[j], acc[j + 4]);
    float st = sw32_sum(acc[8], acc[9]);
    st = sw16_sum(st, st);
#pragma unroll
    for (int j = 0; j < 2; ++j) lds[L_GU + w * 128 + (cu + j) * 2 + mt] = sw16_sum(a4[j], a4[j + 2]);
    if ((lane & 31) == 0) lds[L_ST + w * 2 + hsub] = st;
  }
  float dacc[8];
#pragma unroll
  for (int j = 0; j < 8; ++j) { dacc[j] = 0.f; asm volatile("" : "+v"(dacc[j])); }
  bar();
  {
    v2f gu[8];
    v4f sv[4];
#pragma unroll
    for (int i = 0; i < 4; ++i) sv[i] = *reinterpret_cast<const v4f*>(&lds[L_ST + i * 4]);
#pragma unroll
    for (int ww = 0; ww < 8; ++ww) gu[ww] = *reinterpret_cast<const v2f*>(&lds[L_GU + ww * 128 + u * 2]);
    const v4f s4 = (sv[0] + sv[1]) + (sv[2] + sv[3]);
    const float mean = (s4[0] + s4[2]) * (1.0f / D);
    const float var = fmaxf(fmaf(-mean, mean, (s4[1] + s4[3]) * (1.0f / D)), 0.f);
    const float mb = rbf(mean), rden = rcp(rbf(__builtin_amdgcn_sqrtf(rbf(rbf(var) + 1e-5f))));
    const v2f GUs = ((gu[0] + gu[1]) + (gu[2] + gu[3])) + ((gu[4] + gu[5]) + (gu[6] + gu[7]));
    const v2f GUn = (GUs - mb * SGU) * rden;
    const u32 gub = cvtpk(GUn[0], GUn[1]);
    const float G = bflo(gub), U = bfhi(gub);
    const float z2 = G * fmaf(G * G, 0.044715f * 0.7978845608028654f * 2.0f * LOG2E, 0.7978845608028654f * 2.0f * LOG2E);
    const float sg = rcp(__builtin_amdgcn_exp2f(z2) + 1.0f);
    const float av = rbf(fmaf(-G, sg, G)) * U;
    const u32 pk = cvtpk(av, dpp<X1>(av));
#pragma unroll
    for (int i = 0; i < 4; ++i) fmap(dacc, wdn[2 * i], wdn[2 * i + 1], __builtin_amdgcn_readlane(pk, 2 * i));
    if (lane < 56) {
      *reinterpret_cast<v4f*>(&lds[L_DN + w * 448 + lane * 8]) = v4f{dacc[0], dacc[1], dacc[2], dacc[3]};
      *reinterpret_cast<v4f*>(&lds[L_DN + w * 448 + lane * 8 + 4]) = v4f{dacc[4], dacc[5], dacc[6], dacc[7]};
    }
  }
  bar();
  if (tid < 224) {
    v2f a[8];
#pragma unroll
    for (int ww = 0; ww < 8; ++ww) a[ww] = *reinterpret_cast<const v2f*>(&lds[L_DN + ww * 448 + tid * 2]);
    const v2f t = ((a[0] + a[1]) + (a[2] + a[3])) + ((a[4] + a[5]) + (a[6] + a[7]));
    stg(rs, WS_PD + (f * 224 + tid) * 16, __float_as_uint(t[0]), __float_as_uint(t[1]), t0, t1);
  }
  STAMP(10);
  if (w < 7) {
    int off[1];
    v4u gr[1];
    off[0] = lane < 32 ? WS_PD + ((4 * w + (lane >> 3)) * 224 + 8 * f + (lane & 7)) * 16 : OOB;
    gr[0] = ldg(rs, off[0]);
    mark(gr[0], lane >= 32, tag);
    poll<1, 64>(srd, off, gr, tag);
    STAMP(11);
    float a0 = __uint_as_float(gr[0][0]), a1 = __uint_as_float(gr[0][1]);
    float r = sw16_sum(a0, a1);
    r += dpp<X8>(r);
    if (lane < 32 && (lane & 8) == 0) lds[L_FN + w * 16 + (lane & 7) * 2 + (lane >> 4)] = r;
    asm volatile("s_waitcnt lgkmcnt(0)" ::: "memory");
    u32 old = 0;
    if (lane == 0) old = __hip_atomic_fetch_add((LAS u32*)(&lds[L_FC]), 1u, __ATOMIC_RELAXED, __HIP_MEMORY_SCOPE_WORKGROUP);
    if (__builtin_amdgcn_readfirstlane(old) == 6u) {
      asm volatile("" ::: "memory");
      if (lane < 8) {
        v2f a[7];
#pragma unroll
        for (int ww = 0; ww < 7; ++ww) a[ww] = *reinterpret_cast<const v2f*>(&lds[L_FN + ww * 16 + lane * 2]);
        const v2f t = ((a[0] + a[1]) + (a[2] + a[3])) + ((a[4] + a[5]) + a[6]);
        const u32 hv = reinterpret_cast<const u32*>(&lds[L_HS])[lane];
        const u32 tb = cvtpk(t[0], t[1]);
        *reinterpret_cast<u32*>(out + 16 * f + lane * 2) = cvtpk(bflo(hv) + bflo(tb), bfhi(hv) + bfhi(tb));
        if (f == 0 && lane == 0) __builtin_amdgcn_raw_buffer_store_b64(v2u{t0, t1}, rs, WS_EPOCH, 0, AUX_SC1);
      }
    }
  }
  STAMP(12);
  STAMP_FLUSH();
}

}

extern "C" void kernel_launch(void* const* d_in, const int* in_sizes, int n_in, void* d_out, int out_size, void* d_ws,
                              size_t ws_size, hipStream_t stream) {
  fused_decode_block<<<256, 512, 0, stream>>>((const u16*)d_in[0], (const u16*)d_in[1], (const u16*)d_in[2], (const u16*)d_in[3],
                                              (const u16*)d_in[4], (const u16*)d_in[6], (const u16*)d_in[7], (const u16*)d_in[8],
                                              (u16*)d_out, (unsigned char*)d_ws);
}
```

```cpp
#include <hip/hip_runtime.h>
#include <hip/hip_bf16.h>
#include <stdint.h>

typedef unsigned short u16;
typedef unsigned int u32;
typedef unsigned long long u64;
typedef u32 v4u __attribute__((ext_vector_type(4)));
typedef u32 v2u __attribute__((ext_vector_type(2)));
typedef float v4f __attribute__((ext_vector_type(4)));
typedef float v2f __attribute__((ext_vector_type(2)));
typedef __bf16 bf16x2 __attribute__((ext_vector_type(2)));
typedef __bf16 bf16x8 __attribute__((ext_vector_type(8)));
typedef short s16x4 __attribute__((ext_vector_type(4)));

#define LAS __attribute__((address_space(3)))
#define PRETOUCH 1
#define FEW 4
#define GSL 4
#define DIAG 0

namespace {

constexpr int D = 448, SK = 8192, DH = 256, FF = 1792, NQ = 4096, NSP = 28;
constexpr int WS_EPOCH = 0;
constexpr int WS_Q = 4096;
constexpr int WS_ML = 32768;
constexpr int WS_PO = 131072;
constexpr int WS_PH = WS_PO + 524288;
constexpr int WS_PD = WS_PH + 65536;
constexpr int WS_END = WS_PD + 28 * 3584;
constexpr int WS_STAMP = 1 << 20;
constexpr int AUX_SC1 = 16, AUX_NT = 2;
constexpr int OOB = 0x7ffffff0;
constexpr u32 SPIN_CAP = 1u << 17;
constexpr float LOG2E = 1.4426950408889634f;

constexpr int L_QS = 0;
constexpr int L_WMAX = 256;
constexpr int L_WSUM = 272;
constexpr int L_S1 = 288;
constexpr int L_FLAG = 544;
constexpr int L_CML = 548;
constexpr int L_ORED = 576;
constexpr int L_P = L_ORED + 2048;
constexpr int L_CMB = L_P + 2560;
constexpr int L_QST = L_CMB + 5 * 256;
constexpr int L_END = L_QST + 4 * 512;
constexpr int L_QR = 0;
constexpr int L_ST = 1024;
constexpr int L_XN = 1040;
constexpr int L_HS = 1296;
constexpr int L_GU = 1312;
constexpr int L_DN = 2336;
constexpr int L_FN = 0;
constexpr int L_FC = 5920;

__device__ __forceinline__ float bflo(u32 u) { return __uint_as_float(u << 16); }
__device__ __forceinline__ float bfhi(u32 u) { return __uint_as_float(u & 0xffff0000u); }
__device__ __forceinline__ u32 cvtpk(float lo, float hi) {
  v2f v = {lo, hi};
  return __builtin_bit_cast(u32, __builtin_convertvector(v, bf16x2));
}
__device__ __forceinline__ float rbf(float f) { return bflo(cvtpk(f, 0.f)); }
__device__ __forceinline__ float dot2(u32 w, u32 a, float acc) {
  return __builtin_amdgcn_fdot2_f32_bf16(__builtin_bit_cast(bf16x2, w), __builtin_bit_cast(bf16x2, a), acc, false);
}
__device__ __forceinline__ float bsel(u32 m, float a, float b) {
  return __uint_as_float((__float_as_uint(a) & m) | (__float_as_uint(b) & ~m));
}
template <int CTRL> __device__ __forceinline__ float dpp(float x) {
  return __builtin_bit_cast(float, __builtin_amdgcn_mov_dpp(__builtin_bit_cast(int, x), CTRL, 0xf, 0xf, true));
}
constexpr int X1 = 0xB1, X2 = 0x4E, X7 = 0x141, X8 = 0x128;
__device__ __forceinline__ float sw32_sum(float a, float b) {
  auto r = __builtin_amdgcn_permlane32_swap(__float_as_uint(a), __float_as_uint(b), false, false);
  return __uint_as_float(r[0]) + __uint_as_float(r[1]);
}
__device__ __forceinline__ float sw16_sum(float a, float b) {
  auto r = __builtin_amdgcn_permlane16_swap(__float_as_uint(a), __float_as_uint(b), false, false);
  return __uint_as_float(r[0]) + __uint_as_float(r[1]);
}
__device__ __forceinline__ float sw32_max(float a) {
  auto r = __builtin_amdgcn_permlane32_swap(__float_as_uint(a), __float_as_uint(a), false, false);
  return fmaxf(__uint_as_float(r[0]), __uint_as_float(r[1]));
}
__device__ __forceinline__ float sw16_max(float a) {
  auto r = __builtin_amdgcn_permlane16_swap(__float_as_uint(a), __float_as_uint(a), false, false);
  return fmaxf(__uint_as_float(r[0]), __uint_as_float(r[1]));
}
template <int CTRL> __device__ __forceinline__ float tr_step(float lo, float hi, bool sel) {
  const float t1 = lo + dpp<CTRL>(lo), t2 = hi + dpp<CTRL>(hi);
  return sel ? t2 : t1;
}
__device__ __forceinline__ float wave_sum(float x) {
  x += dpp<X1>(x); x += dpp<X2>(x); x += dpp<X7>(x); x += dpp<X8>(x);
  x = sw16_sum(x, x); return sw32_sum(x, x);
}
__device__ __forceinline__ float half_sum(float x) {
  x += dpp<X1>(x); x += dpp<X2>(x); x += dpp<X7>(x); x += dpp<X8>(x);
  return sw16_sum(x, x);
}
__device__ __forceinline__ v4u ldnt(const u16* p) { return __builtin_nontemporal_load(reinterpret_cast<const v4u*>(p)); }
__device__ __forceinline__ v4u ldb(__amdgpu_buffer_rsrc_t rs, int off) { return __builtin_amdgcn_raw_buffer_load_b128(rs, off, 0, AUX_NT); }
__device__ __forceinline__ v4u ldg(__amdgpu_buffer_rsrc_t rs, int off) { return __builtin_amdgcn_raw_buffer_load_b128(rs, off, 0, AUX_SC1); }
__device__ __forceinline__ void stg(__amdgpu_buffer_rsrc_t rs, int off, u32 a, u32 b, u32 t0, u32 t1) {
  v4u v = {a, b, t0, t1};
  __builtin_amdgcn_raw_buffer_store_b128(v, rs, off, 0, AUX_SC1);
}
__device__ __forceinline__ void bar() {
  asm volatile("s_waitcnt lgkmcnt(0)" ::: "memory");
  __builtin_amdgcn_s_barrier();
  asm volatile("" ::: "memory");
}
__device__ __forceinline__ v4f mfma(v4u a, v4u b, v4f c) {
  return __builtin_amdgcn_mfma_f32_16x16x32_bf16(__builtin_bit_cast(bf16x8, a), __builtin_bit_cast(bf16x8, b), c, 0, 0, 0);
}
__device__ __forceinline__ v4f mfma16(v2u a, v2u b, v4f c) {
  return __builtin_amdgcn_mfma_f32_16x16x16bf16_1k(__builtin_bit_cast(s16x4, a), __builtin_bit_cast(s16x4, b), c, 0, 0, 0);
}
__device__ __forceinline__ u32 plo(u32 r0, u32 r1) { return __builtin_amdgcn_perm(r1, r0, 0x05040100u); }
__device__ __forceinline__ u32 phi(u32 r0, u32 r1) { return __builtin_amdgcn_perm(r1, r0, 0x07060302u); }
__device__ __forceinline__ void pairup(v4u& a, v4u& b) {
  v4u x, y;
  x[0] = plo(a[0], b[0]); x[1] = phi(a[0], b[0]); x[2] = plo(a[1], b[1]); x[3] = phi(a[1], b[1]);
  y[0] = plo(a[2], b[2]); y[1] = phi(a[2], b[2]); y[2] = plo(a[3], b[3]); y[3] = phi(a[3], b[3]);
  a = x; b = y;
}
__device__ __forceinline__ void fmap(float (&acc)[8], v4u a, v4u b, u32 n) {
#pragma unroll
  for (int j = 0; j < 4; ++j) { acc[j] = dot2(a[j], n, acc[j]); acc[4 + j] = dot2(b[j], n, acc[4 + j]); }
}
__device__ __forceinline__ void fma8(float (&acc)[8], v4u wv, u32 alo, u32 ahi) {
#pragma unroll
  for (int j = 0; j < 4; ++j) { acc[2 * j] = dot2(wv[j], alo, acc[2 * j]); acc[2 * j + 1] = dot2(wv[j], ahi, acc[2 * j + 1]); }
}
__device__ __forceinline__ u64 gtag(const v4u& g) { return ((u64)g[3] << 32) | (u64)g[2]; }
__device__ __forceinline__ void mark(v4u& g, bool unused, u64 tag) {
  g[2] = unused ? (u32)tag : g[2]; g[3] = unused ? (u32)(tag >> 32) : g[3];
}
__device__ __forceinline__ void reload(v4u (&g)[1], const int (&o)[1], const u64 (&m)[1], v4u srd) {
  asm volatile("s_mov_b64 exec, %2\n\tbuffer_load_dwordx4 %0, %1, %3, 0 offen sc1\n\ts_mov_b64 exec, -1\n\ts_waitcnt vmcnt(0)"
               : "+v"(g[0]) : "v"(o[0]), "s"(m[0]), "s"(srd) : "memory");
}
__device__ __forceinline__ void reload(v4u (&g)[2], const int (&o)[2], const u64 (&m)[2], v4u srd) {
  asm volatile("s_mov_b64 exec, %4\n\tbuffer_load_dwordx4 %0, %2, %6, 0 offen sc1\n\t"
               "s_mov_b64 exec, %5\n\tbuffer_load_dwordx4 %1, %3, %6, 0 offen sc1\n\t"
               "s_mov_b64 exec, -1\n\ts_waitcnt vmcnt(0)"
               : "+v"(g[0]), "+v"(g[1])
               : "v"(o[0]), "v"(o[1]), "s"(m[0]), "s"(m[1]), "s"(srd) : "memory");
}
__device__ __forceinline__ void reload(v4u (&g)[4], const int (&o)[4], const u64 (&m)[4], v4u srd) {
  asm volatile("s_mov_b64 exec, %8\n\tbuffer_load_dwordx4 %0, %4, %12, 0 offen sc1\n\t"
               "s_mov_b64 exec, %9\n\tbuffer_load_dwordx4 %1, %5, %12, 0 offen sc1\n\t"
               "s_mov_b64 exec, %10\n\tbuffer_load_dwordx4 %2, %6, %12, 0 offen sc1\n\t"
               "s_mov_b64 exec, %11\n\tbuffer_load_dwordx4 %3, %7, %12, 0 offen sc1\n\t"
               "s_mov_b64 exec, -1\n\ts_waitcnt vmcnt(0)"
               : "+v"(g[0]), "+v"(g[1]), "+v"(g[2]), "+v"(g[3])
               : "v"(o[0]), "v"(o[1]), "v"(o[2]), "v"(o[3]), "s"(m[0]), "s"(m[1]), "s"(m[2]), "s"(m[3]), "s"(srd) : "memory");
}
__device__ __forceinline__ void reload(v4u (&g)[8], const int (&o)[8], const u64 (&m)[8], v4u srd) {
  asm volatile("s_mov_b64 exec, %16\n\tbuffer_load_dwordx4 %0, %8, %24, 0 offen sc1\n\t"
               "s_mov_b64 exec, %17\n\tbuffer_load_dwordx4 %1, %9, %24, 0 offen sc1\n\t"
               "s_mov_b64 exec, %18\n\tbuffer_load_dwordx4 %2, %10, %24, 0 offen sc1\n\t"
               "s_mov_b64 exec, %19\n\tbuffer_load_dwordx4 %3, %11, %24, 0 offen sc1\n\t"
               "s_mov_b64 exec, %20\n\tbuffer_load_dwordx4 %4, %12, %24, 0 offen sc1\n\t"
               "s_mov_b64 exec, %21\n\tbuffer_load_dwordx4 %5, %13, %24, 0 offen sc1\n\t"
               "s_mov_b64 exec, %22\n\tbuffer_load_dwordx4 %6, %14, %24, 0 offen sc1\n\t"
               "s_mov_b64 exec, %23\n\tbuffer_load_dwordx4 %7, %15, %24, 0 offen sc1\n\t"
               "s_mov_b64 exec, -1\n\ts_waitcnt vmcnt(0)"
               : "+v"(g[0]), "+v"(g[1]), "+v"(g[2]), "+v"(g[3]), "+v"(g[4]), "+v"(g[5]), "+v"(g[6]), "+v"(g[7])
               : "v"(o[0]), "v"(o[1]), "v"(o[2]), "v"(o[3]), "v"(o[4]), "v"(o[5]), "v"(o[6]), "v"(o[7]), "s"(m[0]), "s"(m[1]), "s"(m[2]), "s"(m[3]), "s"(m[4]), "s"(m[5]), "s"(m[6]), "s"(m[7]), "s"(srd) : "memory");
}
template <int N, int MANY> __device__ __forceinline__ u32 poll(v4u srd, const int (&off)[N], v4u (&gr)[N], u64 tag) {
  u32 spins = 0;
  u64 m[N], any = 0;
#pragma unroll
  for (int i = 0; i < N; ++i) { m[i] = __ballot(gtag(gr[i]) != tag); any |= m[i]; }
  if (__builtin_expect(any != 0, 0)) {
    do {
      if (MANY < 64 && __builtin_popcountll(any) > MANY) __builtin_amdgcn_s_sleep(8);
      reload(gr, off, m, srd);
      any = 0;
#pragma unroll
      for (int i = 0; i < N; ++i) { m[i] = __ballot(gtag(gr[i]) != tag); any |= m[i]; }
    } while (any != 0 && ++spins < SPIN_CAP);
    ++spins;
  }
  return spins;
}
__device__ __forceinline__ float rcp(float x) { return __builtin_amdgcn_rcpf(x); }
#define STAMP(i) do { } while (0)
#define STAMP_FLUSH() do { } while (0)
#define SB() __builtin_amdgcn_sched_barrier(0)
#define TAG() const u64 tag = (((u64)(u32)__builtin_amdgcn_readfirstlane(ep[1]) << 32) | (u64)(u32)__builtin_amdgcn_readfirstlane(ep[0])) + 1; \
  const u32 t0 = (u32)tag, t1 = (u32)(tag >> 32)

__global__ void __launch_bounds__(512, 2)
fused_decode_block(const u16* __restrict__ x, const u16* __restrict__ Kc, const u16* __restrict__ Vc, const u16* __restrict__ wq,
                   const u16* __restrict__ wo, const u16* __restrict__ wg, const u16* __restrict__ wu, const u16* __restrict__ wd,
                   u16* __restrict__ out, unsigned char* ws) {
  __shared__ __attribute__((aligned(16))) float lds[L_END];
  asm volatile("" :: "s"(x), "s"(Kc), "s"(Vc), "s"(wq), "s"(wo), "s"(wg), "s"(wu), "s"(wd), "s"(out), "s"(ws));
  const int tid = threadIdx.x, lane = tid & 63;
  const int w = __builtin_amdgcn_readfirstlane(tid >> 6);
  const int g = blockIdx.x & 7, s = ((blockIdx.x >> 3) + 28) & 31, b = s * 8 + g;
  const __amdgpu_buffer_rsrc_t rs = __builtin_amdgcn_make_buffer_rsrc((void*)ws, 0, WS_END, 0x00020000);
  const v4u srd = {(u32)(uintptr_t)ws, (u32)((uintptr_t)ws >> 32) & 0xffffu, (u32)WS_END, 0x00020000u};
  STAMP(0);
  const v2u ep = __builtin_amdgcn_raw_buffer_load_b64(rs, WS_EPOCH, 0, AUX_SC1);

  if (s < NSP) {
    volatile LAS u32* lflag = (volatile LAS u32*)(&lds[L_FLAG]);
    if (tid == 0) { lflag[0] = 0u; lflag[1] = 0u; lflag[2] = 0u; lflag[3] = 0u; }
    __builtin_amdgcn_s_setprio(1);
    bar();
    const int g4 = lane >> 4, c = lane & 15;
    const int tile0 = (256 * s) / 7, nx = (256 * (s + 1)) / 7 - tile0 - 36;
    const int wi = w & 3;
    const bool ten = wi < nx;
    const int key0 = (tile0 + 9 * wi + (wi ? nx : 0)) * 8;
    const int oh = 2 * g + s / 14, cb = s % 14;
    const int sub = lane >> 3, gq = lane & 7;
    const int orow = 64 * wi + 4 * gq + 2 * (lane >> 5);
    v4u wor[4];
    if (w < 4) {
      const __amdgpu_buffer_rsrc_t rk = __builtin_amdgcn_make_buffer_rsrc((void*)Kc, 0, 8 * SK * DH * 2, 0x00020000);
      v4u kr[40];
      const int kb = ((g * SK + key0 + (c >> 1)) * DH + (c & 1) * 32 + g4 * 8) * 2;
#pragma unroll
      for (int t = 0; t < 9; ++t)
#pragma unroll
        for (int ks = 0; ks < 4; ++ks) kr[t * 4 + ks] = ldb(rk, kb + t * 4096 + ks * 128);
#pragma unroll
      for (int ks = 0; ks < 4; ++ks) kr[36 + ks] = ldb(rk, ten ? kb + 9 * 4096 + ks * 128 : OOB);
      SB();
      STAMP(1);
      TAG();
      {
        __builtin_amdgcn_s_setprio(3);
        const int qo0 = WS_Q + (g * 128 + lane) * 16;
        const u32 st0 = (u32)(uintptr_t)(LAS float*)(&lds[L_QST + w * 512]);
        const u32 fl = (u32)(uintptr_t)(LAS float*)(&lds[L_FLAG]);
#define Q_SAMPLE(pend) do { u32 keep_, tmp_, vt_; \
          asm volatile("s_mov_b32 %1, m0\n\t" \
                       "s_mov_b32 m0, %5\n\ts_nop 0\n\tbuffer_load_dwordx4 %4, %6, 0 offen sc1 lds\n\t" \
                       "buffer_load_dwordx4 %4, %6, 0 offen offset:1024 sc1 lds\n\t"   \
                       "s_mov_b32 m0, %1\n" \
                       "1:\n\t" \
                       "s_getreg_b32 %0, hwreg(HW_REG_IB_STS)\n\ts_and_b32 %0, %0, 0xc0000f\n\ts_cbranch_scc0 2f\n\t" \
                       "ds_read_b32 %3, %7\n\ts_waitcnt lgkmcnt(0)\n\tv_readfirstlane_b32 %2, %3\n\ts_cmp_eq_u32 %2, 0\n\ts_cbranch_scc1 1b\n" \
                       "2:" \
                       : "=&s"(pend), "=&s"(keep_), "=&s"(tmp_), "=&v"(vt_) : "v"(qo0), "s"(st0), "s"(srd), "v"(fl) : "memory", "scc"); } while (0)
        u32 pend;
        Q_SAMPLE(pend);
        __builtin_amdgcn_s_waitcnt(0x0F70);
        pend = 0u;
        for (u32 sp = 0; pend == 0u && lflag[0] == 0u && sp < SPIN_CAP; ++sp) {
          asm volatile("" ::: "memory");
          const v4u q0 = *reinterpret_cast<const v4u*>(&lds[L_QST + w * 512 + lane * 4]);
          const v4u q1 = *reinterpret_cast<const v4u*>(&lds[L_QST + w * 512 + 256 + lane * 4]);
          if (__ballot(gtag(q0) != tag || gtag(q1) != tag) == 0) {
            *reinterpret_cast<v2u*>(&lds[L_QS + lane * 2]) = v2u{q0[0], q0[1]};
            *reinterpret_cast<v2u*>(&lds[L_QS + (64 + lane) * 2]) = v2u{q1[0], q1[1]};
            asm volatile("s_waitcnt lgkmcnt(0)" ::: "memory");
            lflag[0] = 1u;
            break;
          }
          Q_SAMPLE(pend);
        }
        __builtin_amdgcn_s_setprio(1);
      }
      asm volatile("" ::: "memory");
      STAMP(2);
      const int hd = (c >> 2) & 1;
      const u32 meta = ((c >> 1) & 1) ? 0xffffffffu : 0u, mvar = (c & 1) ? 0xffffffffu : 0u;
      float sc[20];
      {
        v4u qf[4];
#pragma unroll
        for (int ks = 0; ks < 4; ++ks) qf[ks] = *reinterpret_cast<const v4u*>(&lds[L_QS + hd * 128 + ks * 32 + ((c >> 1) & 1) * 16 + g4 * 4]);
#pragma unroll
        for (int t = 0; t < 10; ++t) {
          v4f d = {0.f, 0.f, 0.f, 0.f};
#pragma unroll
          for (int ks = 0; ks < 4; ++ks) d = mfma(kr[t * 4 + ks], qf[ks], d);
          const float sa = bsel(meta, d[1], d[0]), sb = bsel(meta, d[3], d[2]);
          const u32 sp = cvtpk(sa + dpp<X2>(sa), sb + dpp<X2>(sb));
          sc[2 * t] = bflo(sp) * (0.0625f * LOG2E);
          sc[2 * t + 1] = bfhi(sp) * (0.0625f * LOG2E);
        }
        if (!ten) { sc[18] = -INFINITY; sc[19] = -INFINITY; }
      }
      float m = sc[0];
#pragma unroll
      for (int j = 1; j < 20; ++j) m = fmaxf(m, sc[j]);
      m = sw32_max(sw16_max(m));
      if ((lane & 0x3b) == 0) lds[L_WMAX + w * 2 + hd] = m;
      asm volatile("s_waitcnt lgkmcnt(0)" ::: "memory");
      if (lane == 0) __hip_atomic_fetch_add((LAS u32*)(&lds[L_FLAG + 2]), 1u, __ATOMIC_RELAXED, __HIP_MEMORY_SCOPE_WORKGROUP);
      for (u32 sp = 0; lflag[2] < 4u && sp < (SPIN_CAP << 4); ++sp) __builtin_amdgcn_s_sleep(1);
      asm volatile("" ::: "memory");
      m = fmaxf(fmaxf(lds[L_WMAX + hd], lds[L_WMAX + 2 + hd]), fmaxf(lds[L_WMAX + 4 + hd], lds[L_WMAX + 6 + hd]));
      {
        float l = 0.f;
        LAS u32* const pw = (LAS u32*)(&lds[L_P + w * 640 + lane]);
#pragma unroll
        for (int t = 0; t < 10; ++t) {
          const float p0 = __builtin_amdgcn_exp2f(sc[2 * t] - m), p1 = __builtin_amdgcn_exp2f(sc[2 * t + 1] - m);
          l += p0 + p1;
          const u32 hi = cvtpk(p0, p1);
          const u32 lo = cvtpk(p0 - bflo(hi), p1 - bfhi(hi));
          pw[t * 64] = (lo & mvar) | (hi & ~mvar);
        }
        l = sw16_sum(l, l); l = sw32_sum(l, l);
        if ((lane & 0x3b) == 0) lds[L_WSUM + w * 2 + hd] = l;
        asm volatile("s_waitcnt lgkmcnt(0)" ::: "memory");
        if (lane == 0) __hip_atomic_fetch_add((LAS u32*)(&lds[L_FLAG + 3]), 1u, __ATOMIC_RELAXED, __HIP_MEMORY_SCOPE_WORKGROUP);
      }
      STAMP(4);
      __builtin_amdgcn_s_setprio(0);
      v4u po[8];
      int poff[8];
      const int mysp = w + 4 * (lane & 7);
      const bool mlwant = lane < 7 && mysp != s;
      poff[7] = WS_ML + (oh * 32 + mysp) * 128;
      po[7] = v4u{mlwant ? 0u : 0xff800000u, 0u, ~t0, 0u};
      mark(po[7], !mlwant, tag);
#pragma unroll
      for (int i = 0; i < 7; ++i) {
        po[i] = v4u{0u, 0u, ~t0, 0u};
        mark(po[i], w + 4 * i == s, tag);
        poff[i] = WS_PO + ((oh * 32 + w + 4 * i) * 64 + lane) * 16;
      }
      for (u32 sp = 0; sp < SPIN_CAP; ++sp) {
        u64 pm[8], pany = 0;
#pragma unroll
        for (int i = 0; i < 8; ++i) { pm[i] = __ballot(gtag(po[i]) != tag); pany |= pm[i]; }
        if (pany == 0) break;
        const bool few = __builtin_popcountll(pm[7]) <= FEW;
        if (GSL && !few && sp) __builtin_amdgcn_s_sleep(GSL);
#pragma unroll
        for (int i = 0; i < 7; ++i)
          if (!few && ((pm[7] >> i) & 1)) pm[i] = 0ull;
        reload(po, poff, pm, srd);
      }
      const v4u ml = po[7];
      STAMP(7);
      const float mi = __uint_as_float(ml[0]);
      float mx = fmaxf(mi, dpp<X1>(mi)); mx = fmaxf(mx, dpp<X2>(mx)); mx = fmaxf(mx, dpp<X7>(mx));
      const float wt = __builtin_amdgcn_exp2f(mi - mx);
      float lw = wt * __uint_as_float(ml[1]);
      lw += dpp<X1>(lw); lw += dpp<X2>(lw); lw += dpp<X7>(lw);
      v4f o = {0.f, 0.f, 0.f, 0.f};
#pragma unroll
      for (int i = 0; i < 7; ++i) {
        const float f = __builtin_bit_cast(float, __builtin_amdgcn_readlane(__builtin_bit_cast(int, wt), i));
        o[0] = fmaf(f, bflo(po[i][0]), o[0]); o[1] = fmaf(f, bfhi(po[i][0]), o[1]);
        o[2] = fmaf(f, bflo(po[i][1]), o[2]); o[3] = fmaf(f, bfhi(po[i][1]), o[3]);
      }
      *reinterpret_cast<v4f*>(&lds[L_CMB + w * 256 + lane * 4]) = o;
      if (lane == 0) *reinterpret_cast<v2f*>(&lds[L_CML + w * 2]) = v2f{mx, lw};
#pragma unroll
      for (int i = 0; i < 4; ++i) wor[i] = v4u{0u, 0u, 0u, 0u};
    } else {
      const __amdgpu_buffer_rsrc_t rv = __builtin_amdgcn_make_buffer_rsrc((void*)Vc, 0, 8 * SK * DH * 2, 0x00020000);
      const __amdgpu_buffer_rsrc_t ro = __builtin_amdgcn_make_buffer_rsrc((void*)wo, 0, NQ * D * 2, 0x00020000);
      v4u vr[40];
      const int vb = ((g * SK + key0 + 2 * g4) * DH + 8 * c) * 2;
#pragma unroll
      for (int hf = 0; hf < 2; ++hf) {
#pragma unroll
        for (int j = 0; j < 18; ++j) vr[hf * 20 + j] = ldb(rv, vb + (8 * (j >> 1) + (j & 1)) * 512 + 256 * hf);
#pragma unroll
        for (int j = 18; j < 20; ++j) vr[hf * 20 + j] = ldb(rv, ten ? vb + (72 + (j & 1)) * 512 + 256 * hf : OOB);
        SB();
      }
      {
        const int oo = ((oh * 256 + orow) * D + 32 * cb + 8 * (sub & 3)) * 2;
        wor[0] = ldb(ro, oo); wor[1] = ldb(ro, oo + D * 2); wor[2] = ldb(ro, oo + 32 * D * 2); wor[3] = ldb(ro, oo + 33 * D * 2);
        SB();
      }
      STAMP(1);
      TAG();
      for (u32 sp = 0; lflag[3] < 4u && sp < (SPIN_CAP << 4); ++sp) __builtin_amdgcn_s_sleep(1);
      asm volatile("" ::: "memory");
      STAMP(2);
      const u32 mhi = (g4 >> 1) ? 0xffffffffu : 0u;
      v4u pa0, pa1;
      v2u pa2;
      {
        const LAS u32* const pr = (const LAS u32*)(&lds[L_P + wi * 640 + lane]);
#pragma unroll
        for (int t = 0; t < 4; ++t) { pa0[t] = pr[t * 64]; pa1[t] = pr[(4 + t) * 64]; }
        pa2[0] = pr[8 * 64]; pa2[1] = pr[9 * 64];
      }
#pragma unroll
      for (int hf = 0; hf < 2; ++hf) {
        v4f d1[8];
#pragma unroll
        for (int kk = 0; kk < 2; ++kk) {
          v4u bb[8];
#pragma unroll
          for (int t = 0; t < 4; ++t)
#pragma unroll
            for (int d = 0; d < 8; ++d)
              bb[d][t] = __builtin_amdgcn_perm(vr[hf * 20 + kk * 8 + 2 * t + 1][d >> 1], vr[hf * 20 + kk * 8 + 2 * t][d >> 1], (d & 1) ? 0x07060302u : 0x05040100u);
          SB();
#pragma unroll
          for (int d = 0; d < 8; ++d) d1[d] = mfma(kk ? pa1 : pa0, bb[d], kk ? d1[d] : v4f{0.f, 0.f, 0.f, 0.f});
          SB();
        }
        v2u b2[8];
#pragma unroll
        for (int t = 0; t < 2; ++t)
#pragma unroll
          for (int d = 0; d < 8; ++d)
            b2[d][t] = __builtin_amdgcn_perm(vr[hf * 20 + 16 + 2 * t + 1][d >> 1], vr[hf * 20 + 16 + 2 * t][d >> 1], (d & 1) ? 0x07060302u : 0x05040100u);
        SB();
        v4f d2[8];
#pragma unroll
        for (int d = 0; d < 8; ++d) d2[d] = mfma16(pa2, b2[d], v4f{0.f, 0.f, 0.f, 0.f});
        float x1[4];
#pragma unroll
        for (int e = 0; e < 4; ++e) x1[e] = bsel(mhi, d1[4 + e][0] + d1[4 + e][1], d1[e][0] + d1[e][1]);
        SB();
        v4f y;
#pragma unroll
        for (int e = 0; e < 4; ++e) y[e] = x1[e] + bsel(mhi, d2[4 + e][0] + d2[4 + e][1], d2[e][0] + d2[e][1]);
        *reinterpret_cast<v4f*>(&lds[L_ORED + (wi * 2 + (g4 & 1)) * 256 + 128 * hf + 8 * c + 4 * (g4 >> 1)]) = y;
      }
      STAMP(4);
      asm volatile("s_waitcnt lgkmcnt(0)" ::: "memory");
      if (lane == 0) __hip_atomic_fetch_add((LAS u32*)(&lds[L_FLAG + 1]), 1u, __ATOMIC_RELAXED, __HIP_MEMORY_SCOPE_WORKGROUP);
      if (w < 6) {
        const int hh = w - 4;
        const float Mh = fmaxf(fmaxf(lds[L_WMAX + hh], lds[L_WMAX + 2 + hh]), fmaxf(lds[L_WMAX + 4 + hh], lds[L_WMAX + 6 + hh]));
        const float L = (lds[L_WSUM + hh] + lds[L_WSUM + 2 + hh]) + (lds[L_WSUM + 4 + hh] + lds[L_WSUM + 6 + hh]);
        for (u32 sp = 0; lflag[1] < 4u && sp < (SPIN_CAP << 4); ++sp) { }
        asm volatile("" ::: "memory");
        STAMP(5);
        v4f t4[4];
#pragma unroll
        for (int ww = 0; ww < 4; ++ww) t4[ww] = *reinterpret_cast<const v4f*>(&lds[L_ORED + (ww * 2 + hh) * 256 + lane * 4]);
        const v4f o = (t4[0] + t4[1]) + (t4[2] + t4[3]);
        const u32 c0 = cvtpk(o[0], o[1]), c1 = cvtpk(o[2], o[3]);
        stg(rs, WS_PO + (((2 * g + hh) * 32 + s) * 64 + lane) * 16, c0, c1, t0, t1);
        if (lane == 0) stg(rs, WS_ML + ((2 * g + hh) * 32 + s) * 128, __float_as_uint(Mh), __float_as_uint(L), t0, t1);
        if (hh == (oh & 1)) {
          *reinterpret_cast<v4f*>(&lds[L_CMB + 4 * 256 + lane * 4]) = v4f{bflo(c0), bfhi(c0), bflo(c1), bfhi(c1)};
          if (lane == 0) *reinterpret_cast<v2f*>(&lds[L_CML + 8]) = v2f{Mh, L};
        }
        STAMP(6);
      }
      pairup(wor[0], wor[1]); pairup(wor[2], wor[3]);
      asm volatile("" : "+v"(wor[0]), "+v"(wor[1]), "+v"(wor[2]), "+v"(wor[3]));
    }
    TAG();
    bar();
    if (w >= 4) {
      v2f cm[5], co[5], c2[5];
#pragma unroll
      for (int i = 0; i < 5; ++i) {
        cm[i] = *reinterpret_cast<const v2f*>(&lds[L_CML + i * 2]);
        co[i] = *reinterpret_cast<const v2f*>(&lds[L_CMB + i * 256 + orow]);
        c2[i] = *reinterpret_cast<const v2f*>(&lds[L_CMB + i * 256 + orow + 32]);
      }
      const float Mx = fmaxf(fmaxf(fmaxf(cm[0][0], cm[1][0]), fmaxf(cm[2][0], cm[3][0])), cm[4][0]);
      float L = 0.f;
      v2f u = {0.f, 0.f}, u2 = {0.f, 0.f};
#pragma unroll
      for (int i = 0; i < 5; ++i) {
        const float f = __builtin_amdgcn_exp2f(cm[i][0] - Mx);
        L = fmaf(f, cm[i][1], L);
        u += f * co[i]; u2 += f * c2[i];
      }
      const float rl = rcp(L);
      float acc[8];
#pragma unroll
      for (int j = 0; j < 8; ++j) acc[j] = 0.f;
      fmap(acc, wor[0], wor[1], cvtpk(u[0] * rl, u[1] * rl));
      fmap(acc, wor[2], wor[3], cvtpk(u2[0] * rl, u2[1] * rl));
      float r4[4], r2[2];
#pragma unroll
      for (int j = 0; j < 4; ++j) r4[j] = sw32_sum(acc[j], acc[j + 4]);
#pragma unroll
      for (int j = 0; j < 2; ++j) r2[j] = tr_step<X7>(r4[j], r4[j + 2], (lane & 4) != 0);
      float r1 = tr_step<X2>(r2[0], r2[1], (lane & 2) != 0);
      r1 += dpp<X1>(r1);
      if ((lane & 1) == 0) lds[L_S1 + (w - 4) * 32 + (sub & 3) * 8 + (lane >> 5) * 4 + ((lane >> 2) & 1) * 2 + ((lane >> 1) & 1)] = r1;
    }
    bar();
    if (w == 0 && lane < 16) {
      v2f a[4];
#pragma unroll
      for (int ww = 0; ww < 4; ++ww) a[ww] = *reinterpret_cast<const v2f*>(&lds[L_S1 + ww * 32 + lane * 2]);
      const v2f t = (a[0] + a[1]) + (a[2] + a[3]);
      stg(rs, WS_PH + (oh * 224 + cb * 16 + lane) * 16, __float_as_uint(t[0]), __float_as_uint(t[1]), t0, t1);
    }
    STAMP(8);
    STAMP_FLUSH();
    return;
  }

  const int f = g * 4 + (s - NSP);
  const bool ffn = f < 28;
  if (tid == 0) *(volatile LAS u32*)(&lds[L_FC]) = 0u;
  const int r4 = lane >> 4;
  const v4u xv = *reinterpret_cast<const v4u*>(x + (lane < 56 ? lane : 0) * 8);
  u32 xq[7];
  v4u wa[14], wf[14], wdn[8];
  {
    const int row0 = 56 * w + 14 * r4;
#pragma unroll
    for (int i = 0; i < 7; ++i) xq[i] = *reinterpret_cast<const u32*>(x + row0 + 2 * i);
    const u16* p = wq + (size_t)row0 * NQ + 128 * f + (lane & 15) * 8;
#pragma unroll
    for (int i = 0; i < 14; ++i) wa[i] = ldnt(p + (size_t)i * NQ);
    SB();
    bar();
    SB();
    const int fc = ffn ? f : 0;
    const u16* pf = (((lane >> 3) & 1) ? wu : wg) + (size_t)row0 * FF + 64 * fc + (lane & 7) * 8;
    float xf[8];
#pragma unroll
    for (int j = 0; j < 4; ++j) { xf[2 * j] = bflo(xv[j]); xf[2 * j + 1] = bfhi(xv[j]); }
    float sm = 0.f;
#pragma unroll
    for (int j = 0; j < 8; ++j) sm += xf[j];
    sm = wave_sum(lane < 56 ? sm : 0.f);
    const float mean = sm * (1.0f / D);
    float sq = 0.f;
#pragma unroll
    for (int j = 0; j < 8; ++j) { const float d = xf[j] - mean; sq += d * d; }
    sq = wave_sum(lane < 56 ? sq : 0.f);
    const float mb = rbf(mean), den = rbf(sqrtf(rbf(rbf(sq * (1.0f / D)) + 1e-5f)));
    float acc[8];
#pragma unroll
    for (int j = 0; j < 8; ++j) acc[j] = 0.f;
#pragma unroll
    for (int i = 0; i < 7; ++i) {
      const u32 n = cvtpk(rbf(bflo(xq[i]) - mb) / den, rbf(bfhi(xq[i]) - mb) / den);
      pairup(wa[2 * i], wa[2 * i + 1]);
      fmap(acc, wa[2 * i], wa[2 * i + 1], n);
    }
    float a4[4];
#pragma unroll
    for (int j = 0; j < 4; ++j) a4[j] = sw32_sum(acc[j], acc[j + 4]);
    v2f a2;
#pragma unroll
    for (int j = 0; j < 2; ++j) a2[j] = sw16_sum(a4[j], a4[j + 2]);
    *reinterpret_cast<v2f*>(&lds[L_QR + w * 128 + (lane & 15) * 8 + (lane >> 5) * 4 + ((lane >> 4) & 1) * 2]) = a2;
    bar();
    TAG();
    if (w == 0 && lane < 32) {
      v4f qv = *reinterpret_cast<const v4f*>(&lds[L_QR + lane * 4]);
#pragma unroll
      for (int ww = 1; ww < 8; ++ww) qv += *reinterpret_cast<const v4f*>(&lds[L_QR + ww * 128 + lane * 4]);
      stg(rs, WS_Q + (f * 32 + lane) * 16, cvtpk(qv[0], qv[1]), cvtpk(qv[2], qv[3]), t0, t1);
    }
    STAMP(1);
    if (!ffn) { STAMP_FLUSH(); return; }
    SB();
#pragma unroll
    for (int i = 0; i < 14; ++i) wf[i] = ldnt(pf + (size_t)i * FF);
    SB();
  }
  const int l56 = lane < 56 ? lane : 0;
  {
    const u16* pd = wd + (size_t)(64 * f + 8 * w) * D + 8 * l56;
#pragma unroll
    for (int i = 0; i < 8; ++i) wdn[i] = ldnt(pd + i * D);
    SB();
  }
#pragma unroll
  for (int i = 0; i < 7; ++i) { pairup(wf[2 * i], wf[2 * i + 1]); asm volatile("" : "+v"(wf[2 * i]), "+v"(wf[2 * i + 1])); }
#pragma unroll
  for (int i = 0; i < 4; ++i) { pairup(wdn[2 * i], wdn[2 * i + 1]); asm volatile("" : "+v"(wdn[2 * i]), "+v"(wdn[2 * i + 1])); }
  const int cu = (lane & 7) * 8 + (lane >> 5) * 4 + ((lane >> 4) & 1) * 2, mt = (lane >> 3) & 1;
  {
    float acc[8];
#pragma unroll
    for (int j = 0; j < 8; ++j) acc[j] = 0.f;
#pragma unroll
    for (int i = 0; i < 7; ++i) fmap(acc, wf[2 * i], wf[2 * i + 1], 0x3f803f80u);
    float a4[4];
#pragma unroll
    for (int j = 0; j < 4; ++j) a4[j] = sw32_sum(acc[j], acc[j + 4]);
#pragma unroll
    for (int j = 0; j < 2; ++j) lds[L_QR + w * 128 + (cu + j) * 2 + mt] = sw16_sum(a4[j], a4[j + 2]);
  }
  bar();
  const int u = 8 * w + (lane & 7);
  v2f SGU = {0.f, 0.f};
#pragma unroll
  for (int ww = 0; ww < 8; ++ww) SGU += *reinterpret_cast<const v2f*>(&lds[L_QR + ww * 128 + u * 2]);
  asm volatile("" : "+v"(SGU));
  {
    const u32 pt0 = __builtin_amdgcn_raw_buffer_load_b32(rs, WS_PD + (f * 224 + (tid < 224 ? tid : 0)) * 16, 0, AUX_SC1);
    const u32 pt1 = *reinterpret_cast<const volatile u32*>(out + 16 * f);
    asm volatile("" :: "v"(pt0), "v"(pt1));
  }
  TAG();
  STAMP(2);
  {
    const int hsub = lane >> 5;
    const bool act = (lane & 31) < 28;
    const int pp = act ? (lane & 31) : 0;
    int off[8];
    v4u gr[8];
    const u32 xp = *reinterpret_cast<const u32*>(x + 56 * w + 2 * pp);
    LAS u32* const xw = (LAS u32*)(&lds[L_XN + w * 32 + (pp / 7) * 8 + pp % 7]);
    const LAS v4u* const xr = (const LAS v4u*)(&lds[L_XN + w * 32 + r4 * 8]);
    const int cp = 28 * w + pp - 8 * f;
    const bool keep = lane < 28 && cp >= 0 && cp < 8;
    float acc[10];
#pragma unroll
    for (int j = 0; j < 10; ++j) { acc[j] = 0.f; asm volatile("" : "+v"(acc[j])); }
#pragma unroll
    for (int i = 0; i < 8; ++i) { off[i] = act ? WS_PH + ((2 * i + hsub) * 224 + 28 * w + pp) * 16 : OOB; gr[i] = ldg(rs, off[i]); }
#pragma unroll
    for (int i = 0; i < 8; ++i) mark(gr[i], !act, tag);
    poll<8, 64>(srd, off, gr, tag);
    STAMP(9);
    v2f hp[8];
#pragma unroll
    for (int i = 0; i < 8; ++i) hp[i] = v2f{__uint_as_float(gr[i][0]), __uint_as_float(gr[i][1])};
    const v2f hs = ((hp[0] + hp[1]) + (hp[2] + hp[3])) + ((hp[4] + hp[5]) + (hp[6] + hp[7]));
    const u32 a01 = cvtpk(sw32_sum(hs[0], hs[0]), sw32_sum(hs[1], hs[1]));
    const u32 h01 = cvtpk(bflo(xp) + bflo(a01), bfhi(xp) + bfhi(a01));
    if (lane < 28) *xw = h01;
    asm volatile("s_waitcnt lgkmcnt(0)" ::: "memory");
    const v4u na = xr[0], nb = xr[1];
    if (keep) reinterpret_cast<u32*>(&lds[L_HS])[cp] = h01;
#pragma unroll
    for (int i = 0; i < 7; ++i) {
      const u32 n = i < 4 ? na[i] : nb[i - 4];
#pragma unroll
      for (int j = 0; j < 4; ++j) { acc[j] = dot2(wf[2 * i][j], n, acc[j]); acc[4 + j] = dot2(wf[2 * i + 1][j], n, acc[4 + j]); }
      acc[8] = dot2(n, 0x3f803f80u, acc[8]);
      acc[9] = dot2(n, n, acc[9]);
    }
    float a4[4];
#pragma unroll
    for (int j = 0; j < 4; ++j) a4[j] = sw32_sum(acc[j], acc[j + 4]);
    float st = sw32_sum(acc[8], acc[9]);
    st = sw16_sum(st, st);
#pragma unroll
    for (int j = 0; j < 2; ++j) lds[L_GU + w * 128 + (cu + j) * 2 + mt] = sw16_sum(a4[j], a4[j + 2]);
    if ((lane & 31) == 0) lds[L_ST + w * 2 + hsub] = st;
  }
  float dacc[8];
#pragma unroll
  for (int j = 0; j < 8; ++j) { dacc[j] = 0.f; asm volatile("" : "+v"(dacc[j])); }
  bar();
  {
    v2f gu[8];
    v4f sv[4];
#pragma unroll
    for (int i = 0; i < 4; ++i) sv[i] = *reinterpret_cast<const v4f*>(&lds[L_ST + i * 4]);
#pragma unroll
    for (int ww = 0; ww < 8; ++ww) gu[ww] = *reinterpret_cast<const v2f*>(&lds[L_GU + ww * 128 + u * 2]);
    const v4f s4 = (sv[0] + sv[1]) + (sv[2] + sv[3]);
    const float mean = (s4[0] + s4[2]) * (1.0f / D);
    const float var = fmaxf(fmaf(-mean, mean, (s4[1] + s4[3]) * (1.0f / D)), 0.f);
    const float mb = rbf(mean), rden = rcp(rbf(__builtin_amdgcn_sqrtf(rbf(rbf(var) + 1e-5f))));
    const v2f GUs = ((gu[0] + gu[1]) + (gu[2] + gu[3])) + ((gu[4] + gu[5]) + (gu[6] + gu[7]));
    const v2f GUn = (GUs - mb * SGU) * rden;
    const u32 gub = cvtpk(GUn[0], GUn[1]);
    const float G = bflo(gub), U = bfhi(gub);
    const float z2 = G * fmaf(G * G, 0.044715f * 0.7978845608028654f * 2.0f * LOG2E, 0.7978845608028654f * 2.0f * LOG2E);
    const float sg = rcp(__builtin_amdgcn_exp2f(z2) + 1.0f);
    const float av = rbf(fmaf(-G, sg, G)) * U;
    const u32 pk = cvtpk(av, dpp<X1>(av));
#pragma unroll
    for (int i = 0; i < 4; ++i) fmap(dacc, wdn[2 * i], wdn[2 * i + 1], __builtin_amdgcn_readlane(pk, 2 * i));
    if (lane < 56) {
      *reinterpret_cast<v4f*>(&lds[L_DN + w * 448 + lane * 8]) = v4f{dacc[0], dacc[1], dacc[2], dacc[3]};
      *reinterpret_cast<v4f*>(&lds[L_DN + w * 448 + lane * 8 + 4]) = v4f{dacc[4], dacc[5], dacc[6], dacc[7]};
    }
  }
  bar();
  if (tid < 224) {
    v2f a[8];
#pragma unroll
    for (int ww = 0; ww < 8; ++ww) a[ww] = *reinterpret_cast<const v2f*>(&lds[L_DN + ww * 448 + tid * 2]);
    const v2f t = ((a[0] + a[1]) + (a[2] + a[3])) + ((a[4] + a[5]) + (a[6] + a[7]));
    stg(rs, WS_PD + (f * 224 + tid) * 16, __float_as_uint(t[0]), __float_as_uint(t[1]), t0, t1);
  }
  STAMP(10);
  if (w < 7) {
    int off[1];
    v4u gr[1];
    off[0] = lane < 32 ? WS_PD + ((4 * w + (lane >> 3)) * 224 + 8 * f + (lane & 7)) * 16 : OOB;
    gr[0] = ldg(rs, off[0]);
    mark(gr[0], lane >= 32, tag);
    poll<1, 64>(srd, off, gr, tag);
    STAMP(11);
    float a0 = __uint_as_float(gr[0][0]), a1 = __uint_as_float(gr[0][1]);
    float r = sw16_sum(a0, a1);
    r += dpp<X8>(r);
    if (lane < 32 && (lane & 8) == 0) lds[L_FN + w * 16 + (lane & 7) * 2 + (lane >> 4)] = r;
    asm volatile("s_waitcnt lgkmcnt(0)" ::: "memory");
    u32 old = 0;
    if (lane == 0) old = __hip_atomic_fetch_add((LAS u32*)(&lds[L_FC]), 1u, __ATOMIC_RELAXED, __HIP_MEMORY_SCOPE_WORKGROUP);
    if (__builtin_amdgcn_readfirstlane(old) == 6u) {
      asm volatile("" ::: "memory");
      if (lane < 8) {
        v2f a[7];
#pragma unroll
        for (int ww = 0; ww < 7; ++ww) a[ww] = *reinterpret_cast<const v2f*>(&lds[L_FN + ww * 16 + lane * 2]);
        const v2f t = ((a[0] + a[1]) + (a[2] + a[3])) + ((a[4] + a[5]) + a[6]);
        const u32 hv = reinterpret_cast<const u32*>(&lds[L_HS])[lane];
        const u32 tb = cvtpk(t[0], t[1]);
        *reinterpret_cast<u32*>(out + 16 * f + lane * 2) = cvtpk(bflo(hv) + bflo(tb), bfhi(hv) + bfhi(tb));
        if (f == 0 && lane == 0) __builtin_amdgcn_raw_buffer_store_b64(v2u{t0, t1}, rs, WS_EPOCH, 0, AUX_SC1);
      }
    }
  }
  STAMP(12);
  STAMP_FLUSH();
}

}

extern "C" void kernel_launch(void* const* d_in, const int* in_sizes, int n_in, void* d_out, int out_size, void* d_ws,
                              size_t ws_size, hipStream_t stream) {
  fused_decode_block<<<256, 512, 0, stream>>>((const u16*)d_in[0], (const u16*)d_in[1], (const u16*)d_in[2], (const u16*)d_in[3],
                                              (const u16*)d_in[4], (const u16*)d_in[6], (const u16*)d_in[7], (const u16*)d_in[8],
                                              (u16*)d_out, (unsigned char*)d_ws);
}
```

```cpp
#include <hip/hip_runtime.h>
#include <hip/hip_bf16.h>
#include <stdint.h>

typedef unsigned short u16;
typedef unsigned int u32;
typedef unsigned long long u64;
typedef u32 v4u __attribute__((ext_vector_type(4)));
typedef u32 v2u __attribute__((ext_vector_type(2)));
typedef float v4f __attribute__((ext_vector_type(4)));
typedef float v2f __attribute__((ext_vector_type(2)));
typedef __bf16 bf16x2 __attribute__((ext_vector_type(2)));
typedef __bf16 bf16x8 __attribute__((ext_vector_type(8)));
typedef short s16x4 __attribute__((ext_vector_type(4)));

#define LAS __attribute__((address_space(3)))
#define PRETOUCH 1
#define FEW 4
#define GSL 4
#define DIAG 1

namespace {

constexpr int D = 448, SK = 8192, DH = 256, FF = 1792, NQ = 4096, NSP = 28;
constexpr int WS_EPOCH = 0;
constexpr int WS_Q = 4096;
constexpr int WS_ML = 32768;
constexpr int WS_PO = 131072;
constexpr int WS_PH = WS_PO + 524288;
constexpr int WS_PD = WS_PH + 65536;
constexpr int WS_END = WS_PD + 28 * 3584;
constexpr int WS_STAMP = 1 << 20;
constexpr int AUX_SC1 = 16, AUX_NT = 2;
constexpr int OOB = 0x7ffffff0;
constexpr u32 SPIN_CAP = 1u << 17;
constexpr float LOG2E = 1.4426950408889634f;

constexpr int L_QS = 0;
constexpr int L_WMAX = 256;
constexpr int L_WSUM = 272;
constexpr int L_S1 = 288;
constexpr int L_FLAG = 544;
constexpr int L_CML = 548;
constexpr int L_ORED = 576;
constexpr int L_P = L_ORED + 2048;
constexpr int L_CMB = L_P + 2560;
constexpr int L_QST = L_CMB + 5 * 256;
constexpr int L_END = L_QST + 4 * 512;
constexpr int L_QR = 0;
constexpr int L_ST = 1024;
constexpr int L_XN = 1040;
constexpr int L_HS = 1296;
constexpr int L_GU = 1312;
constexpr int L_DN = 2336;
constexpr int L_FN = 0;
constexpr int L_FC = 5920;

__device__ __forceinline__ float bflo(u32 u) { return __uint_as_float(u << 16); }
__device__ __forceinline__ float bfhi(u32 u) { return __uint_as_float(u & 0xffff0000u); }
__device__ __forceinline__ u32 cvtpk(float lo, float hi) {
  v2f v = {lo, hi};
  return __builtin_bit_cast(u32, __builtin_convertvector(v, bf16x2));
}
__device__ __forceinline__ float rbf(float f) { return bflo(cvtpk(f, 0.f)); }
__device__ __forceinline__ float dot2(u32 w, u32 a, float acc) {
  return __builtin_amdgcn_fdot2_f32_bf16(__builtin_bit_cast(bf16x2, w), __builtin_bit_cast(bf16x2, a), acc, false);
}
__device__ __forceinline__ float bsel(u32 m, float a, float b) {
  return __uint_as_float((__float_as_uint(a) & m) | (__float_as_uint(b) & ~m));
}
template <int CTRL> __device__ __forceinline__ float dpp(float x) {
  return __builtin_bit_cast(float, __builtin_amdgcn_mov_dpp(__builtin_bit_cast(int, x), CTRL, 0xf, 0xf, true));
}
constexpr int X1 = 0xB1, X2 = 0x4E, X7 = 0x141, X8 = 0x128;
__device__ __forceinline__ float sw32_sum(float a, float b) {
  auto r = __builtin_amdgcn_permlane32_swap(__float_as_uint(a), __float_as_uint(b), false, false);
  return __uint_as_float(r[0]) + __uint_as_float(r[1]);
}
__device__ __forceinline__ float sw16_sum(float a, float b) {
  auto r = __builtin_amdgcn_permlane16_swap(__float_as_uint(a), __float_as_uint(b), false, false);
  return __uint_as_float(r[0]) + __uint_as_float(r[1]);
}
__device__ __forceinline__ float sw32_max(float a) {
  auto r = __builtin_amdgcn_permlane32_swap(__float_as_uint(a), __float_as_uint(a), false, false);
  return fmaxf(__uint_as_float(r[0]), __uint_as_float(r[1]));
}
__device__ __forceinline__ float sw16_max(float a) {
  auto r = __builtin_amdgcn_permlane16_swap(__float_as_uint(a), __float_as_uint(a), false, false);
  return fmaxf(__uint_as_float(r[0]), __uint_as_float(r[1]));
}
template <int CTRL> __device__ __forceinline__ float tr_step(float lo, float hi, bool sel) {
  const float t1 = lo + dpp<CTRL>(lo), t2 = hi + dpp<CTRL>(hi);
  return sel ? t2 : t1;
}
__device__ __forceinline__ float wave_sum(float x) {
  x += dpp<X1>(x); x += dpp<X2>(x); x += dpp<X7>(x); x += dpp<X8>(x);
  x = sw16_sum(x, x); return sw32_sum(x, x);
}
__device__ __forceinline__ float half_sum(float x) {
  x += dpp<X1>(x); x += dpp<X2>(x); x += dpp<X7>(x); x += dpp<X8>(x);
  return sw16_sum(x, x);
}
__device__ __forceinline__ v4u ldnt(const u16* p) { return __builtin_nontemporal_load(reinterpret_cast<const v4u*>(p)); }
__device__ __forceinline__ v4u ldb(__amdgpu_buffer_rsrc_t rs, int off) { return __builtin_amdgcn_raw_buffer_load_b128(rs, off, 0, AUX_NT); }
__device__ __forceinline__ v4u ldg(__amdgpu_buffer_rsrc_t rs, int off) { return __builtin_amdgcn_raw_buffer_load_b128(rs, off, 0, AUX_SC1); }
__device__ __forceinline__ void stg(__amdgpu_buffer_rsrc_t rs, int off, u32 a, u32 b, u32 t0, u32 t1) {
  v4u v = {a, b, t0, t1};
  __builtin_amdgcn_raw_buffer_store_b128(v, rs, off, 0, AUX_SC1);
}
__device__ __forceinline__ void bar() {
  asm volatile("s_waitcnt lgkmcnt(0)" ::: "memory");
  __builtin_amdgcn_s_barrier();
  asm volatile("" ::: "memory");
}
__device__ __forceinline__ v4f mfma(v4u a, v4u b, v4f c) {
  return __builtin_amdgcn_mfma_f32_16x16x32_bf16(__builtin_bit_cast(bf16x8, a), __builtin_bit_cast(bf16x8, b), c, 0, 0, 0);
}
__device__ __forceinline__ v4f mfma16(v2u a, v2u b, v4f c) {
  return __builtin_amdgcn_mfma_f32_16x16x16bf16_1k(__builtin_bit_cast(s16x4, a), __builtin_bit_cast(s16x4, b), c, 0, 0, 0);
}
__device__ __forceinline__ u32 plo(u32 r0, u32 r1) { return __builtin_amdgcn_perm(r1, r0, 0x05040100u); }
__device__ __forceinline__ u32 phi(u32 r0, u32 r1) { return __builtin_amdgcn_perm(r1, r0, 0x07060302u); }
__device__ __forceinline__ void pairup(v4u& a, v4u& b) {
  v4u x, y;
  x[0] = plo(a[0], b[0]); x[1] = phi(a[0], b[0]); x[2] = plo(a[1], b[1]); x[3] = phi(a[1], b[1]);
  y[0] = plo(a[2], b[2]); y[1] = phi(a[2], b[2]); y[2] = plo(a[3], b[3]); y[3] = phi(a[3], b[3]);
  a = x; b = y;
}
__device__ __forceinline__ void fmap(float (&acc)[8], v4u a, v4u b, u32 n) {
#pragma unroll
  for (int j = 0; j < 4; ++j) { acc[j] = dot2(a[j], n, acc[j]); acc[4 + j] = dot2(b[j], n, acc[4 + j]); }
}
__device__ __forceinline__ void fma8(float (&acc)[8], v4u wv, u32 alo, u32 ahi) {
#pragma unroll
  for (int j = 0; j < 4; ++j) { acc[2 * j] = dot2(wv[j], alo, acc[2 * j]); acc[2 * j + 1] = dot2(wv[j], ahi, acc[2 * j + 1]); }
}
__device__ __forceinline__ u64 gtag(const v4u& g) { return ((u64)g[3] << 32) | (u64)g[2]; }
__device__ __forceinline__ void mark(v4u& g, bool unused, u64 tag) {
  g[2] = unused ? (u32)tag : g[2]; g[3] = unused ? (u32)(tag >> 32) : g[3];
}
__device__ __forceinline__ void reload(v4u (&g)[1], const int (&o)[1], const u64 (&m)[1], v4u srd) {
  asm volatile("s_mov_b64 exec, %2\n\tbuffer_load_dwordx4 %0, %1, %3, 0 offen sc1\n\ts_mov_b64 exec, -1\n\ts_waitcnt vmcnt(0)"
               : "+v"(g[0]) : "v"(o[0]), "s"(m[0]), "s"(srd) : "memory");
}
__device__ __forceinline__ void reload(v4u (&g)[2], const int (&o)[2], const u64 (&m)[2], v4u srd) {
  asm volatile("s_mov_b64 exec, %4\n\tbuffer_load_dwordx4 %0, %2, %6, 0 offen sc1\n\t"
               "s_mov_b64 exec, %5\n\tbuffer_load_dwordx4 %1, %3, %6, 0 offen sc1\n\t"
               "s_mov_b64 exec, -1\n\ts_waitcnt vmcnt(0)"
               : "+v"(g[0]), "+v"(g[1])
               : "v"(o[0]), "v"(o[1]), "s"(m[0]), "s"(m[1]), "s"(srd) : "memory");
}
__device__ __forceinline__ void reload(v4u (&g)[4], const int (&o)[4], const u64 (&m)[4], v4u srd) {
  asm volatile("s_mov_b64 exec, %8\n\tbuffer_load_dwordx4 %0, %4, %12, 0 offen sc1\n\t"
               "s_mov_b64 exec, %9\n\tbuffer_load_dwordx4 %1, %5, %12, 0 offen sc1\n\t"
               "s_mov_b64 exec, %10\n\tbuffer_load_dwordx4 %2, %6, %12, 0 offen sc1\n\t"
               "s_mov_b64 exec, %11\n\tbuffer_load_dwordx4 %3, %7, %12, 0 offen sc1\n\t"
               "s_mov_b64 exec, -1\n\ts_waitcnt vmcnt(0)"
               : "+v"(g[0]), "+v"(g[1]), "+v"(g[2]), "+v"(g[3])
               : "v"(o[0]), "v"(o[1]), "v"(o[2]), "v"(o[3]), "s"(m[0]), "s"(m[1]), "s"(m[2]), "s"(m[3]), "s"(srd) : "memory");
}
__device__ __forceinline__ void reload(v4u (&g)[8], const int (&o)[8], const u64 (&m)[8], v4u srd) {
  asm volatile("s_mov_b64 exec, %16\n\tbuffer_load_dwordx4 %0, %8, %24, 0 offen sc1\n\t"
               "s_mov_b64 exec, %17\n\tbuffer_load_dwordx4 %1, %9, %24, 0 offen sc1\n\t"
               "s_mov_b64 exec, %18\n\tbuffer_load_dwordx4 %2, %10, %24, 0 offen sc1\n\t"
               "s_mov_b64 exec, %19\n\tbuffer_load_dwordx4 %3, %11, %24, 0 offen sc1\n\t"
               "s_mov_b64 exec, %20\n\tbuffer_load_dwordx4 %4, %12, %24, 0 offen sc1\n\t"
               "s_mov_b64 exec, %21\n\tbuffer_load_dwordx4 %5, %13, %24, 0 offen sc1\n\t"
               "s_mov_b64 exec, %22\n\tbuffer_load_dwordx4 %6, %14, %24, 0 offen sc1\n\t"
               "s_mov_b64 exec, %23\n\tbuffer_load_dwordx4 %7, %15, %24, 0 offen sc1\n\t"
               "s_mov_b64 exec, -1\n\ts_waitcnt vmcnt(0)"
               : "+v"(g[0]), "+v"(g[1]), "+v"(g[2]), "+v"(g[3]), "+v"(g[4]), "+v"(g[5]), "+v"(g[6]), "+v"(g[7])
               : "v"(o[0]), "v"(o[1]), "v"(o[2]), "v"(o[3]), "v"(o[4]), "v"(o[5]), "v"(o[6]), "v"(o[7]), "s"(m[0]), "s"(m[1]), "s"(m[2]), "s"(m[3]), "s"(m[4]), "s"(m[5]), "s"(m[6]), "s"(m[7]), "s"(srd) : "memory");
}
template <int N, int MANY> __device__ __forceinline__ u32 poll(v4u srd, const int (&off)[N], v4u (&gr)[N], u64 tag) {
  u32 spins = 0;
  u64 m[N], any = 0;
#pragma unroll
  for (int i = 0; i < N; ++i) { m[i] = __ballot(gtag(gr[i]) != tag); any |= m[i]; }
  if (__builtin_expect(any != 0, 0)) {
    do {
      if (MANY < 64 && __builtin_popcountll(any) > MANY) __builtin_amdgcn_s_sleep(8);
      reload(gr, off, m, srd);
      any = 0;
#pragma unroll
      for (int i = 0; i < N; ++i) { m[i] = __ballot(gtag(gr[i]) != tag); any |= m[i]; }
    } while (any != 0 && ++spins < SPIN_CAP);
    ++spins;
  }
  return spins;
}
__device__ __forceinline__ float rcp(float x) { return __builtin_amdgcn_rcpf(x); }
#define STAMP(i) do { u64 t_; __builtin_amdgcn_sched_barrier(0); asm volatile("s_memrealtime %0\n\ts_waitcnt lgkmcnt(0)" : "=s"(t_) :: "memory"); __builtin_amdgcn_sched_barrier(0); stamps[i] = t_; } while (0)
#define STAMP_FLUSH() do { if (lane == 0) { _Pragma("unroll") for (int i_ = 0; i_ < 16; ++i_) __hip_atomic_store((u64*)(ws + WS_STAMP + ((b * 8 + w) * 16 + i_) * 8), stamps[i_], __ATOMIC_RELAXED, __HIP_MEMORY_SCOPE_AGENT); } } while (0)
#define SB() __builtin_amdgcn_sched_barrier(0)
#define TAG() const u64 tag = (((u64)(u32)__builtin_amdgcn_readfirstlane(ep[1]) << 32) | (u64)(u32)__builtin_amdgcn_readfirstlane(ep[0])) + 1; \
  const u32 t0 = (u32)tag, t1 = (u32)(tag >> 32)

__global__ void __launch_bounds__(512, 2)
fused_decode_block(const u16* __restrict__ x, const u16* __restrict__ Kc, const u16* __restrict__ Vc, const u16* __restrict__ wq,
                   const u16* __restrict__ wo, const u16* __restrict__ wg, const u16* __restrict__ wu, const u16* __restrict__ wd,
                   u16* __restrict__ out, unsigned char* ws) {
  __shared__ __attribute__((aligned(16))) float lds[L_END];
  asm volatile("" :: "s"(x), "s"(Kc), "s"(Vc), "s"(wq), "s"(wo), "s"(wg), "s"(wu), "s"(wd), "s"(out), "s"(ws));
  const int tid = threadIdx.x, lane = tid & 63;
  const int w = __builtin_amdgcn_readfirstlane(tid >> 6);
  const int g = blockIdx.x & 7, s = ((blockIdx.x >> 3) + 28) & 31, b = s * 8 + g;
  const __amdgpu_buffer_rsrc_t rs = __builtin_amdgcn_make_buffer_rsrc((void*)ws, 0, WS_END, 0x00020000);
  const v4u srd = {(u32)(uintptr_t)ws, (u32)((uintptr_t)ws >> 32) & 0xffffu, (u32)WS_END, 0x00020000u};
  u64 stamps[16];
#pragma unroll
  for (int i_ = 0; i_ < 16; ++i_) stamps[i_] = 0;
  STAMP(0);
  const v2u ep = __builtin_amdgcn_raw_buffer_load_b64(rs, WS_EPOCH, 0, AUX_SC1);

  if (s < NSP) {
    volatile LAS u32* lflag = (volatile LAS u32*)(&lds[L_FLAG]);
    if (tid == 0) { lflag[0] = 0u; lflag[1] = 0u; lflag[2] = 0u; lflag[3] = 0u; }
    __builtin_amdgcn_s_setprio(1);
    bar();
    const int g4 = lane >> 4, c = lane & 15;
    const int tile0 = (256 * s) / 7, nx = (256 * (s + 1)) / 7 - tile0 - 36;
    const int wi = w & 3;
    const bool ten = wi < nx;
    const int key0 = (tile0 + 9 * wi + (wi ? nx : 0)) * 8;
    const int oh = 2 * g + s / 14, cb = s % 14;
    const int sub = lane >> 3, gq = lane & 7;
    const int orow = 64 * wi + 4 * gq + 2 * (lane >> 5);
    v4u wor[4];
    if (w < 4) {
      const __amdgpu_buffer_rsrc_t rk = __builtin_amdgcn_make_buffer_rsrc((void*)Kc, 0, 8 * SK * DH * 2, 0x00020000);
      v4u kr[40];
      const int kb = ((g * SK + key0 + (c >> 1)) * DH + (c & 1) * 32 + g4 * 8) * 2;
#pragma unroll
      for (int t = 0; t < 9; ++t)
#pragma unroll
        for (int ks = 0; ks < 4; ++ks) kr[t * 4 + ks] = ldb(rk, kb + t * 4096 + ks * 128);
#pragma unroll
      for (int ks = 0; ks < 4; ++ks) kr[36 + ks] = ldb(rk, ten ? kb + 9 * 4096 + ks * 128 : OOB);
      SB();
      STAMP(1);
      TAG();
      {
        __builtin_amdgcn_s_setprio(3);
        const int qo0 = WS_Q + (g * 128 + lane) * 16;
        const u32 st0 = (u32)(uintptr_t)(LAS float*)(&lds[L_QST + w * 512]);
        const u32 fl = (u32)(uintptr_t)(LAS float*)(&lds[L_FLAG]);
#define Q_SAMPLE(pend) do { u32 keep_, tmp_, vt_; \
          asm volatile("s_mov_b32 %1, m0\n\t" \
                       "s_mov_b32 m0, %5\n\ts_nop 0\n\tbuffer_load_dwordx4 %4, %6, 0 offen sc1 lds\n\t" \
                       "buffer_load_dwordx4 %4, %6, 0 offen offset:1024 sc1 lds\n\t"   \
                       "s_mov_b32 m0, %1\n" \
                       "1:\n\t" \
                       "s_getreg_b32 %0, hwreg(HW_REG_IB_STS)\n\ts_and_b32 %0, %0, 0xc0000f\n\ts_cbranch_scc0 2f\n\t" \
                       "ds_read_b32 %3, %7\n\ts_waitcnt lgkmcnt(0)\n\tv_readfirstlane_b32 %2, %3\n\ts_cmp_eq_u32 %2, 0\n\ts_cbranch_scc1 1b\n" \
                       "2:" \
                       : "=&s"(pend), "=&s"(keep_), "=&s"(tmp_), "=&v"(vt_) : "v"(qo0), "s"(st0), "s"(srd), "v"(fl) : "memory", "scc"); } while (0)
        u32 pend;
        Q_SAMPLE(pend);
        __builtin_amdgcn_s_waitcnt(0x0F70);
        pend = 0u;
        for (u32 sp = 0; pend == 0u && lflag[0] == 0u && sp < SPIN_CAP; ++sp) {
          asm volatile("" ::: "memory");
          const v4u q0 = *reinterpret_cast<const v4u*>(&lds[L_QST + w * 512 + lane * 4]);
          const v4u q1 = *reinterpret_cast<const v4u*>(&lds[L_QST + w * 512 + 256 + lane * 4]);
          if (__ballot(gtag(q0) != tag || gtag(q1) != tag) == 0) {
            *reinterpret_cast<v2u*>(&lds[L_QS + lane * 2]) = v2u{q0[0], q0[1]};
            *reinterpret_cast<v2u*>(&lds[L_QS + (64 + lane) * 2]) = v2u{q1[0], q1[1]};
            asm volatile("s_waitcnt lgkmcnt(0)" ::: "memory");
            lflag[0] = 1u;
            break;
          }
          Q_SAMPLE(pend);
        }
        __builtin_amdgcn_s_setprio(1);
      }
      asm volatile("" ::: "memory");
      STAMP(2);
      const int hd = (c >> 2) & 1;
      const u32 meta = ((c >> 1) & 1) ? 0xffffffffu : 0u, mvar = (c & 1) ? 0xffffffffu : 0u;
      float sc[20];
      {
        v4u qf[4];
#pragma unroll
        for (int ks = 0; ks < 4; ++ks) qf[ks] = *reinterpret_cast<const v4u*>(&lds[L_QS + hd * 128 + ks * 32 + ((c >> 1) & 1) * 16 + g4 * 4]);
#pragma unroll
        for (int t = 0; t < 10; ++t) {
          v4f d = {0.f, 0.f, 0.f, 0.f};
#pragma unroll
          for (int ks = 0; ks < 4; ++ks) d = mfma(kr[t * 4 + ks], qf[ks], d);
          const float sa = bsel(meta, d[1], d[0]), sb = bsel(meta, d[3], d[2]);
          const u32 sp = cvtpk(sa + dpp<X2>(sa), sb + dpp<X2>(sb));
          sc[2 * t] = bflo(sp) * (0.0625f * LOG2E);
          sc[2 * t + 1] = bfhi(sp) * (0.0625f * LOG2E);
        }
        if (!ten) { sc[18] = -INFINITY; sc[19] = -INFINITY; }
      }
      float m = sc[0];
#pragma unroll
      for (int j = 1; j < 20; ++j) m = fmaxf(m, sc[j]);
      m = sw32_max(sw16_max(m));
      if ((lane & 0x3b) == 0) lds[L_WMAX + w * 2 + hd] = m;
      asm volatile("s_waitcnt lgkmcnt(0)" ::: "memory");
      if (lane == 0) __hip_atomic_fetch_add((LAS u32*)(&lds[L_FLAG + 2]), 1u, __ATOMIC_RELAXED, __HIP_MEMORY_SCOPE_WORKGROUP);
      for (u32 sp = 0; lflag[2] < 4u && sp < (SPIN_CAP << 4); ++sp) __builtin_amdgcn_s_sleep(1);
      asm volatile("" ::: "memory");
      m = fmaxf(fmaxf(lds[L_WMAX + hd], lds[L_WMAX + 2 + hd]), fmaxf(lds[L_WMAX + 4 + hd], lds[L_WMAX + 6 + hd]));
      {
        float l = 0.f;
        LAS u32* const pw = (LAS u32*)(&lds[L_P + w * 640 + lane]);
#pragma unroll
        for (int t = 0; t < 10; ++t) {
          const float p0 = __builtin_amdgcn_exp2f(sc[2 * t] - m), p1 = __builtin_amdgcn_exp2f(sc[2 * t + 1] - m);
          l += p0 + p1;
          const u32 hi = cvtpk(p0, p1);
          const u32 lo = cvtpk(p0 - bflo(hi), p1 - bfhi(hi));
          pw[t * 64] = (lo & mvar) | (hi & ~mvar);
        }
        l = sw16_sum(l, l); l = sw32_sum(l, l);
        if ((lane & 0x3b) == 0) lds[L_WSUM + w * 2 + hd] = l;
        asm volatile("s_waitcnt lgkmcnt(0)" ::: "memory");
        if (lane == 0) __hip_atomic_fetch_add((LAS u32*)(&lds[L_FLAG + 3]), 1u, __ATOMIC_RELAXED, __HIP_MEMORY_SCOPE_WORKGROUP);
      }
      STAMP(4);
      __builtin_amdgcn_s_setprio(0);
      v4u po[8];
      int poff[8];
      const int mysp = w + 4 * (lane & 7);
      const bool mlwant = lane < 7 && mysp != s;
      poff[7] = WS_ML + (oh * 32 + mysp) * 128;
      po[7] = v4u{mlwant ? 0u : 0xff800000u, 0u, ~t0, 0u};
      mark(po[7], !mlwant, tag);
#pragma unroll
      for (int i = 0; i < 7; ++i) {
        po[i] = v4u{0u, 0u, ~t0, 0u};
        mark(po[i], w + 4 * i == s, tag);
        poff[i] = WS_PO + ((oh * 32 + w + 4 * i) * 64 + lane) * 16;
      }
      for (u32 sp = 0; sp < SPIN_CAP; ++sp) {
        u64 pm[8], pany = 0;
#pragma unroll
        for (int i = 0; i < 8; ++i) { pm[i] = __ballot(gtag(po[i]) != tag); pany |= pm[i]; }
        if (pany == 0) break;
        const bool few = __builtin_popcountll(pm[7]) <= FEW;
        if (GSL && !few && sp) __builtin_amdgcn_s_sleep(GSL);
#pragma unroll
        for (int i = 0; i < 7; ++i)
          if (!few && ((pm[7] >> i) & 1)) pm[i] = 0ull;
        reload(po, poff, pm, srd);
      }
      const v4u ml = po[7];
      STAMP(7);
      const float mi = __uint_as_float(ml[0]);
      float mx = fmaxf(mi, dpp<X1>(mi)); mx = fmaxf(mx, dpp<X2>(mx)); mx = fmaxf(mx, dpp<X7>(mx));
      const float wt = __builtin_amdgcn_exp2f(mi - mx);
      float lw = wt * __uint_as_float(ml[1]);
      lw += dpp<X1>(lw); lw += dpp<X2>(lw); lw += dpp<X7>(lw);
      v4f o = {0.f, 0.f, 0.f, 0.f};
#pragma unroll
      for (int i = 0; i < 7; ++i) {
        const float f = __builtin_bit_cast(float, __builtin_amdgcn_readlane(__builtin_bit_cast(int, wt), i));
        o[0] = fmaf(f, bflo(po[i][0]), o[0]); o[1] = fmaf(f, bfhi(po[i][0]), o[1]);
        o[2] = fmaf(f, bflo(po[i][1]), o[2]); o[3] = fmaf(f, bfhi(po[i][1]), o[3]);
      }
      *reinterpret_cast<v4f*>(&lds[L_CMB + w * 256 + lane * 4]) = o;
      if (lane == 0) *reinterpret_cast<v2f*>(&lds[L_CML + w * 2]) = v2f{mx, lw};
#pragma unroll
      for (int i = 0; i < 4; ++i) wor[i] = v4u{0u, 0u, 0u, 0u};
    } else {
      const __amdgpu_buffer_rsrc_t rv = __builtin_amdgcn_make_buffer_rsrc((void*)Vc, 0, 8 * SK * DH * 2, 0x00020000);
      const __amdgpu_buffer_rsrc_t ro = __builtin_amdgcn_make_buffer_rsrc((void*)wo, 0, NQ * D * 2, 0x00020000);
      v4u vr[40];
      const int vb = ((g * SK + key0 + 2 * g4) * DH + 8 * c) * 2;
#pragma unroll
      for (int hf = 0; hf < 2; ++hf) {
#pragma unroll
        for (int j = 0; j < 18; ++j) vr[hf * 20 + j] = ldb(rv, vb + (8 * (j >> 1) + (j & 1)) * 512 + 256 * hf);
#pragma unroll
        for (int j = 18; j < 20; ++j) vr[hf * 20 + j] = ldb(rv, ten ? vb + (72 + (j & 1)) * 512 + 256 * hf : OOB);
        SB();
      }
      {
        const int oo = ((oh * 256 + orow) * D + 32 * cb + 8 * (sub & 3)) * 2;
        wor[0] = ldb(ro, oo); wor[1] = ldb(ro, oo + D * 2); wor[2] = ldb(ro, oo + 32 * D * 2); wor[3] = ldb(ro, oo + 33 * D * 2);
        SB();
      }
      STAMP(1);
      TAG();
      for (u32 sp = 0; lflag[3] < 4u && sp < (SPIN_CAP << 4); ++sp) __builtin_amdgcn_s_sleep(1);
      asm volatile("" ::: "memory");
      STAMP(2);
      const u32 mhi = (g4 >> 1) ? 0xffffffffu : 0u;
      v4u pa0, pa1;
      v2u pa2;
      {
        const LAS u32* const pr = (const LAS u32*)(&lds[L_P + wi * 640 + lane]);
#pragma unroll
        for (int t = 0; t < 4; ++t) { pa0[t] = pr[t * 64]; pa1[t] = pr[(4 + t) * 64]; }
        pa2[0] = pr[8 * 64]; pa2[1] = pr[9 * 64];
      }
#pragma unroll
      for (int hf = 0; hf < 2; ++hf) {
        v4f d1[8];
#pragma unroll
        for (int kk = 0; kk < 2; ++kk) {
          v4u bb[8];
#pragma unroll
          for (int t = 0; t < 4; ++t)
#pragma unroll
            for (int d = 0; d < 8; ++d)
              bb[d][t] = __builtin_amdgcn_perm(vr[hf * 20 + kk * 8 + 2 * t + 1][d >> 1], vr[hf * 20 + kk * 8 + 2 * t][d >> 1], (d & 1) ? 0x07060302u : 0x05040100u);
          SB();
#pragma unroll
          for (int d = 0; d < 8; ++d) d1[d] = mfma(kk ? pa1 : pa0, bb[d], kk ? d1[d] : v4f{0.f, 0.f, 0.f, 0.f});
          SB();
        }
        v2u b2[8];
#pragma unroll
        for (int t = 0; t < 2; ++t)
#pragma unroll
          for (int d = 0; d < 8; ++d)
            b2[d][t] = __builtin_amdgcn_perm(vr[hf * 20 + 16 + 2 * t + 1][d >> 1], vr[hf * 20 + 16 + 2 * t][d >> 1], (d & 1) ? 0x07060302u : 0x05040100u);
        SB();
        v4f d2[8];
#pragma unroll
        for (int d = 0; d < 8; ++d) d2[d] = mfma16(pa2, b2[d], v4f{0.f, 0.f, 0.f, 0.f});
        float x1[4];
#pragma unroll
        for (int e = 0; e < 4; ++e) x1[e] = bsel(mhi, d1[4 + e][0] + d1[4 + e][1], d1[e][0] + d1[e][1]);
        SB();
        v4f y;
#pragma unroll
        for (int e = 0; e < 4; ++e) y[e] = x1[e] + bsel(mhi, d2[4 + e][0] + d2[4 + e][1], d2[e][0] + d2[e][1]);
        *reinterpret_cast<v4f*>(&lds[L_ORED + (wi * 2 + (g4 & 1)) * 256 + 128 * hf + 8 * c + 4 * (g4 >> 1)]) = y;
      }
      STAMP(4);
      asm volatile("s_waitcnt lgkmcnt(0)" ::: "memory");
      if (lane == 0) __hip_atomic_fetch_add((LAS u32*)(&lds[L_FLAG + 1]), 1u, __ATOMIC_RELAXED, __HIP_MEMORY_SCOPE_WORKGROUP);
      if (w < 6) {
        const int hh = w - 4;
        const float Mh = fmaxf(fmaxf(lds[L_WMAX + hh], lds[L_WMAX + 2 + hh]), fmaxf(lds[L_WMAX + 4 + hh], lds[L_WMAX + 6 + hh]));
        const float L = (lds[L_WSUM + hh] + lds[L_WSUM + 2 + hh]) + (lds[L_WSUM + 4 + hh] + lds[L_WSUM + 6 + hh]);
        for (u32 sp = 0; lflag[1] < 4u && sp < (SPIN_CAP << 4); ++sp) { }
        asm volatile("" ::: "memory");
        STAMP(5);
        v4f t4[4];
#pragma unroll
        for (int ww = 0; ww < 4; ++ww) t4[ww] = *reinterpret_cast<const v4f*>(&lds[L_ORED + (ww * 2 + hh) * 256 + lane * 4]);
        const v4f o = (t4[0] + t4[1]) + (t4[2] + t4[3]);
        const u32 c0 = cvtpk(o[0], o[1]), c1 = cvtpk(o[2], o[3]);
        stg(rs, WS_PO + (((2 * g + hh) * 32 + s) * 64 + lane) * 16, c0, c1, t0, t1);
        if (lane == 0) stg(rs, WS_ML + ((2 * g + hh) * 32 + s) * 128, __float_as_uint(Mh), __float_as_uint(L), t0, t1);
        if (hh == (oh & 1)) {
          *reinterpret_cast<v4f*>(&lds[L_CMB + 4 * 256 + lane * 4]) = v4f{bflo(c0), bfhi(c0), bflo(c1), bfhi(c1)};
          if (lane == 0) *reinterpret_cast<v2f*>(&lds[L_CML + 8]) = v2f{Mh, L};
        }
        STAMP(6);
      }
      pairup(wor[0], wor[1]); pairup(wor[2], wor[3]);
      asm volatile("" : "+v"(wor[0]), "+v"(wor[1]), "+v"(wor[2]), "+v"(wor[3]));
    }
    TAG();
    bar();
    if (w >= 4) {
      v2f cm[5], co[5], c2[5];
#pragma unroll
      for (int i = 0; i < 5; ++i) {
        cm[i] = *reinterpret_cast<const v2f*>(&lds[L_CML + i * 2]);
        co[i] = *reinterpret_cast<const v2f*>(&lds[L_CMB + i * 256 + orow]);
        c2[i] = *reinterpret_cast<const v2f*>(&lds[L_CMB + i * 256 + orow + 32]);
      }
      const float Mx = fmaxf(fmaxf(fmaxf(cm[0][0], cm[1][0]), fmaxf(cm[2][0], cm[3][0])), cm[4][0]);
      float L = 0.f;
      v2f u = {0.f, 0.f}, u2 = {0.f, 0.f};
#pragma unroll
      for (int i = 0; i < 5; ++i) {
        const float f = __builtin_amdgcn_exp2f(cm[i][0] - Mx);
        L = fmaf(f, cm[i][1], L);
        u += f * co[i]; u2 += f * c2[i];
      }
      const float rl = rcp(L);
      float acc[8];
#pragma unroll
      for (int j = 0; j < 8; ++j) acc[j] = 0.f;
      fmap(acc, wor[0], wor[1], cvtpk(u[0] * rl, u[1] * rl));
      fmap(acc, wor[2], wor[3], cvtpk(u2[0] * rl, u2[1] * rl));
      float r4[4], r2[2];
#pragma unroll
      for (int j = 0; j < 4; ++j) r4[j] = sw32_sum(acc[j], acc[j + 4]);
#pragma unroll
      for (int j = 0; j < 2; ++j) r2[j] = tr_step<X7>(r4[j], r4[j + 2], (lane & 4) != 0);
      float r1 = tr_step<X2>(r2[0], r2[1], (lane & 2) != 0);
      r1 += dpp<X1>(r1);
      if ((lane & 1) == 0) lds[L_S1 + (w - 4) * 32 + (sub & 3) * 8 + (lane >> 5) * 4 + ((lane >> 2) & 1) * 2 + ((lane >> 1) & 1)] = r1;
    }
    bar();
    if (w == 0 && lane < 16) {
      v2f a[4];
#pragma unroll
      for (int ww = 0; ww < 4; ++ww) a[ww] = *reinterpret_cast<const v2f*>(&lds[L_S1 + ww * 32 + lane * 2]);
      const v2f t = (a[0] + a[1]) + (a[2] + a[3]);
      stg(rs, WS_PH + (oh * 224 + cb * 16 + lane) * 16, __float_as_uint(t[0]), __float_as_uint(t[1]), t0, t1);
    }
    STAMP(8);
    STAMP_FLUSH();
    return;
  }

  const int f = g * 4 + (s - NSP);
  const bool ffn = f < 28;
  if (tid == 0) *(volatile LAS u32*)(&lds[L_FC]) = 0u;
  const int r4 = lane >> 4;
  const v4u xv = *reinterpret_cast<const v4u*>(x + (lane < 56 ? lane : 0) * 8);
  u32 xq[7];
  v4u wa[14], wf[14], wdn[8];
  {
    const int row0 = 56 * w + 14 * r4;
#pragma unroll
    for (int i = 0; i < 7; ++i) xq[i] = *reinterpret_cast<const u32*>(x + row0 + 2 * i);
    const u16* p = wq + (size_t)row0 * NQ + 128 * f + (lane & 15) * 8;
#pragma unroll
    for (int i = 0; i < 14; ++i) wa[i] = ldnt(p + (size_t)i * NQ);
    SB();
    bar();
    SB();
    const int fc = ffn ? f : 0;
    const u16* pf = (((lane >> 3) & 1) ? wu : wg) + (size_t)row0 * FF + 64 * fc + (lane & 7) * 8;
    float xf[8];
#pragma unroll
    for (int j = 0; j < 4; ++j) { xf[2 * j] = bflo(xv[j]); xf[2 * j + 1] = bfhi(xv[j]); }
    float sm = 0.f;
#pragma unroll
    for (int j = 0; j < 8; ++j) sm += xf[j];
    sm = wave_sum(lane < 56 ? sm : 0.f);
    const float mean = sm * (1.0f / D);
    float sq = 0.f;
#pragma unroll
    for (int j = 0; j < 8; ++j) { const float d = xf[j] - mean; sq += d * d; }
    sq = wave_sum(lane < 56 ? sq : 0.f);
    const float mb = rbf(mean), den = rbf(sqrtf(rbf(rbf(sq * (1.0f / D)) + 1e-5f)));
    float acc[8];
#pragma unroll
    for (int j = 0; j < 8; ++j) acc[j] = 0.f;
#pragma unroll
    for (int i = 0; i < 7; ++i) {
      const u32 n = cvtpk(rbf(bflo(xq[i]) - mb) / den, rbf(bfhi(xq[i]) - mb) / den);
      pairup(wa[2 * i], wa[2 * i + 1]);
      fmap(acc, wa[2 * i], wa[2 * i + 1], n);
    }
    float a4[4];
#pragma unroll
    for (int j = 0; j < 4; ++j) a4[j] = sw32_sum(acc[j], acc[j + 4]);
    v2f a2;
#pragma unroll
    for (int j = 0; j < 2; ++j) a2[j] = sw16_sum(a4[j], a4[j + 2]);
    *reinterpret_cast<v2f*>(&lds[L_QR + w * 128 + (lane & 15) * 8 + (lane >> 5) * 4 + ((lane >> 4) & 1) * 2]) = a2;
    bar();
    TAG();
    if (w == 0 && lane < 32) {
      v4f qv = *reinterpret_cast<const v4f*>(&lds[L_QR + lane * 4]);
#pragma unroll
      for (int ww = 1; ww < 8; ++ww) qv += *reinterpret_cast<const v4f*>(&lds[L_QR + ww * 128 + lane * 4]);
      stg(rs, WS_Q + (f * 32 + lane) * 16, cvtpk(qv[0], qv[1]), cvtpk(qv[2], qv[3]), t0, t1);
    }
    STAMP(1);
    if (!ffn) { STAMP_FLUSH(); return; }
    SB();
#pragma unroll
    for (int i = 0; i < 14; ++i) wf[i] = ldnt(pf + (size_t)i * FF);
    SB();
  }
  const int l56 = lane < 56 ? lane : 0;
  {
    const u16* pd = wd + (size_t)(64 * f + 8 * w) * D + 8 * l56;
#pragma unroll
    for (int i = 0; i < 8; ++i) wdn[i] = ldnt(pd + i * D);
    SB();
  }
#pragma unroll
  for (int i = 0; i < 7; ++i) { pairup(wf[2 * i], wf[2 * i + 1]); asm volatile("" : "+v"(wf[2 * i]), "+v"(wf[2 * i + 1])); }
#pragma unroll
  for (int i = 0; i < 4; ++i) { pairup(wdn[2 * i], wdn[2 * i + 1]); asm volatile("" : "+v"(wdn[2 * i]), "+v"(wdn[2 * i + 1])); }
  const int cu = (lane & 7) * 8 + (lane >> 5) * 4 + ((lane >> 4) & 1) * 2, mt = (lane >> 3) & 1;
  {
    float acc[8];
#pragma unroll
    for (int j = 0; j < 8; ++j) acc[j] = 0.f;
#pragma unroll
    for (int i = 0; i < 7; ++i) fmap(acc, wf[2 * i], wf[2 * i + 1], 0x3f803f80u);
    float a4[4];
#pragma unroll
    for (int j = 0; j < 4; ++j) a4[j] = sw32_sum(acc[j], acc[j + 4]);
#pragma unroll
    for (int j = 0; j < 2; ++j) lds[L_QR + w * 128 + (cu + j) * 2 + mt] = sw16_sum(a4[j], a4[j + 2]);
  }
  bar();
  const int u = 8 * w + (lane & 7);
  v2f SGU = {0.f, 0.f};
#pragma unroll
  for (int ww = 0; ww < 8; ++ww) SGU += *reinterpret_cast<const v2f*>(&lds[L_QR + ww * 128 + u * 2]);
  asm volatile("" : "+v"(SGU));
  {
    const u32 pt0 = __builtin_amdgcn_raw_buffer_load_b32(rs, WS_PD + (f * 224 + (tid < 224 ? tid : 0)) * 16, 0, AUX_SC1);
    const u32 pt1 = *reinterpret_cast<const volatile u32*>(out + 16 * f);
    asm volatile("" :: "v"(pt0), "v"(pt1));
  }
  TAG();
  STAMP(2);
  {
    const int hsub = lane >> 5;
    const bool act = (lane & 31) < 28;
    const int pp = act ? (lane & 31) : 0;
    int off[8];
    v4u gr[8];
    const u32 xp = *reinterpret_cast<const u32*>(x + 56 * w + 2 * pp);
    LAS u32* const xw = (LAS u32*)(&lds[L_XN + w * 32 + (pp / 7) * 8 + pp % 7]);
    const LAS v4u* const xr = (const LAS v4u*)(&lds[L_XN + w * 32 + r4 * 8]);
    const int cp = 28 * w + pp - 8 * f;
    const bool keep = lane < 28 && cp >= 0 && cp < 8;
    float acc[10];
#pragma unroll
    for (int j = 0; j < 10; ++j) { acc[j] = 0.f; asm volatile("" : "+v"(acc[j])); }
#pragma unroll
    for (int i = 0; i < 8; ++i) { off[i] = act ? WS_PH + ((2 * i + hsub) * 224 + 28 * w + pp) * 16 : OOB; gr[i] = ldg(rs, off[i]); }
#pragma unroll
    for (int i = 0; i < 8; ++i) mark(gr[i], !act, tag);
    poll<8, 64>(srd, off, gr, tag);
    STAMP(9);
    v2f hp[8];
#pragma unroll
    for (int i = 0; i < 8; ++i) hp[i] = v2f{__uint_as_float(gr[i][0]), __uint_as_float(gr[i][1])};
    const v2f hs = ((hp[0] + hp[1]) + (hp[2] + hp[3])) + ((hp[4] + hp[5]) + (hp[6] + hp[7]));
    const u32 a01 = cvtpk(sw32_sum(hs[0], hs[0]), sw32_sum(hs[1], hs[1]));
    const u32 h01 = cvtpk(bflo(xp) + bflo(a01), bfhi(xp) + bfhi(a01));
    if (lane < 28) *xw = h01;
    asm volatile("s_waitcnt lgkmcnt(0)" ::: "memory");
    const v4u na = xr[0], nb = xr[1];
    if (keep) reinterpret_cast<u32*>(&lds[L_HS])[cp] = h01;
#pragma unroll
    for (int i = 0; i < 7; ++i) {
      const u32 n = i < 4 ? na[i] : nb[i - 4];
#pragma unroll
      for (int j = 0; j < 4; ++j) { acc[j] = dot2(wf[2 * i][j], n, acc[j]); acc[4 + j] = dot2(wf[2 * i + 1][j], n, acc[4 + j]); }
      acc[8] = dot2(n, 0x3f803f80u, acc[8]);
      acc[9] = dot2(n, n, acc[9]);
    }
    float a4[4];
#pragma unroll
    for (int j = 0; j < 4; ++j) a4[j] = sw32_sum(acc[j], acc[j + 4]);
    float st = sw32_sum(acc[8], acc[9]);
    st = sw16_sum(st, st);
#pragma unroll
    for (int j = 0; j < 2; ++j) lds[L_GU + w * 128 + (cu + j) * 2 + mt] = sw16_sum(a4[j], a4[j + 2]);
    if ((lane & 31) == 0) lds[L_ST + w * 2 + hsub] = st;
  }
  float dacc[8];
#pragma unroll
  for (int j = 0; j < 8; ++j) { dacc[j] = 0.f; asm volatile("" : "+v"(dacc[j])); }
  bar();
  {
    v2f gu[8];
    v4f sv[4];
#pragma unroll
    for (int i = 0; i < 4; ++i) sv[i] = *reinterpret_cast<const v4f*>(&lds[L_ST + i * 4]);
#pragma unroll
    for (int ww = 0; ww < 8; ++ww) gu[ww] = *reinterpret_cast<const v2f*>(&lds[L_GU + ww * 128 + u * 2]);
    const v4f s4 = (sv[0] + sv[1]) + (sv[2] + sv[3]);
    const float mean = (s4[0] + s4[2]) * (1.0f / D);
    const float var = fmaxf(fmaf(-mean, mean, (s4[1] + s4[3]) * (1.0f / D)), 0.f);
    const float mb = rbf(mean), rden = rcp(rbf(__builtin_amdgcn_sqrtf(rbf(rbf(var) + 1e-5f))));
    const v2f GUs = ((gu[0] + gu[1]) + (gu[2] + gu[3])) + ((gu[4] + gu[5]) + (gu[6] + gu[7]));
    const v2f GUn = (GUs - mb * SGU) * rden;
    const u32 gub = cvtpk(GUn[0], GUn[1]);
    const float G = bflo(gub), U = bfhi(gub);
    const float z2 = G * fmaf(G * G, 0.044715f * 0.7978845608028654f * 2.0f * LOG2E, 0.7978845608028654f * 2.0f * LOG2E);
    const float sg = rcp(__builtin_amdgcn_exp2f(z2) + 1.0f);
    const float av = rbf(fmaf(-G, sg, G)) * U;
    const u32 pk = cvtpk(av, dpp<X1>(av));
#pragma unroll
    for (int i = 0; i < 4; ++i) fmap(dacc, wdn[2 * i], wdn[2 * i + 1], __builtin_amdgcn_readlane(pk, 2 * i));
    if (lane < 56) {
      *reinterpret_cast<v4f*>(&lds[L_DN + w * 448 + lane * 8]) = v4f{dacc[0], dacc[1], dacc[2], dacc[3]};
      *reinterpret_cast<v4f*>(&lds[L_DN + w * 448 + lane * 8 + 4]) = v4f{dacc[4], dacc[5], dacc[6], dacc[7]};
    }
  }
  bar();
  if (tid < 224) {
    v2f a[8];
#pragma unroll
    for (int ww = 0; ww < 8; ++ww) a[ww] = *reinterpret_cast<const v2f*>(&lds[L_DN + ww * 448 + tid * 2]);
    const v2f t = ((a[0] + a[1]) + (a[2] + a[3])) + ((a[4] + a[5]) + (a[6] + a[7]));
    stg(rs, WS_PD + (f * 224 + tid) * 16, __float_as_uint(t[0]), __float_as_uint(t[1]), t0, t1);
  }
  STAMP(10);
  if (w < 7) {
    int off[1];
    v4u gr[1];
    off[0] = lane < 32 ? WS_PD + ((4 * w + (lane >> 3)) * 224 + 8 * f + (lane & 7)) * 16 : OOB;
    gr[0] = ldg(rs, off[0]);
    mark(gr[0], lane >= 32, tag);
    poll<1, 64>(srd, off, gr, tag);
    STAMP(11);
    float a0 = __uint_as_float(gr[0][0]), a1 = __uint_as_float(gr[0][1]);
    float r = sw16_sum(a0, a1);
    r += dpp<X8>(r);
    if (lane < 32 && (lane & 8) == 0) lds[L_FN + w * 16 + (lane & 7) * 2 + (lane >> 4)] = r;
    asm volatile("s_waitcnt lgkmcnt(0)" ::: "memory");
    u32 old = 0;
    if (lane == 0) old = __hip_atomic_fetch_add((LAS u32*)(&lds[L_FC]), 1u, __ATOMIC_RELAXED, __HIP_MEMORY_SCOPE_WORKGROUP);
    if (__builtin_amdgcn_readfirstlane(old) == 6u) {
      asm volatile("" ::: "memory");
      if (lane < 8) {
        v2f a[7];
#pragma unroll
        for (int ww = 0; ww < 7; ++ww) a[ww] = *reinterpret_cast<const v2f*>(&lds[L_FN + ww * 16 + lane * 2]);
        const v2f t = ((a[0] + a[1]) + (a[2] + a[3])) + ((a[4] + a[5]) + a[6]);
        const u32 hv = reinterpret_cast<const u32*>(&lds[L_HS])[lane];
        const u32 tb = cvtpk(t[0], t[1]);
        *reinterpret_cast<u32*>(out + 16 * f + lane * 2) = cvtpk(bflo(hv) + bflo(tb), bfhi(hv) + bfhi(tb));
        if (f == 0 && lane == 0) __builtin_amdgcn_raw_buffer_store_b64(v2u{t0, t1}, rs, WS_EPOCH, 0, AUX_SC1);
      }
    }
  }
  STAMP(12);
  STAMP_FLUSH();
  if (f == 0 && w == 7) {
    u64 ts = __builtin_amdgcn_s_memrealtime(); while (__builtin_amdgcn_s_memrealtime() - ts < 800) __builtin_amdgcn_s_sleep(10);
    u64 mn = ~0ull, mx = 0;
    const u64 ref = stamps[0];
    for (int i = lane; i < 2048; i += 64) {
      u64 t = __hip_atomic_load((u64*)(ws + WS_STAMP + (i * 16) * 8), __ATOMIC_RELAXED, __HIP_MEMORY_SCOPE_AGENT); if (t < mn) mn = t;
      u64 v = __hip_atomic_load((u64*)(ws + WS_STAMP + (i * 16 + 0) * 8), __ATOMIC_RELAXED, __HIP_MEMORY_SCOPE_AGENT); if (((i >> 6) >= 28) == (0 == 1) && v + 1000 > ref && v < ref + 100000 && v > mx) mx = v; }
    for (int o = 32; o > 0; o >>= 1) { u64 a = __shfl_xor(mn, o), c2 = __shfl_xor(mx, o); mn = a < mn ? a : mn; mx = c2 > mx ? c2 : mx; }
    const u64 span = mx - mn;
    ts = __builtin_amdgcn_s_memrealtime(); while (__builtin_amdgcn_s_memrealtime() - ts < span * 50) __builtin_amdgcn_s_sleep(10);
  }
}

}

extern "C" void kernel_launch(void* const* d_in, const int* in_sizes, int n_in, void* d_out, int out_size, void* d_ws,
                              size_t ws_size, hipStream_t stream) {
  fused_decode_block<<<256, 512, 0, stream>>>((const u16*)d_in[0], (const u16*)d_in[1], (const u16*)d_in[2], (const u16*)d_in[3],
                                              (const u16*)d_in[4], (const u16*)d_in[6], (const u16*)d_in[7], (const u16*)d_in[8],
                                              (u16*)d_out, (unsigned char*)d_ws);
}
```

```cpp
#include <hip/hip_runtime.h>
#include <hip/hip_bf16.h>
#include <stdint.h>

typedef unsigned short u16;
typedef unsigned int u32;
typedef unsigned long long u64;
typedef u32 v4u __attribute__((ext_vector_type(4)));
typedef u32 v2u __attribute__((ext_vector_type(2)));
typedef float v4f __attribute__((ext_vector_type(4)));
typedef float v2f __attribute__((ext_vector_type(2)));
typedef __bf16 bf16x2 __attribute__((ext_vector_type(2)));
typedef __bf16 bf16x8 __attribute__((ext_vector_type(8)));
typedef short s16x4 __attribute__((ext_vector_type(4)));

#define LAS __attribute__((address_space(3)))
#define PRETOUCH 1
#define FEW 4
#define GSL 4
#define DIAG 0

namespace {

constexpr int D = 448, SK = 8192, DH = 256, FF = 1792, NQ = 4096, NSP = 28;
constexpr int WS_EPOCH = 0;
constexpr int WS_Q = 4096;
constexpr int WS_ML = 32768;
constexpr int WS_PO = 131072;
constexpr int WS_PH = WS_PO + 524288;
constexpr int WS_PD = WS_PH + 65536;
constexpr int WS_END = WS_PD + 28 * 3584;
constexpr int WS_STAMP = 1 << 20;
constexpr int AUX_SC1 = 16, AUX_NT = 2;
constexpr int OOB = 0x7ffffff0;
constexpr u32 SPIN_CAP = 1u << 17;
constexpr float LOG2E = 1.4426950408889634f;

constexpr int L_QS = 0;
constexpr int L_WMAX = 256;
constexpr int L_WSUM = 272;
constexpr int L_S1 = 288;
constexpr int L_FLAG = 544;
constexpr int L_CML = 548;
constexpr int L_ORED = 576;
constexpr int L_P = L_ORED + 2048;
constexpr int L_CMB = L_P + 2560;
constexpr int L_QST = L_CMB + 5 * 256;
constexpr int L_END = L_QST + 4 * 512;
constexpr int L_QR = 0;
constexpr int L_ST = 1024;
constexpr int L_XN = 1040;
constexpr int L_HS = 1296;
constexpr int L_GU = 1312;
constexpr int L_DN = 2336;
constexpr int L_FN = 0;
constexpr int L_FC = 5920;

__device__ __forceinline__ float bflo(u32 u) { return __uint_as_float(u << 16); }
__device__ __forceinline__ float bfhi(u32 u) { return __uint_as_float(u & 0xffff0000u); }
__device__ __forceinline__ u32 cvtpk(float lo, float hi) {
  v2f v = {lo, hi};
  return __builtin_bit_cast(u32, __builtin_convertvector(v, bf16x2));
}
__device__ __forceinline__ float rbf(float f) { return bflo(cvtpk(f, 0.f)); }
__device__ __forceinline__ float dot2(u32 w, u32 a, float acc) {
  return __builtin_amdgcn_fdot2_f32_bf16(__builtin_bit_cast(bf16x2, w), __builtin_bit_cast(bf16x2, a), acc, false);
}
__device__ __forceinline__ float bsel(u32 m, float a, float b) {
  return __uint_as_float((__float_as_uint(a) & m) | (__float_as_uint(b) & ~m));
}
template <int CTRL> __device__ __forceinline__ float dpp(float x) {
  return __builtin_bit_cast(float, __builtin_amdgcn_mov_dpp(__builtin_bit_cast(int, x), CTRL, 0xf, 0xf, true));
}
constexpr int X1 = 0xB1, X2 = 0x4E, X7 = 0x141, X8 = 0x128;
__device__ __forceinline__ float sw32_sum(float a, float b) {
  auto r = __builtin_amdgcn_permlane32_swap(__float_as_uint(a), __float_as_uint(b), false, false);
  return __uint_as_float(r[0]) + __uint_as_float(r[1]);
}
__device__ __forceinline__ float sw16_sum(float a, float b) {
  auto r = __builtin_amdgcn_permlane16_swap(__float_as_uint(a), __float_as_uint(b), false, false);
  return __uint_as_float(r[0]) + __uint_as_float(r[1]);
}
__device__ __forceinline__ float sw32_max(float a) {
  auto r = __builtin_amdgcn_permlane32_swap(__float_as_uint(a), __float_as_uint(a), false, false);
  return fmaxf(__uint_as_float(r[0]), __uint_as_float(r[1]));
}
__device__ __forceinline__ float sw16_max(float a) {
  auto r = __builtin_amdgcn_permlane16_swap(__float_as_uint(a), __float_as_uint(a), false, false);
  return fmaxf(__uint_as_float(r[0]), __uint_as_float(r[1]));
}
template <int CTRL> __device__ __forceinline__ float tr_step(float lo, float hi, bool sel) {
  const float t1 = lo + dpp<CTRL>(lo), t2 = hi + dpp<CTRL>(hi);
  return sel ? t2 : t1;
}
__device__ __forceinline__ float wave_sum(float x) {
  x += dpp<X1>(x); x += dpp<X2>(x); x += dpp<X7>(x); x += dpp<X8>(x);
  x = sw16_sum(x, x); return sw32_sum(x, x);
}
__device__ __forceinline__ v4u ldnt(const u16* p) { return __builtin_nontemporal_load(reinterpret_cast<const v4u*>(p)); }
__device__ __forceinline__ v4u ldb(__amdgpu_buffer_rsrc_t rs, int off) { return __builtin_amdgcn_raw_buffer_load_b128(rs, off, 0, AUX_NT); }
__device__ __forceinline__ v4u ldg(__amdgpu_buffer_rsrc_t rs, int off) { return __builtin_amdgcn_raw_buffer_load_b128(rs, off, 0, AUX_SC1); }
__device__ __forceinline__ void stg(__amdgpu_buffer_rsrc_t rs, int off, u32 a, u32 b, u32 t0, u32 t1) {
  v4u v = {a, b, t0, t1};
  __builtin_amdgcn_raw_buffer_store_b128(v, rs, off, 0, AUX_SC1);
}
__device__ __forceinline__ void bar() {
  asm volatile("s_waitcnt lgkmcnt(0)" ::: "memory");
  __builtin_amdgcn_s_barrier();
  asm volatile("" ::: "memory");
}
__device__ __forceinline__ v4f mfma(v4u a, v4u b, v4f c) {
  return __builtin_amdgcn_mfma_f32_16x16x32_bf16(__builtin_bit_cast(bf16x8, a), __builtin_bit_cast(bf16x8, b), c, 0, 0, 0);
}
__device__ __forceinline__ v4f mfma16(v2u a, v2u b, v4f c) {
  return __builtin_amdgcn_mfma_f32_16x16x16bf16_1k(__builtin_bit_cast(s16x4, a), __builtin_bit_cast(s16x4, b), c, 0, 0, 0);
}
__device__ __forceinline__ u32 plo(u32 r0, u32 r1) { return __builtin_amdgcn_perm(r1, r0, 0x05040100u); }
__device__ __forceinline__ u32 phi(u32 r0, u32 r1) { return __builtin_amdgcn_perm(r1, r0, 0x07060302u); }
__device__ __forceinline__ void pairup(v4u& a, v4u& b) {
  v4u x, y;
  x[0] = plo(a[0], b[0]); x[1] = phi(a[0], b[0]); x[2] = plo(a[1], b[1]); x[3] = phi(a[1], b[1]);
  y[0] = plo(a[2], b[2]); y[1] = phi(a[2], b[2]); y[2] = plo(a[3], b[3]); y[3] = phi(a[3], b[3]);
  a = x; b = y;
}
__device__ __forceinline__ void fmap(float (&acc)[8], v4u a, v4u b, u32 n) {
#pragma unroll
  for (int j = 0; j < 4; ++j) { acc[j] = dot2(a[j], n, acc[j]); acc[4 + j] = dot2(b[j], n, acc[4 + j]); }
}
__device__ __forceinline__ u64 gtag(const v4u& g) { return ((u64)g[3] << 32) | (u64)g[2]; }
__device__ __forceinline__ void mark(v4u& g, bool unused, u64 tag) {
  g[2] = unused ? (u32)tag : g[2]; g[3] = unused ? (u32)(tag >> 32) : g[3];
}
__device__ __forceinline__ void reload(v4u (&g)[1], const int (&o)[1], const u64 (&m)[1], v4u srd) {
  asm volatile("s_mov_b64 exec, %2\n\tbuffer_load_dwordx4 %0, %1, %3, 0 offen sc1\n\ts_mov_b64 exec, -1\n\ts_waitcnt vmcnt(0)"
               : "+v"(g[0]) : "v"(o[0]), "s"(m[0]), "s"(srd) : "memory");
}
__device__ __forceinline__ void reload(v4u (&g)[8], const int (&o)[8], const u64 (&m)[8], v4u srd) {
  asm volatile("s_mov_b64 exec, %16\n\tbuffer_load_dwordx4 %0, %8, %24, 0 offen sc1\n\t"
               "s_mov_b64 exec, %17\n\tbuffer_load_dwordx4 %1, %9, %24, 0 offen sc1\n\t"
               "s_mov_b64 exec, %18\n\tbuffer_load_dwordx4 %2, %10, %24, 0 offen sc1\n\t"
               "s_mov_b64 exec, %19\n\tbuffer_load_dwordx4 %3, %11, %24, 0 offen sc1\n\t"
               "s_mov_b64 exec, %20\n\tbuffer_load_dwordx4 %4, %12, %24, 0 offen sc1\n\t"
               "s_mov_b64 exec, %21\n\tbuffer_load_dwordx4 %5, %13, %24, 0 offen sc1\n\t"
               "s_mov_b64 exec, %22\n\tbuffer_load_dwordx4 %6, %14, %24, 0 offen sc1\n\t"
               "s_mov_b64 exec, %23\n\tbuffer_load_dwordx4 %7, %15, %24, 0 offen sc1\n\t"
               "s_mov_b64 exec, -1\n\ts_waitcnt vmcnt(0)"
               : "+v"(g[0]), "+v"(g[1]), "+v"(g[2]), "+v"(g[3]), "+v"(g[4]), "+v"(g[5]), "+v"(g[6]), "+v"(g[7])
               : "v"(o[0]), "v"(o[1]), "v"(o[2]), "v"(o[3]), "v"(o[4]), "v"(o[5]), "v"(o[6]), "v"(o[7]), "s"(m[0]), "s"(m[1]), "s"(m[2]), "s"(m[3]), "s"(m[4]), "s"(m[5]), "s"(m[6]), "s"(m[7]), "s"(srd) : "memory");
}
template <int N, int MANY> __device__ __forceinline__ u32 poll(v4u srd, const int (&off)[N], v4u (&gr)[N], u64 tag) {
  u32 spins = 0;
  u64 m[N], any = 0;
#pragma unroll
  for (int i = 0; i < N; ++i) { m[i] = __ballot(gtag(gr[i]) != tag); any |= m[i]; }
  if (__builtin_expect(any != 0, 0)) {
    do {
      if (MANY < 64 && __builtin_popcountll(any) > MANY) __builtin_amdgcn_s_sleep(8);
      reload(gr, off, m, srd);
      any = 0;
#pragma unroll
      for (int i = 0; i < N; ++i) { m[i] = __ballot(gtag(gr[i]) != tag); any |= m[i]; }
    } while (any != 0 && ++spins < SPIN_CAP);
    ++spins;
  }
  return spins;
}
__device__ __forceinline__ float rcp(float x) { return __builtin_amdgcn_rcpf(x); }
#define STAMP(i) do { } while (0)
#define STAMP_FLUSH() do { } while (0)
#define SB() __builtin_amdgcn_sched_barrier(0)
#define TAG() const u64 tag = (((u64)(u32)__builtin_amdgcn_readfirstlane(ep[1]) << 32) | (u64)(u32)__builtin_amdgcn_readfirstlane(ep[0])) + 1; \
  const u32 t0 = (u32)tag, t1 = (u32)(tag >> 32)

__global__ void __launch_bounds__(512, 2)
fused_decode_block(const u16* __restrict__ x, const u16* __restrict__ Kc, const u16* __restrict__ Vc, const u16* __restrict__ wq,
                   const u16* __restrict__ wo, const u16* __restrict__ wg, const u16* __restrict__ wu, const u16* __restrict__ wd,
                   u16* __restrict__ out, unsigned char* ws) {
  __shared__ __attribute__((aligned(16))) float lds[L_END];
  asm volatile("" :: "s"(x), "s"(Kc), "s"(Vc), "s"(wq), "s"(wo), "s"(wg), "s"(wu), "s"(wd), "s"(out), "s"(ws));
  const int tid = threadIdx.x, lane = tid & 63;
  const int w = __builtin_amdgcn_readfirstlane(tid >> 6);
  const int g = blockIdx.x & 7, s = ((blockIdx.x >> 3) + 28) & 31, b = s * 8 + g;
  const __amdgpu_buffer_rsrc_t rs = __builtin_amdgcn_make_buffer_rsrc((void*)ws, 0, WS_END, 0x00020000);
  const v4u srd = {(u32)(uintptr_t)ws, (u32)((uintptr_t)ws >> 32) & 0xffffu, (u32)WS_END, 0x00020000u};
  STAMP(0);
  const v2u ep = __builtin_amdgcn_raw_buffer_load_b64(rs, WS_EPOCH, 0, AUX_SC1);

  if (s < NSP) {
    volatile LAS u32* lflag = (volatile LAS u32*)(&lds[L_FLAG]);
    if (tid == 0) { lflag[0] = 0u; lflag[1] = 0u; lflag[2] = 0u; lflag[3] = 0u; }
    __builtin_amdgcn_s_setprio(1);
    bar();
    const int g4 = lane >> 4, c = lane & 15;
    const int tile0 = (256 * s) / 7, nx = (256 * (s + 1)) / 7 - tile0 - 36;
    const int wi = w & 3;
    const bool ten = wi < nx;
    const int key0 = (tile0 + 9 * wi + (wi ? nx : 0)) * 8;
    const int oh = 2 * g + s / 14, cb = s % 14;
    const int sub = lane >> 3, gq = lane & 7;
    const int orow = 64 * wi + 4 * gq + 2 * (lane >> 5);
    v4u wor[4];
    if (w < 4) {
      const __amdgpu_buffer_rsrc_t rk = __builtin_amdgcn_make_buffer_rsrc((void*)Kc, 0, 8 * SK * DH * 2, 0x00020000);
      v4u kr[40];
      const int kb = ((g * SK + key0 + (c >> 1)) * DH + (c & 1) * 32 + g4 * 8) * 2;
#pragma unroll
      for (int t = 0; t < 9; ++t)
#pragma unroll
        for (int ks = 0; ks < 4; ++ks) kr[t * 4 + ks] = ldb(rk, kb + t * 4096 + ks * 128);
#pragma unroll
      for (int ks = 0; ks < 4; ++ks) kr[36 + ks] = ldb(rk, ten ? kb + 9 * 4096 + ks * 128 : OOB);
      SB();
      STAMP(1);
      TAG();
      {
        __builtin_amdgcn_s_setprio(3);
        const int qo0 = WS_Q + (g * 128 + lane) * 16;
        const u32 st0 = (u32)(uintptr_t)(LAS float*)(&lds[L_QST + w * 512]);
        const u32 fl = (u32)(uintptr_t)(LAS float*)(&lds[L_FLAG]);
#define Q_SAMPLE(pend) do { u32 keep_, tmp_, vt_; \
          asm volatile("s_mov_b32 %1, m0\n\t" \
                       "s_mov_b32 m0, %5\n\ts_nop 0\n\tbuffer_load_dwordx4 %4, %6, 0 offen sc1 lds\n\t" \
                       "buffer_load_dwordx4 %4, %6, 0 offen offset:1024 sc1 lds\n\t"   \
                       "s_mov_b32 m0, %1\n" \
                       "1:\n\t" \
                       "s_getreg_b32 %0, hwreg(HW_REG_IB_STS)\n\ts_and_b32 %0, %0, 0xc0000f\n\ts_cbranch_scc0 2f\n\t" \
                       "ds_read_b32 %3, %7\n\ts_waitcnt lgkmcnt(0)\n\tv_readfirstlane_b32 %2, %3\n\ts_cmp_eq_u32 %2, 0\n\ts_cbranch_scc1 1b\n" \
                       "2:" \
                       : "=&s"(pend), "=&s"(keep_), "=&s"(tmp_), "=&v"(vt_) : "v"(qo0), "s"(st0), "s"(srd), "v"(fl) : "memory", "scc"); } while (0)
        u32 pend;
        Q_SAMPLE(pend);
        __builtin_amdgcn_s_waitcnt(0x0F70);
        pend = 0u;
        for (u32 sp = 0; pend == 0u && lflag[0] == 0u && sp < SPIN_CAP; ++sp) {
          asm volatile("" ::: "memory");
          const v4u q0 = *reinterpret_cast<const v4u*>(&lds[L_QST + w * 512 + lane * 4]);
          const v4u q1 = *reinterpret_cast<const v4u*>(&lds[L_QST + w * 512 + 256 + lane * 4]);
          if (__ballot(gtag(q0) != tag || gtag(q1) != tag) == 0) {
            *reinterpret_cast<v2u*>(&lds[L_QS + lane * 2]) = v2u{q0[0], q0[1]};
            *reinterpret_cast<v2u*>(&lds[L_QS + (64 + lane) * 2]) = v2u{q1[0], q1[1]};
            asm volatile("s_waitcnt lgkmcnt(0)" ::: "memory");
            lflag[0] = 1u;
            break;
          }
          Q_SAMPLE(pend);
        }
        __builtin_amdgcn_s_setprio(1);
      }
      asm volatile("" ::: "memory");
      STAMP(2);
      const int hd = (c >> 2) & 1;
      const u32 meta = ((c >> 1) & 1) ? 0xffffffffu : 0u, mvar = (c & 1) ? 0xffffffffu : 0u;
      float sc[20];
      {
        v4u qf[4];
#pragma unroll
        for (int ks = 0; ks < 4; ++ks) qf[ks] = *reinterpret_cast<const v4u*>(&lds[L_QS + hd * 128 + ks * 32 + ((c >> 1) & 1) * 16 + g4 * 4]);
#pragma unroll
        for (int t = 0; t < 10; ++t) {
          v4f d = {0.f, 0.f, 0.f, 0.f};
#pragma unroll
          for (int ks = 0; ks < 4; ++ks) d = mfma(kr[t * 4 + ks], qf[ks], d);
          const float sa = bsel(meta, d[1], d[0]), sb = bsel(meta, d[3], d[2]);
          const u32 sp = cvtpk(sa + dpp<X2>(sa), sb + dpp<X2>(sb));
          sc[2 * t] = bflo(sp) * (0.0625f * LOG2E);
          sc[2 * t + 1] = bfhi(sp) * (0.0625f * LOG2E);
        }
        if (!ten) { sc[18] = -INFINITY; sc[19] = -INFINITY; }
      }
      float m = sc[0];
#pragma unroll
      for (int j = 1; j < 20; ++j) m = fmaxf(m, sc[j]);
      m = sw32_max(sw16_max(m));
      if ((lane & 0x3b) == 0) lds[L_WMAX + w * 2 + hd] = m;
      asm volatile("s_waitcnt lgkmcnt(0)" ::: "memory");
      if (lane == 0) __hip_atomic_fetch_add((LAS u32*)(&lds[L_FLAG + 2]), 1u, __ATOMIC_RELAXED, __HIP_MEMORY_SCOPE_WORKGROUP);
      for (u32 sp = 0; lflag[2] < 4u && sp < (SPIN_CAP << 4); ++sp) __builtin_amdgcn_s_sleep(1);
      asm volatile("" ::: "memory");
      m = fmaxf(fmaxf(lds[L_WMAX + hd], lds[L_WMAX + 2 + hd]), fmaxf(lds[L_WMAX + 4 + hd], lds[L_WMAX + 6 + hd]));
      {
        float l = 0.f;
        LAS u32* const pw = (LAS u32*)(&lds[L_P + w * 640 + lane]);
#pragma unroll
        for (int t = 0; t < 10; ++t) {
          const float p0 = __builtin_amdgcn_exp2f(sc[2 * t] - m), p1 = __builtin_amdgcn_exp2f(sc[2 * t + 1] - m);
          l += p0 + p1;
          const u32 hi = cvtpk(p0, p1);
          const u32 lo = cvtpk(p0 - bflo(hi), p1 - bfhi(hi));
          pw[t * 64] = (lo & mvar) | (hi & ~mvar);
        }
        l = sw16_sum(l, l); l = sw32_sum(l, l);
        if ((lane & 0x3b) == 0) lds[L_WSUM + w * 2 + hd] = l;
        asm volatile("s_waitcnt lgkmcnt(0)" ::: "memory");
        if (lane == 0) __hip_atomic_fetch_add((LAS u32*)(&lds[L_FLAG + 3]), 1u, __ATOMIC_RELAXED, __HIP_MEMORY_SCOPE_WORKGROUP);
      }
      STAMP(4);
      __builtin_amdgcn_s_setprio(0);
      v4u po[8];
      int poff[8];
      const int mysp = w + 4 * (lane & 7);
      const bool mlwant = lane < 7 && mysp != s;
      poff[7] = WS_ML + (oh * 32 + mysp) * 128;
      po[7] = v4u{mlwant ? 0u : 0xff800000u, 0u, ~t0, 0u};
      mark(po[7], !mlwant, tag);
#pragma unroll
      for (int i = 0; i < 7; ++i) {
        po[i] = v4u{0u, 0u, ~t0, 0u};
        mark(po[i], w + 4 * i == s, tag);
        poff[i] = WS_PO + ((oh * 32 + w + 4 * i) * 64 + lane) * 16;
      }
      for (u32 sp = 0; sp < SPIN_CAP; ++sp) {
        u64 pm[8], pany = 0;
#pragma unroll
        for (int i = 0; i < 8; ++i) { pm[i] = __ballot(gtag(po[i]) != tag); pany |= pm[i]; }
        if (pany == 0) break;
        const bool few = __builtin_popcountll(pm[7]) <= FEW;
        if (GSL && !few && sp) __builtin_amdgcn_s_sleep(GSL);
#pragma unroll
        for (int i = 0; i < 7; ++i)
          if (!few && ((pm[7] >> i) & 1)) pm[i] = 0ull;
        reload(po, poff, pm, srd);
      }
      const v4u ml = po[7];
      STAMP(7);
      const float mi = __uint_as_float(ml[0]);
      float mx = fmaxf(mi, dpp<X1>(mi)); mx = fmaxf(mx, dpp<X2>(mx)); mx = fmaxf(mx, dpp<X7>(mx));
      const float wt = __builtin_amdgcn_exp2f(mi - mx);
      float lw = wt * __uint_as_float(ml[1]);
      lw += dpp<X1>(lw); lw += dpp<X2>(lw); lw += dpp<X7>(lw);
      v4f o = {0.f, 0.f, 0.f, 0.f};
#pragma unroll
      for (int i = 0; i < 7; ++i) {
        const float f = __builtin_bit_cast(float, __builtin_amdgcn_readlane(__builtin_bit_cast(int, wt), i));
        o[0] = fmaf(f, bflo(po[i][0]), o[0]); o[1] = fmaf(f, bfhi(po[i][0]), o[1]);
        o[2] = fmaf(f, bflo(po[i][1]), o[2]); o[3] = fmaf(f, bfhi(po[i][1]), o[3]);
      }
      *reinterpret_cast<v4f*>(&lds[L_CMB + w * 256 + lane * 4]) = o;
      if (lane == 0) *reinterpret_cast<v2f*>(&lds[L_CML + w * 2]) = v2f{mx, lw};
#pragma unroll
      for (int i = 0; i < 4; ++i) wor[i] = v4u{0u, 0u, 0u, 0u};
    } else {
      const __amdgpu_buffer_rsrc_t rv = __builtin_amdgcn_make_buffer_rsrc((void*)Vc, 0, 8 * SK * DH * 2, 0x00020000);
      const __amdgpu_buffer_rsrc_t ro = __builtin_amdgcn_make_buffer_rsrc((void*)wo, 0, NQ * D * 2, 0x00020000);
      v4u vr[40];
      const int vb = ((g * SK + key0 + 2 * g4) * DH + 8 * c) * 2;
#pragma unroll
      for (int hf = 0; hf < 2; ++hf) {
#pragma unroll
        for (int j = 0; j < 18; ++j) vr[hf * 20 + j] = ldb(rv, vb + (8 * (j >> 1) + (j & 1)) * 512 + 256 * hf);
#pragma unroll
        for (int j = 18; j < 20; ++j) vr[hf * 20 + j] = ldb(rv, ten ? vb + (72 + (j & 1)) * 512 + 256 * hf : OOB);
        SB();
      }
      {
        const int oo = ((oh * 256 + orow) * D + 32 * cb + 8 * (sub & 3)) * 2;
        wor[0] = ldb(ro, oo); wor[1] = ldb(ro, oo + D * 2); wor[2] = ldb(ro, oo + 32 * D * 2); wor[3] = ldb(ro, oo + 33 * D * 2);
        SB();
      }
      STAMP(1);
      TAG();
      for (u32 sp = 0; lflag[3] < 4u && sp < (SPIN_CAP << 4); ++sp) __builtin_amdgcn_s_sleep(1);
      asm volatile("" ::: "memory");
      STAMP(2);
      const u32 mhi = (g4 >> 1) ? 0xffffffffu : 0u;
      v4u pa0, pa1;
      v2u pa2;
      {
        const LAS u32* const pr = (const LAS u32*)(&lds[L_P + wi * 640 + lane]);
#pragma unroll
        for (int t = 0; t < 4; ++t) { pa0[t] = pr[t * 64]; pa1[t] = pr[(4 + t) * 64]; }
        pa2[0] = pr[8 * 64]; pa2[1] = pr[9 * 64];
      }
#pragma unroll
      for (int hf = 0; hf < 2; ++hf) {
        v4f d1[8];
#pragma unroll
        for (int kk = 0; kk < 2; ++kk) {
          v4u bb[8];
#pragma unroll
          for (int t = 0; t < 4; ++t)
#pragma unroll
            for (int d = 0; d < 8; ++d)
              bb[d][t] = __builtin_amdgcn_perm(vr[hf * 20 + kk * 8 + 2 * t + 1][d >> 1], vr[hf * 20 + kk * 8 + 2 * t][d >> 1], (d & 1) ? 0x07060302u : 0x05040100u);
          SB();
#pragma unroll
          for (int d = 0; d < 8; ++d) d1[d] = mfma(kk ? pa1 : pa0, bb[d], kk ? d1[d] : v4f{0.f, 0.f, 0.f, 0.f});
          SB();
        }
        v2u b2[8];
#pragma unroll
        for (int t = 0; t < 2; ++t)
#pragma unroll
          for (int d = 0; d < 8; ++d)
            b2[d][t] = __builtin_amdgcn_perm(vr[hf * 20 + 16 + 2 * t + 1][d >> 1], vr[hf * 20 + 16 + 2 * t][d >> 1], (d & 1) ? 0x07060302u : 0x05040100u);
        SB();
        v4f d2[8];
#pragma unroll
        for (int d = 0; d < 8; ++d) d2[d] = mfma16(pa2, b2[d], v4f{0.f, 0.f, 0.f, 0.f});
        float x1[4];
#pragma unroll
        for (int e = 0; e < 4; ++e) x1[e] = bsel(mhi, d1[4 + e][0] + d1[4 + e][1], d1[e][0] + d1[e][1]);
        SB();
        v4f y;
#pragma unroll
        for (int e = 0; e < 4; ++e) y[e] = x1[e] + bsel(mhi, d2[4 + e][0] + d2[4 + e][1], d2[e][0] + d2[e][1]);
        *reinterpret_cast<v4f*>(&lds[L_ORED + (wi * 2 + (g4 & 1)) * 256 + 128 * hf + 8 * c + 4 * (g4 >> 1)]) = y;
      }
      STAMP(4);
      asm volatile("s_waitcnt lgkmcnt(0)" ::: "memory");
      if (lane == 0) __hip_atomic_fetch_add((LAS u32*)(&lds[L_FLAG + 1]), 1u, __ATOMIC_RELAXED, __HIP_MEMORY_SCOPE_WORKGROUP);
      if (w < 6) {
        const int hh = w - 4;
        const float Mh = fmaxf(fmaxf(lds[L_WMAX + hh], lds[L_WMAX + 2 + hh]), fmaxf(lds[L_WMAX + 4 + hh], lds[L_WMAX + 6 + hh]));
        const float L = (lds[L_WSUM + hh] + lds[L_WSUM + 2 + hh]) + (lds[L_WSUM + 4 + hh] + lds[L_WSUM + 6 + hh]);
        for (u32 sp = 0; lflag[1] < 4u && sp < (SPIN_CAP << 4); ++sp) { }
        asm volatile("" ::: "memory");
        STAMP(5);
        v4f t4[4];
#pragma unroll
        for (int ww = 0; ww < 4; ++ww) t4[ww] = *reinterpret_cast<const v4f*>(&lds[L_ORED + (ww * 2 + hh) * 256 + lane * 4]);
        const v4f o = (t4[0] + t4[1]) + (t4[2] + t4[3]);
        const u32 c0 = cvtpk(o[0], o[1]), c1 = cvtpk(o[2], o[3]);
        stg(rs, WS_PO + (((2 * g + hh) * 32 + s) * 64 + lane) * 16, c0, c1, t0, t1);
        if (lane == 0) stg(rs, WS_ML + ((2 * g + hh) * 32 + s) * 128, __float_as_uint(Mh), __float_as_uint(L), t0, t1);
        if (hh == (oh & 1)) {
          *reinterpret_cast<v4f*>(&lds[L_CMB + 4 * 256 + lane * 4]) = v4f{bflo(c0), bfhi(c0), bflo(c1), bfhi(c1)};
          if (lane == 0) *reinterpret_cast<v2f*>(&lds[L_CML + 8]) = v2f{Mh, L};
        }
        STAMP(6);
      }
      pairup(wor[0], wor[1]); pairup(wor[2], wor[3]);
      asm volatile("" : "+v"(wor[0]), "+v"(wor[1]), "+v"(wor[2]), "+v"(wor[3]));
    }
    TAG();
    bar();
    if (w >= 4) {
      v2f cm[5], co[5], c2[5];
#pragma unroll
      for (int i = 0; i < 5; ++i) {
        cm[i] = *reinterpret_cast<const v2f*>(&lds[L_CML + i * 2]);
        co[i] = *reinterpret_cast<const v2f*>(&lds[L_CMB + i * 256 + orow]);
        c2[i] = *reinterpret_cast<const v2f*>(&lds[L_CMB + i * 256 + orow + 32]);
      }
      const float Mx = fmaxf(fmaxf(fmaxf(cm[0][0], cm[1][0]), fmaxf(cm[2][0], cm[3][0])), cm[4][0]);
      float L = 0.f;
      v2f u = {0.f, 0.f}, u2 = {0.f, 0.f};
#pragma unroll
      for (int i = 0; i < 5; ++i) {
        const float f = __builtin_amdgcn_exp2f(cm[i][0] - Mx);
        L = fmaf(f, cm[i][1], L);
        u += f * co[i]; u2 += f * c2[i];
      }
      const float rl = rcp(L);
      float acc[8];
#pragma unroll
      for (int j = 0; j < 8; ++j) acc[j] = 0.f;
      fmap(acc, wor[0], wor[1], cvtpk(u[0] * rl, u[1] * rl));
      fmap(acc, wor[2], wor[3], cvtpk(u2[0] * rl, u2[1] * rl));
      float r4[4], r2[2];
#pragma unroll
      for (int j = 0; j < 4; ++j) r4[j] = sw32_sum(acc[j], acc[j + 4]);
#pragma unroll
      for (int j = 0; j < 2; ++j) r2[j] = tr_step<X7>(r4[j], r4[j + 2], (lane & 4) != 0);
      float r1 = tr_step<X2>(r2[0], r2[1], (lane & 2) != 0);
      r1 += dpp<X1>(r1);
      if ((lane & 1) == 0) lds[L_S1 + (w - 4) * 32 + (sub & 3) * 8 + (lane >> 5) * 4 + ((lane >> 2) & 1) * 2 + ((lane >> 1) & 1)] = r1;
    }
    bar();
    if (w == 0 && lane < 16) {
      v2f a[4];
#pragma unroll
      for (int ww = 0; ww < 4; ++ww) a[ww] = *reinterpret_cast<const v2f*>(&lds[L_S1 + ww * 32 + lane * 2]);
      const v2f t = (a[0] + a[1]) + (a[2] + a[3]);
      stg(rs, WS_PH + (oh * 224 + cb * 16 + lane) * 16, __float_as_uint(t[0]), __float_as_uint(t[1]), t0, t1);
    }
    STAMP(8);
    STAMP_FLUSH();
    return;
  }

  const int f = g * 4 + (s - NSP);
  const bool ffn = f < 28;
  if (tid == 0) *(volatile LAS u32*)(&lds[L_FC]) = 0u;
  const int r4 = lane >> 4;
  const v4u xv = *reinterpret_cast<const v4u*>(x + (lane < 56 ? lane : 0) * 8);
  u32 xq[7];
  v4u wa[14], wf[14], wdn[8];
  {
    const int row0 = 56 * w + 14 * r4;
#pragma unroll
    for (int i = 0; i < 7; ++i) xq[i] = *reinterpret_cast<const u32*>(x + row0 + 2 * i);
    const u16* p = wq + (size_t)row0 * NQ + 128 * f + (lane & 15) * 8;
#pragma unroll
    for (int i = 0; i < 14; ++i) wa[i] = ldnt(p + (size_t)i * NQ);
    SB();
    bar();
    SB();
    const int fc = ffn ? f : 0;
    const u16* pf = (((lane >> 3) & 1) ? wu : wg) + (size_t)row0 * FF + 64 * fc + (lane & 7) * 8;
    float xf[8];
#pragma unroll
    for (int j = 0; j < 4; ++j) { xf[2 * j] = bflo(xv[j]); xf[2 * j + 1] = bfhi(xv[j]); }
    float sm = 0.f;
#pragma unroll
    for (int j = 0; j < 8; ++j) sm += xf[j];
    sm = wave_sum(lane < 56 ? sm : 0.f);
    const float mean = sm * (1.0f / D);
    float sq = 0.f;
#pragma unroll
    for (int j = 0; j < 8; ++j) { const float d = xf[j] - mean; sq += d * d; }
    sq = wave_sum(lane < 56 ? sq : 0.f);
    const float mb = rbf(mean), den = rbf(sqrtf(rbf(rbf(sq * (1.0f / D)) + 1e-5f)));
    float acc[8];
#pragma unroll
    for (int j = 0; j < 8; ++j) acc[j] = 0.f;
#pragma unroll
    for (int i = 0; i < 7; ++i) {
      const u32 n = cvtpk(rbf(bflo(xq[i]) - mb) / den, rbf(bfhi(xq[i]) - mb) / den);
      pairup(wa[2 * i], wa[2 * i + 1]);
      fmap(acc, wa[2 * i], wa[2 * i + 1], n);
    }
    float a4[4];
#pragma unroll
    for (int j = 0; j < 4; ++j) a4[j] = sw32_sum(acc[j], acc[j + 4]);
    v2f a2;
#pragma unroll
    for (int j = 0; j < 2; ++j) a2[j] = sw16_sum(a4[j], a4[j + 2]);
    *reinterpret_cast<v2f*>(&lds[L_QR + w * 128 + (lane & 15) * 8 + (lane >> 5) * 4 + ((lane >> 4) & 1) * 2]) = a2;
    bar();
    TAG();
    if (w == 0 && lane < 32) {
      v4f qv = *reinterpret_cast<const v4f*>(&lds[L_QR + lane * 4]);
#pragma unroll
      for (int ww = 1; ww < 8; ++ww) qv += *reinterpret_cast<const v4f*>(&lds[L_QR + ww * 128 + lane * 4]);
      stg(rs, WS_Q + (f * 32 + lane) * 16, cvtpk(qv[0], qv[1]), cvtpk(qv[2], qv[3]), t0, t1);
    }
    STAMP(1);
    if (!ffn) { STAMP_FLUSH(); return; }
    SB();
#pragma unroll
    for (int i = 0; i < 14; ++i) wf[i] = ldnt(pf + (size_t)i * FF);
    SB();
  }
  const int l56 = lane < 56 ? lane : 0;
  {
    const u16* pd = wd + (size_t)(64 * f + 8 * w) * D + 8 * l56;
#pragma unroll
    for (int i = 0; i < 8; ++i) wdn[i] = ldnt(pd + i * D);
    SB();
  }
#pragma unroll
  for (int i = 0; i < 7; ++i) { pairup(wf[2 * i], wf[2 * i + 1]); asm volatile("" : "+v"(wf[2 * i]), "+v"(wf[2 * i + 1])); }
#pragma unroll
  for (int i = 0; i < 4; ++i) { pairup(wdn[2 * i], wdn[2 * i + 1]); asm volatile("" : "+v"(wdn[2 * i]), "+v"(wdn[2 * i + 1])); }
  const int cu = (lane & 7) * 8 + (lane >> 5) * 4 + ((lane >> 4) & 1) * 2, mt = (lane >> 3) & 1;
  {
    float acc[8];
#pragma unroll
    for (int j = 0; j < 8; ++j) acc[j] = 0.f;
#pragma unroll
    for (int i = 0; i < 7; ++i) fmap(acc, wf[2 * i], wf[2 * i + 1], 0x3f803f80u);
    float a4[4];
#pragma unroll
    for (int j = 0; j < 4; ++j) a4[j] = sw32_sum(acc[j], acc[j + 4]);
#pragma unroll
    for (int j = 0; j < 2; ++j) lds[L_QR + w * 128 + (cu + j) * 2 + mt] = sw16_sum(a4[j], a4[j + 2]);
  }
  bar();
  const int u = 8 * w + (lane & 7);
  v2f SGU = {0.f, 0.f};
#pragma unroll
  for (int ww = 0; ww < 8; ++ww) SGU += *reinterpret_cast<const v2f*>(&lds[L_QR + ww * 128 + u * 2]);
  asm volatile("" : "+v"(SGU));
  {
    const u32 pt0 = __builtin_amdgcn_raw_buffer_load_b32(rs, WS_PD + (f * 224 + (tid < 224 ? tid : 0)) * 16, 0, AUX_SC1);
    const u32 pt1 = *reinterpret_cast<const volatile u32*>(out + 16 * f);
    asm volatile("" :: "v"(pt0), "v"(pt1));
  }
  TAG();
  STAMP(2);
  {
    const int hsub = lane >> 5;
    const bool act = (lane & 31) < 28;
    const int pp = act ? (lane & 31) : 0;
    int off[8];
    v4u gr[8];
    const u32 xp = *reinterpret_cast<const u32*>(x + 56 * w + 2 * pp);
    LAS u32* const xw = (LAS u32*)(&lds[L_XN + w * 32 + (pp / 7) * 8 + pp % 7]);
    const LAS v4u* const xr = (const LAS v4u*)(&lds[L_XN + w * 32 + r4 * 8]);
    const int cp = 28 * w + pp - 8 * f;
    const bool keep = lane < 28 && cp >= 0 && cp < 8;
    float acc[10];
#pragma unroll
    for (int j = 0; j < 10; ++j) { acc[j] = 0.f; asm volatile("" : "+v"(acc[j])); }
#pragma unroll
    for (int i = 0; i < 8; ++i) { off[i] = act ? WS_PH + ((2 * i + hsub) * 224 + 28 * w + pp) * 16 : OOB; gr[i] = ldg(rs, off[i]); }
#pragma unroll
    for (int i = 0; i < 8; ++i) mark(gr[i], !act, tag);
    poll<8, 64>(srd, off, gr, tag);
    STAMP(9);
    v2f hp[8];
#pragma unroll
    for (int i = 0; i < 8; ++i) hp[i] = v2f{__uint_as_float(gr[i][0]), __uint_as_float(gr[i][1])};
    const v2f hs = ((hp[0] + hp[1]) + (hp[2] + hp[3])) + ((hp[4] + hp[5]) + (hp[6] + hp[7]));
    const u32 a01 = cvtpk(sw32_sum(hs[0], hs[0]), sw32_sum(hs[1], hs[1]));
    const u32 h01 = cvtpk(bflo(xp) + bflo(a01), bfhi(xp) + bfhi(a01));
    if (lane < 28) *xw = h01;
    asm volatile("s_waitcnt lgkmcnt(0)" ::: "memory");
    const v4u na = xr[0], nb = xr[1];
    if (keep) reinterpret_cast<u32*>(&lds[L_HS])[cp] = h01;
#pragma unroll
    for (int i = 0; i < 7; ++i) {
      const u32 n = i < 4 ? na[i] : nb[i - 4];
#pragma unroll
      for (int j = 0; j < 4; ++j) { acc[j] = dot2(wf[2 * i][j], n, acc[j]); acc[4 + j] = dot2(wf[2 * i + 1][j], n, acc[4 + j]); }
      acc[8] = dot2(n, 0x3f803f80u, acc[8]);
      acc[9] = dot2(n, n, acc[9]);
    }
    float a4[4];
#pragma unroll
    for (int j = 0; j < 4; ++j) a4[j] = sw32_sum(acc[j], acc[j + 4]);
    float st = sw32_sum(acc[8], acc[9]);
    st = sw16_sum(st, st);
#pragma unroll
    for (int j = 0; j < 2; ++j) lds[L_GU + w * 128 + (cu + j) * 2 + mt] = sw16_sum(a4[j], a4[j + 2]);
    if ((lane & 31) == 0) lds[L_ST + w * 2 + hsub] = st;
  }
  float dacc[8];
#pragma unroll
  for (int j = 0; j < 8; ++j) { dacc[j] = 0.f; asm volatile("" : "+v"(dacc[j])); }
  bar();
  {
    v2f gu[8];
    v4f sv[4];
#pragma unroll
    for (int i = 0; i < 4; ++i) sv[i] = *reinterpret_cast<const v4f*>(&lds[L_ST + i * 4]);
#pragma unroll
    for (int ww = 0; ww < 8; ++ww) gu[ww] = *reinterpret_cast<const v2f*>(&lds[L_GU + ww * 128 + u * 2]);
    const v4f s4 = (sv[0] + sv[1]) + (sv[2] + sv[3]);
    const float mean = (s4[0] + s4[2]) * (1.0f / D);
    const float var = fmaxf(fmaf(-mean, mean, (s4[1] + s4[3]) * (1.0f / D)), 0.f);
    const float mb = rbf(mean), rden = rcp(rbf(__builtin_amdgcn_sqrtf(rbf(rbf(var) + 1e-5f))));
    const v2f GUs = ((gu[0] + gu[1]) + (gu[2] + gu[3])) + ((gu[4] + gu[5]) + (gu[6] + gu[7]));
    const v2f GUn = (GUs - mb * SGU) * rden;
    const u32 gub = cvtpk(GUn[0], GUn[1]);
    const float G = bflo(gub), U = bfhi(gub);
    const float z2 = G * fmaf(G * G, 0.044715f * 0.7978845608028654f * 2.0f * LOG2E, 0.7978845608028654f * 2.0f * LOG2E);
    const float sg = rcp(__builtin_amdgcn_exp2f(z2) + 1.0f);
    const float av = rbf(fmaf(-G, sg, G)) * U;
    const u32 pk = cvtpk(av, dpp<X1>(av));
#pragma unroll
    for (int i = 0; i < 4; ++i) fmap(dacc, wdn[2 * i], wdn[2 * i + 1], __builtin_amdgcn_readlane(pk, 2 * i));
    if (lane < 56) {
      *reinterpret_cast<v4f*>(&lds[L_DN + w * 448 + lane * 8]) = v4f{dacc[0], dacc[1], dacc[2], dacc[3]};
      *reinterpret_cast<v4f*>(&lds[L_DN + w * 448 + lane * 8 + 4]) = v4f{dacc[4], dacc[5], dacc[6], dacc[7]};
    }
  }
  bar();
  if (tid < 224) {
    v2f a[8];
#pragma unroll
    for (int ww = 0; ww < 8; ++ww) a[ww] = *reinterpret_cast<const v2f*>(&lds[L_DN + ww * 448 + tid * 2]);
    const v2f t = ((a[0] + a[1]) + (a[2] + a[3])) + ((a[4] + a[5]) + (a[6] + a[7]));
    stg(rs, WS_PD + (f * 224 + tid) * 16, __float_as_uint(t[0]), __float_as_uint(t[1]), t0, t1);
  }
  STAMP(10);
  if (w < 7) {
    int off[1];
    v4u gr[1];
    off[0] = lane < 32 ? WS_PD + ((4 * w + (lane >> 3)) * 224 + 8 * f + (lane & 7)) * 16 : OOB;
    gr[0] = ldg(rs, off[0]);
    mark(gr[0], lane >= 32, tag);
    poll<1, 64>(srd, off, gr, tag);
    STAMP(11);
    float a0 = __uint_as_float(gr[0][0]), a1 = __uint_as_float(gr[0][1]);
    float r = sw16_sum(a0, a1);
    r += dpp<X8>(r);
    if (lane < 32 && (lane & 8) == 0) lds[L_FN + w * 16 + (lane & 7) * 2 + (lane >> 4)] = r;
    asm volatile("s_waitcnt lgkmcnt(0)" ::: "memory");
    u32 old = 0;
    if (lane == 0) old = __hip_atomic_fetch_add((LAS u32*)(&lds[L_FC]), 1u, __ATOMIC_RELAXED, __HIP_MEMORY_SCOPE_WORKGROUP);
    if (__builtin_amdgcn_readfirstlane(old) == 6u) {
      asm volatile("" ::: "memory");
      if (lane < 8) {
        v2f a[7];
#pragma unroll
        for (int ww = 0; ww < 7; ++ww) a[ww] = *reinterpret_cast<const v2f*>(&lds[L_FN + ww * 16 + lane * 2]);
        const v2f t = ((a[0] + a[1]) + (a[2] + a[3])) + ((a[4] + a[5]) + a[6]);
        const u32 hv = reinterpret_cast<const u32*>(&lds[L_HS])[lane];
        const u32 tb = cvtpk(t[0], t[1]);
        *reinterpret_cast<u32*>(out + 16 * f + lane * 2) = cvtpk(bflo(hv) + bflo(tb), bfhi(hv) + bfhi(tb));
        if (f == 0 && lane == 0) __builtin_amdgcn_raw_buffer_store_b64(v2u{t0, t1}, rs, WS_EPOCH, 0, AUX_SC1);
      }
    }
  }
  STAMP(12);
  STAMP_FLUSH();
}

}

extern "C" void kernel_launch(void* const* d_in, const int* in_sizes, int n_in, void* d_out, int out_size, void* d_ws,
                              size_t ws_size, hipStream_t stream) {
  fused_decode_block<<<256, 512, 0, stream>>>((const u16*)d_in[0], (const u16*)d_in[1], (const u16*)d_in[2], (const u16*)d_in[3],
                                              (const u16*)d_in[4], (const u16*)d_in[6], (const u16*)d_in[7], (const u16*)d_in[8],
                                              (u16*)d_out, (unsigned char*)d_ws);
}
```
